```python
import torch

def _softmax(x, axis=-1):
    return torch.softmax(x, dim=axis)
def _gelu(x):
    return torch.nn.functional.gelu(x, approximate="tanh")
def _silu(x):
    return torch.nn.functional.silu(x)
def _sigmoid(x):
    return torch.sigmoid(x)
def _layernorm(x, axis=-1, eps=1e-5):
    m = x.mean(dim=axis, keepdim=True)
    v = x.var(dim=axis, keepdim=True, unbiased=False)
    return (x - m) / torch.sqrt(v + eps)
def _scatter_add(tgt, idx, src, axis=0):
    return tgt.clone().index_add_(axis, idx, src)
def _segment_sum(src, seg, n):
    out = torch.zeros((n,) + src.shape[1:], dtype=src.dtype)
    return out.index_add_(0, seg, src)
def _pad(x, pw):
    flat = tuple(v for pair in reversed(pw) for v in pair)
    return torch.nn.functional.pad(x, flat)

kernel_name = 'interleave_layers__sliding_local_dilated__mixer_channel_mlp__prompt'

def setup_inputs():

    _w = (lambda a: (lambda t: t.to(torch.bfloat16) if t.is_floating_point() else t)(torch.as_tensor(a)))
    def _A_setup():
        B, S, D = 1, 2048, 576
        return {
            "x": _w((lambda *s: torch.randn(*s, dtype=torch.bfloat16) * 0.05)(B, S, D)),
            "wqkv": _w((lambda *s: torch.randn(*s, dtype=torch.bfloat16) * 0.05)(D, 3 * D)),
            "wo": _w((lambda *s: torch.randn(*s, dtype=torch.bfloat16) * 0.05)(D, D)),
        }
    _A_in = _A_setup()

    def _B_setup():
        B, S, D, F = 1, 2048, 576, 4608
        return {
            "x": _w((lambda *s: torch.randn(*s, dtype=torch.bfloat16) * 0.05)(B, S, D)),
            "w1": _w((lambda *s: torch.randn(*s, dtype=torch.bfloat16) * 3.2)(D, F) * (D ** -0.5)),
            "b1": _w((lambda *s: torch.randn(*s, dtype=torch.bfloat16) * 0.8)(F)),
            "w2": _w((lambda *s: torch.randn(*s, dtype=torch.bfloat16) * 0.2)(F, D) * (F ** -0.5)),
            "b2": _w((lambda *s: torch.randn(*s, dtype=torch.bfloat16) * 0.2)(D)),
        }
    _B_in = _B_setup()

    return {
        "A_x": _A_in["x"],
        "A_wqkv": _A_in["wqkv"],
        "A_wo": _A_in["wo"],
        "B_x": _B_in["x"],
        "B_w1": _B_in["w1"],
        "B_b1": _B_in["b1"],
        "B_w2": _B_in["w2"],
        "B_b2": _B_in["b2"],
    }

def reference(A_x, A_wqkv, A_wo, B_x, B_w1, B_b1, B_w2, B_b2):
    def _A_ref(x, wqkv, wo):
        B, S, D = x.shape
        H = 32; Dh = D // H
        W, dil = 8, 2
        qkv = (lambda x, *d: x.permute(*d))((x @ wqkv).reshape(B, S, 3, H, Dh), 2, 0, 3, 1, 4)
        q, k, v = qkv[0], qkv[1], qkv[2]
        scale = Dh ** -0.5
        pos = torch.arange(S)
        off = torch.arange(W) * dil
        idx = pos[:, None] - off[None, :]
        mask = idx >= 0
        idx_c = torch.where(mask, idx, 0).reshape(-1)
        kg = k[:, :, idx_c, :].reshape(B, H, S, W, Dh)
        vg = v[:, :, idx_c, :].reshape(B, H, S, W, Dh)
        logits = torch.einsum("bhsd,bhswd->bhsw", q, kg) * scale
        logits = torch.where(mask[None, None, :, :], logits, -1e9)
        attn = _softmax(logits, axis=-1)
        o = torch.einsum("bhsw,bhswd->bhsd", attn, vg)
        return (lambda x, *d: x.permute(*d))(o, 0, 2, 1, 3).reshape(B, S, D) @ wo

    def _B_ref(x, w1, b1, w2, b2):
        y = _layernorm(x, axis=-1)
        h = _gelu(torch.einsum("bsd,df->bsf", y, w1) + b1)
        return torch.einsum("bsf,fd->bsd", h, w2) + b2

    x = A_x
    x = x + _A_ref(_layernorm(x, axis=-1), A_wqkv, A_wo)
    x = x + _B_ref(_layernorm(x, axis=-1), B_w1, B_b1, B_w2, B_b2)
    x = x + _A_ref(_layernorm(x, axis=-1), A_wqkv, A_wo)
    x = x + _B_ref(_layernorm(x, axis=-1), B_w1, B_b1, B_w2, B_b2)
    return x
```

```cpp
#include <hip/hip_runtime.h>
#include <hip/hip_bf16.h>
typedef unsigned short u16;
constexpr int S=2048, D=576, F=4608, H=32, DH=18;
__device__ __forceinline__ float bf2f(u16 b){ return __uint_as_float(((unsigned)b)<<16);}
__device__ __forceinline__ u16 f2bf(float f){ unsigned u=__float_as_uint(f); u += 0x7fffu + ((u>>16)&1u); return (u16)(u>>16);}
__device__ __forceinline__ float rb(float f){ return bf2f(f2bf(f)); }

__global__ void k_ln(const u16* x, u16* y, int times){
  int row = blockIdx.x; int lane = threadIdx.x;
  float v[9];
  for(int i=0;i<9;i++) v[i]=bf2f(x[row*D+lane+64*i]);
  for(int t=0;t<times;t++){
    float s=0; for(int i=0;i<9;i++) s+=v[i];
    for(int o=32;o;o>>=1) s+=__shfl_xor(s,o);
    float m=s/D; float q=0; for(int i=0;i<9;i++){float d=v[i]-m; q+=d*d;}
    for(int o=32;o;o>>=1) q+=__shfl_xor(q,o);
    float r=rsqrtf(q/D+1e-5f);
    for(int i=0;i<9;i++) v[i]=rb((v[i]-m)*r);
  }
  for(int i=0;i<9;i++) y[row*D+lane+64*i]=f2bf(v[i]);
}
template<int MODE>
__global__ void k_gemm(const u16* A,const u16* B,const u16* bias,const u16* res,u16* C,int N,int K){
  __shared__ float As[16][65]; __shared__ float Bs[16][65];
  int tx=threadIdx.x&15, ty=threadIdx.x>>4;
  int m0=blockIdx.y*64, n0=blockIdx.x*64;
  float acc[4][4]={};
  for(int k0=0;k0<K;k0+=16){
    for(int i=threadIdx.x;i<1024;i+=256){ int r=i>>4,c=i&15; As[c][r]=bf2f(A[(m0+r)*K+k0+c]); }
    for(int i=threadIdx.x;i<1024;i+=256){ int r=i>>6,c=i&63; Bs[r][c]=bf2f(B[(k0+r)*N+n0+c]); }
    __syncthreads();
    for(int k=0;k<16;k++){ for(int i=0;i<4;i++)for(int j=0;j<4;j++) acc[i][j]+=As[k][ty*4+i]*Bs[k][tx*4+j]; }
    __syncthreads();
  }
  for(int i=0;i<4;i++)for(int j=0;j<4;j++){
    int r=m0+ty*4+i,c=n0+tx*4+j; float v=acc[i][j];
    if(MODE==1){ v=v+bf2f(bias[c]); float u=0.7978845608f*(v+0.044715f*v*v*v); v=0.5f*v*(1.f+tanhf(u)); }
    if(MODE==2){ v=v+bf2f(bias[c]); v+=bf2f(res[r*N+c]); }
    if(MODE==3){ v+=bf2f(res[r*N+c]); }
    C[r*N+c]=f2bf(v);
  }
}
__global__ void k_attn(const u16* qkv,u16* o){
  int t=blockIdx.x*blockDim.x+threadIdx.x; int s=t/H,h=t%H;
  float q[DH]; for(int d=0;d<DH;d++) q[d]=bf2f(qkv[s*3*D+h*DH+d]);
  float l[8]; float mx=-1e30f;
  for(int w=0;w<8;w++){ int i=s-2*w; if(i>=0){ float a=0; for(int d=0;d<DH;d++) a+=q[d]*bf2f(qkv[i*3*D+D+h*DH+d]); l[w]=a*0.23570226f;} else l[w]=-1e9f; mx=fmaxf(mx,l[w]); }
  float sum=0; for(int w=0;w<8;w++){ l[w]=__expf(l[w]-mx); sum+=l[w]; }
  float ov[DH]={};
  for(int w=0;w<8;w++){ int i=s-2*w; if(i<0) i=0; float p=l[w]/sum; for(int d=0;d<DH;d++) ov[d]+=p*bf2f(qkv[i*3*D+2*D+h*DH+d]); }
  for(int d=0;d<DH;d++) o[s*D+h*DH+d]=f2bf(ov[d]);
}
extern "C" void kernel_launch(void* const* d_in, const int* in_sizes, int n_in, void* d_out, int out_size, void* d_ws, size_t ws_size, hipStream_t stream){
  const u16* x=(const u16*)d_in[0]; const u16* wqkv=(const u16*)d_in[1]; const u16* wo=(const u16*)d_in[2];
  const u16* w1=(const u16*)d_in[4]; const u16* b1=(const u16*)d_in[5]; const u16* w2=(const u16*)d_in[6]; const u16* b2=(const u16*)d_in[7];
  u16* ws=(u16*)d_ws; u16* y=ws; u16* qkv=y+S*D; u16* o=qkv+S*3*D; u16* x1=o+S*D; u16* h=x1+S*D; u16* x2=h+S*F;
  const u16* xin=x;
  for(int l=0;l<2;l++){
    u16* xout = l==0? x2 : (u16*)d_out;
    k_ln<<<S,64,0,stream>>>(xin,y,1);
    k_gemm<0><<<dim3(3*D/64,S/64),256,0,stream>>>(y,wqkv,nullptr,nullptr,qkv,3*D,D);
    k_attn<<<S*H/256,256,0,stream>>>(qkv,o);
    k_gemm<3><<<dim3(D/64,S/64),256,0,stream>>>(o,wo,nullptr,xin,x1,D,D);
    k_ln<<<S,64,0,stream>>>(x1,y,2);
    k_gemm<1><<<dim3(F/64,S/64),256,0,stream>>>(y,w1,b1,nullptr,h,F,D);
    k_gemm<2><<<dim3(D/64,S/64),256,0,stream>>>(h,w2,b2,x1,xout,D,F);
    xin=xout;
  }
}
```

```cpp
#include <hip/hip_runtime.h>
#include <hip/hip_bf16.h>

typedef unsigned short u16;
typedef unsigned u32;
typedef unsigned long long u64;
typedef u32 v4u __attribute__((ext_vector_type(4)));
typedef u32 v2u __attribute__((ext_vector_type(2)));
typedef float f32x4 __attribute__((ext_vector_type(4)));
typedef float f32x2 __attribute__((ext_vector_type(2)));
typedef short s16x4 __attribute__((ext_vector_type(4)));
typedef __bf16 bf16x2 __attribute__((ext_vector_type(2)));
typedef __bf16 bf16x8 __attribute__((ext_vector_type(8)));
#define GAS __attribute__((address_space(1)))
#define LAS __attribute__((address_space(3)))
#define DI __device__ __forceinline__
#define RLX __ATOMIC_RELAXED, __HIP_MEMORY_SCOPE_AGENT

#define XG 2
#define AUX1 6
#define AUX2 9
#define AUX3 13
#define BF1 12
#define PSL 12
#define BSL 32
#define ABL 0
#define STAMPS 0

#define CODE_BYTES 40960
constexpr int EP_OFF = 0;
constexpr int MB_OFF = 4096, MB_BYTES = 3 * 256 * 1024;
constexpr int MB_O = 0, MB_X1 = 16, MB_A = 32, MB_B = 48, MB_X2 = 64, MB_WQ = 0, MB_W1 = 32, MB_W2 = 64;
constexpr int CSQ_OFF = MB_OFF + MB_BYTES;
constexpr int CS1_OFF = CSQ_OFF + 8 * 18432;
constexpr int WQ_OFF = CS1_OFF + 8 * 20736;
constexpr int WO_OFF = WQ_OFF + 64 * 36864;
constexpr int WM_OFF = WO_OFF + 80 * 18432;
constexpr int OB_OFF = WM_OFF + 64 * 165888;
constexpr int X1_OFF = OB_OFF + 64 * 73728;
constexpr int X2_OFF = X1_OFF + 64 * 73728;
constexpr int PB_OFF = X2_OFF + 32 * 73728;
constexpr int ST_OFF = PB_OFF + 2 * 32 * 64 * 9216;
constexpr int WS_USED = ST_OFF + 2 * 256 * 32 * 8 + 256 * 2048;
constexpr int WS_DUMP = WS_USED;
constexpr int L_E = 92160, L_OI = 92160, L_H = 73728;
constexpr int L_TQ = 92160, L_TW = 129024;
constexpr int L_PX = 129024;
constexpr int L_SS = 147456;
constexpr int L_RK = L_SS + 1024;
constexpr int L_CNT = L_RK + 2048;
constexpr int L_TA = L_CNT + 32;
constexpr int L_TB2 = L_TA + 4608;
constexpr int L_TCQ = L_TB2 + 2304;
constexpr int L_DBG = L_TCQ + 1024;
constexpr int L_TOTAL = L_DBG + (STAMPS ? 2048 : 0);

DI float bf2f(u16 b) { return __builtin_bit_cast(float, (u32)((u32)b) << 16); }
DI float lo2f(u32 x) { return __builtin_bit_cast(float, (u32)x << 16); }
DI float hi2f(u32 x) { return __builtin_bit_cast(float, (u32)x & 0xffff0000u); }
DI u32 pk(float a, float b) { f32x2 f = {a, b}; return __builtin_bit_cast(u32, __builtin_convertvector(f, bf16x2)); }
DI f32x4 mfma(v4u a, v4u b, f32x4 c) {
  return __builtin_amdgcn_mfma_f32_16x16x32_bf16(__builtin_bit_cast(bf16x8, a), __builtin_bit_cast(bf16x8, b), c, 0, 0, 0);
}
DI void mm(f32x4& c, v4u a, v4u b) { asm volatile("v_mfma_f32_16x16x32_bf16 %0, %1, %2, %0" : "+v"(c) : "v"(a), "v"(b)); }
DI void mm0(f32x4& c, v4u a, v4u b) { asm volatile("v_mfma_f32_16x16x32_bf16 %0, %1, %2, 0" : "=&v"(c) : "v"(a), "v"(b)); }
DI void mmk16(f32x4& c, v2u a, v2u b) { asm volatile("v_mfma_f32_16x16x16_bf16 %0, %1, %2, %0" : "+v"(c) : "v"(a), "v"(b)); }
DI void mm_end() { __builtin_amdgcn_sched_barrier(0); asm volatile("s_nop 7\n\ts_nop 1" ::: "memory"); __builtin_amdgcn_sched_barrier(0); }
DI float xsum(float v) {
  u32 x = __builtin_bit_cast(u32, v);
  auto a = __builtin_amdgcn_permlane16_swap(x, x, false, false);
  v = __builtin_bit_cast(float, (u32)a[0]) + __builtin_bit_cast(float, (u32)a[1]);
  x = __builtin_bit_cast(u32, v);
  auto b = __builtin_amdgcn_permlane32_swap(x, x, false, false);
  return __builtin_bit_cast(float, (u32)b[0]) + __builtin_bit_cast(float, (u32)b[1]);
}
DI float xmax(float v) {
  u32 x = __builtin_bit_cast(u32, v);
  auto a = __builtin_amdgcn_permlane16_swap(x, x, false, false);
  v = __builtin_fmaxf(__builtin_bit_cast(float, (u32)a[0]), __builtin_bit_cast(float, (u32)a[1]));
  x = __builtin_bit_cast(u32, v);
  auto b = __builtin_amdgcn_permlane32_swap(x, x, false, false);
  return __builtin_fmaxf(__builtin_bit_cast(float, (u32)b[0]), __builtin_bit_cast(float, (u32)b[1]));
}
#define SB() __builtin_amdgcn_sched_barrier(0)
DI int smul(int a, int k) { int r; asm("s_mul_i32 %0, %1, %2" : "=s"(r) : "s"(a), "s"(k)); return r; }
DI void bar() {
  asm volatile("s_waitcnt lgkmcnt(0)" ::: "memory");
  __builtin_amdgcn_s_barrier();
  asm volatile("" ::: "memory");
}
typedef __amdgpu_buffer_rsrc_t rsrc_t;
DI v4u ldw(rsrc_t rs, int v, int s) { return __builtin_amdgcn_raw_buffer_load_b128(rs, v, s, 16); }
DI v4u ldp(rsrc_t rs, int v, int s) { return __builtin_amdgcn_raw_buffer_load_b128(rs, v, s, 0); }
constexpr int OOB = 0x70000000;
DI v4u ldx(rsrc_t rs, int v, int s) { return __builtin_amdgcn_raw_buffer_load_b128(rs, v, s, 16); }
DI v2u ldx2(rsrc_t rs, int v, int s) { return __builtin_amdgcn_raw_buffer_load_b64(rs, v, s, 16); }
template <int AUX> DI void st128(v4u d, rsrc_t rs, int v, int s) { __builtin_amdgcn_raw_buffer_store_b128(d, rs, v, s, AUX); asm volatile("s_nop 1" :: "v"(d)); }
DI void stx(v4u d, rsrc_t rs, int v, int s) { st128<16>(d, rs, v, s); }
DI void stx2(v2u d, rsrc_t rs, int v, int s) { __builtin_amdgcn_raw_buffer_store_b64(d, rs, v, s, 16); }
DI v4u lds128(u32 a) { return *(const LAS v4u*)(size_t)a; }
DI v2u lds64(u32 a) { return *(const LAS v2u*)(size_t)a; }
DI f32x4 ldsf4(u32 a) { return *(const LAS f32x4*)(size_t)a; }
DI f32x2 ldsf2(u32 a) { return *(const LAS f32x2*)(size_t)a; }
DI void sts128(u32 a, v4u v) { *(LAS v4u*)(size_t)a = v; }
DI void sts64(u32 a, v2u v) { *(LAS v2u*)(size_t)a = v; }
DI v2u ldstr(u32 a) { return __builtin_bit_cast(v2u, __builtin_amdgcn_ds_read_tr16_b64_v4i16((LAS s16x4*)(size_t)a)); }

__host__ __device__ __forceinline__ int colq(int c, int w, int t, int m) {
  if (w < 4 || t == 0) {
    const int h = w & 3, sec = w < 4 ? (t == 0 ? 1 : 0) : 2;
    return sec * 576 + 72 * c + 36 * (h >> 1) + 20 * (h & 1) + m;
  }
  const int qd = m >> 2;
  if (w < 6) return (qd & 1) * 576 + 72 * c + 36 * (qd >> 1) + 16 + (m & 3);
  return 2 * 576 + 72 * c + 36 * (qd & 1) + 16 + (m & 3);
}
__host__ __device__ __forceinline__ int krot(int c) { return 4 * (c & 3) + (c & 2); }
__host__ __device__ __forceinline__ int colp(int u, int e, int m) { return 32 * u + 8 * (m >> 2) + 4 * e + (m & 3); }
__host__ __device__ __forceinline__ int rowo(int ks, int g, int j) {
  if (ks < 16) return 72 * (ks >> 1) + 36 * (ks & 1) + 20 * (j >> 2) + 4 * g + (j & 3);
  return 72 * (4 * (ks - 16) + g) + 36 * (j >> 2) + 16 + (j & 3);
}
#define REFILL(f_, NU) do { const int q_ = (f_) % 18, u_ = (f_) / 18; \
    if (u_ < (NU) - 1) ring[q_] = ldw(rs, v16 + (q_ & 3) * 1024, s0 + u_ * 18432 + (q_ >> 2) * 4096); \
    else if (LAST) ring[q_] = ldw(rs, vlast + (q_ & 3) * 1024, slast + (q_ >> 2) * 4096); } while (0)
template <int NT, int MIDBAR, bool LAST>
DI void group(f32x4 (&acc)[NT][4], v4u (&ring)[18], u32 xa, rsrc_t rs, int v16, int s0, int vlast, int slast, bool mid) {
  v4u xf[2][4];
  u32 xb = xa + 9 * 4096;
  asm volatile("" : "+v"(xa), "+v"(xb));
#pragma unroll
  for (int t = 0; t < 4; ++t) xf[0][t] = lds128(xa + t * 1024);
  SB();
#pragma unroll
  for (int ks = 0; ks < 18; ++ks) {
    const int kn = ks + 1;
#pragma unroll
    for (int n = 0; n < NT; ++n) {
      const int f = ks * NT + n;
#pragma unroll
      for (int t = 0; t < 4; ++t) {
        if (ks == 0) mm0(acc[n][t], ring[f % 18], xf[0][t]); else mm(acc[n][t], ring[f % 18], xf[ks & 1][t]);
        if (t == 0 && f > 0) REFILL(f - 1, NT);
        const int n0 = kn == MIDBAR ? NT - 2 : 0;
        const int q = n == n0 ? t - 1 : (n == n0 + 1 && t == 1 ? 3 : -1);
        if (q == 0 && kn == MIDBAR) { if (mid) bar(); SB(); }
        if (q >= 0 && kn < 18) xf[kn & 1][q] = kn < 9 ? lds128(xa + kn * 4096 + q * 1024) : lds128(xb + (kn - 9) * 4096 + q * 1024);
        SB();
      }
    }
  }
  mm_end();
  REFILL(18 * NT - 1, NT);
  SB();
}
DI void groupq(f32x4 (&a0)[5], f32x4 (&a1)[4], v4u (&ring)[18], u32 x0, u32 x1, u32 x4, rsrc_t rs, int v16, int s0, int slast, bool mid) {
  constexpr bool LAST = true;
  const int vlast = v16;
  v4u xf[2][5];
  u32 y0 = x0 + 46080, y1 = x1 + 46080, y4 = x4 + 46080;
  asm volatile("" : "+v"(y0), "+v"(y1), "+v"(y4));
  xf[0][0] = lds128(x0); xf[0][1] = lds128(x1); xf[0][2] = lds128(x1 + 1024); xf[0][3] = lds128(x1 + 2048); xf[0][4] = lds128(x4);
  SB();
#pragma unroll
  for (int ks = 0; ks < 18; ++ks) {
    const int sl = (2 * ks) % 18, kn = ks + 1, o = (kn % 9) * 5120;
#pragma unroll
    for (int t = 0; t < 5; ++t) {
      if (ks == 0) mm0(a0[t], ring[sl], xf[0][t]); else mm(a0[t], ring[sl], xf[ks & 1][t]);
      if (t == 0 && ks > 0) REFILL(2 * ks - 1, 2);
      if (t == 1 && kn == 6) { if (mid) bar(); SB(); }
      if (t > 0 && kn < 18) xf[kn & 1][t - 1] = lds128(t == 1 ? (kn < 9 ? x0 : y0) + o : (kn < 9 ? x1 : y1) + o + (t - 2) * 1024);
      SB();
    }
#pragma unroll
    for (int t = 0; t < 4; ++t) {
      if (ks == 0) mm0(a1[t], ring[sl + 1], xf[0][t]); else mm(a1[t], ring[sl + 1], xf[ks & 1][t]);
      if (t == 0) REFILL(2 * ks, 2);
      if (t == 1 && kn < 18) xf[kn & 1][4] = lds128((kn < 9 ? x4 : y4) + o);
      SB();
    }
  }
  mm_end();
  REFILL(35, 2);
  SB();
}
DI void groupo(f32x4 (&a)[3], const v4u (&ring)[18], const v4u (&ex)[6], u32 x0, u32 xe) {
  constexpr int PD = 3, NB = PD + 1;
  v4u xf[NB][2], ef[6];
#pragma unroll
  for (int k = 0; k < PD; ++k) { xf[k][0] = lds128(x0 + k * 2048); xf[k][1] = lds128(x0 + k * 2048 + 1024); }
  SB();
#pragma unroll
  for (int ks = 0; ks < 18; ++ks) {
    const int kn = ks + PD;
    if (ks == 0) mm0(a[0], ring[ks], xf[0][0]); else mm(a[0], ring[ks], xf[ks % NB][0]);
    if (kn < 18) xf[kn % NB][0] = lds128(x0 + kn * 2048); else ef[2 * (kn - 18)] = lds128(xe + 2 * (kn - 18) * 2048);
    SB();
    if (ks == 0) mm0(a[1], ring[ks], xf[0][1]); else mm(a[1], ring[ks], xf[ks % NB][1]);
    if (kn < 18) xf[kn % NB][1] = lds128(x0 + kn * 2048 + 1024); else ef[2 * (kn - 18) + 1] = lds128(xe + (2 * (kn - 18) + 1) * 2048);
    SB();
  }
#pragma unroll
  for (int k = 0; k < 6; ++k) { if (k == 0) mm0(a[2], ex[0], ef[0]); else mm(a[2], ex[k], ef[k]); SB(); }
  mm_end();
}
template <int LO, int HI> DI void fill(v4u (&ring)[18], rsrc_t rs, int v16, int s) {
  SB();
#pragma unroll
  for (int i = LO; i < HI; ++i) ring[i] = ldw(rs, v16 + (i & 3) * 1024, s + (i >> 2) * 4096);
  SB();
}
DI void stat_acc(v4u v, f32x4& s, f32x4& q) { s = mfma(v4u{0x3f803f80u, 0x3f803f80u, 0x3f803f80u, 0x3f803f80u}, v, s); q = mfma(v, v, q); }
DI float diag(f32x4 q, int g, int tl) {
  const float a = (tl & 1) ? q[1] : q[0], b = (tl & 1) ? q[3] : q[2];
  return xsum(g == (tl >> 2) ? ((tl & 2) ? b : a) : 0.f);
}
DI void lds_arrive(u32 a, int lane) {
  asm volatile("s_waitcnt lgkmcnt(0)" ::: "memory");
  if (lane == 0) __hip_atomic_fetch_add((LAS u32*)(size_t)a, 1u, __ATOMIC_RELAXED, __HIP_MEMORY_SCOPE_WORKGROUP);
}
DI void lds_wait(u32 a, u32 n) {
  while ((u32)__builtin_amdgcn_readfirstlane(*(volatile LAS u32*)(size_t)a) < n) {}
  asm volatile("" ::: "memory");
}
DI void split3(float x, u32& h, u32& m, u32& l) {
  h = pk(x, 0.f); x -= lo2f(h);
  m = pk(x, 0.f); x -= lo2f(m);
  l = pk(x, 0.f);
}
struct RowK { float k1[4], k2[4], ri[4]; v2u aug[4]; };
DI void gelu_tile(f32x4 (&a)[4], const RowK& k, v2u (&o)[4]) {
#pragma unroll
  for (int t = 0; t < 4; ++t) {
    float y[4];
#pragma unroll
    for (int r = 0; r < 4; ++r) {
      const float v = a[t][r];
      const float e = __builtin_amdgcn_exp2f(v * __builtin_fmaf(v * v, k.k2[t], k.k1[t]));
      y[r] = v * __builtin_amdgcn_rcpf(__builtin_fmaf(e, k.ri[t], k.ri[t]));
    }
    o[t] = v2u{pk(y[0], y[1]), pk(y[2], y[3])};
  }
}
DI void aug_tile(f32x4 (&a)[4], v2u ta, const RowK& k) {
#pragma unroll
  for (int t = 0; t < 4; ++t) mmk16(a[t], ta, k.aug[t]);
}
DI void gelu_pair(f32x4 (&a0)[4], f32x4 (&a1)[4], int u, u32 lb, int lane, const RowK& k) {
  v2u o0[4], o1[4];
  gelu_tile(a0, k, o0);
  gelu_tile(a1, k, o1);
#pragma unroll
  for (int t = 0; t < 4; ++t) sts128(lb + L_H + u * 4096 + t * 1024 + lane * 16, v4u{o0[t][0], o0[t][1], o1[t][0], o1[t][1]});
}
DI void part_pair(const f32x4 (&a0)[4], const f32x4 (&a1)[4], rsrc_t rs, int v16, int sb, int town, u32 lown) {
#pragma unroll
  for (int t = 0; t < 4; ++t) {
    const v4u d = {pk(a0[t][0], a0[t][1]), pk(a0[t][2], a0[t][3]), pk(a1[t][0], a1[t][1]), pk(a1[t][2], a1[t][3])};
    if (t == town) sts128(lown, d); else stx(d, rs, v16, sb + t * (2 * 8 * 9216));
  }
}
typedef GAS u64 gu64;
DI void drain() { asm volatile("s_waitcnt vmcnt(0) lgkmcnt(0)" ::: "memory"); }
DI void flag(gu64* p, bool on, u64 tag) { if (on) __hip_atomic_store(p, tag, RLX); }
template <int SL> DI void poll(gu64* p, bool on, u64 tag) {
  for (u32 sp = 0; sp < (1u << 16); ++sp) {
    const u64 x = on ? __hip_atomic_load(p, RLX) : tag;
    if (__builtin_amdgcn_ballot_w64(x != tag) == 0) break;
    if (SL) __builtin_amdgcn_s_sleep(SL);
  }
  asm volatile("" ::: "memory");
}
DI u32 lds_ticket(u32 a, int lane) {
  u32 r = 0;
  if (lane == 0) r = __hip_atomic_fetch_add((LAS u32*)(size_t)a, 1u, __ATOMIC_RELAXED, __HIP_MEMORY_SCOPE_WORKGROUP);
  return __builtin_amdgcn_readfirstlane(r);
}
template <int MT> DI void tile_mlp(const v4u (&m)[8], u32 lw, rsrc_t rs, int lane, int c, int cb, int p0, int part) {
  const int g = lane >> 4, tl = lane & 15;
  const u32 wr = lw + (lane >> 3) * 144 + (lane & 7) * 16, tr = lw + (8 * g + (tl >> 2)) * 144 + (tl & 3) * 16;
  const int wmc = WM_OFF + smul(c, 8 * 165888);
  f32x4 cs[4];
#pragma unroll
  for (int q = 0; q < 4; ++q) cs[q] = f32x4{0.f, 0.f, 0.f, 0.f};
#pragma unroll
  for (int h = 0; h < 2; ++h) {
#pragma unroll
    for (int j = 0; j < 4; ++j) sts128(wr + j * 8 * 144, m[4 * h + j]);
    asm volatile("s_waitcnt lgkmcnt(0)" ::: "memory");
    v4u f[4];
#pragma unroll
    for (int q = 0; q < 4; ++q) {
      const v2u lo = ldstr(tr + (q >> 1) * 64 + (q & 1) * 8), hi = ldstr(tr + (q >> 1) * 64 + (q & 1) * 8 + 4 * 144);
      f[q] = v4u{lo[0], lo[1], hi[0], hi[1]};
    }
    asm volatile("s_waitcnt lgkmcnt(0)" ::: "memory");
#pragma unroll
    for (int q = 0; q < 4; ++q) {
      const int u = 2 * cb + (q >> 1), e = q & 1;
      int wd, y0, ys;
      if (MT == 0) {
        wd = u < 8 ? u : u < 16 ? u - 8 : 4 + q;
        y0 = u < 8 ? e : u < 16 ? 36 + e : 38;
        ys = u < 12 ? 2 : 3;
        cs[q] = mfma(f[q], v4u{0x3f803f80u, 0x3f803f80u, 0x3f803f80u, 0x3f803f80u}, cs[q]);
      } else {
        const bool sp = cb == 8, fa = (cb & 1) == 0;
        wd = sp ? q : fa ? cb >> 1 : 4 + (cb >> 1);
        y0 = sp ? 76 : fa ? 72 + q : 90 + 36 * (q >> 1) + e;
        ys = fa ? 5 : 2;
      }
      stx(f[q], rs, lane * 16, wmc + smul(wd, 165888) + (y0 + smul(ys, p0)) * 1024 + (h ? ys * 1024 : 0));
    }
  }
  if (MT == 0) {
    const f32x4 d = tl == 0 ? cs[0] : tl == 1 ? cs[1] : tl == 2 ? cs[2] : cs[3];
    stx(__builtin_bit_cast(v4u, d), rs, tl < 4 ? tl * 64 + g * 16 : OOB, CS1_OFF + smul(c, 20736) + smul(part, 2304) + cb * 256);
  }
}
#define FRESH() \
  int lane = lane_, w = w_, G = G_, c = c_; \
  asm volatile("" : "+v"(lane), "+s"(w), "+s"(G), "+s"(c)); \
  const int g = lane >> 4, tl = lane & 15, v16 = lane * 16, v16s = (lane ^ g) * 16; \
  const int wq = WQ_OFF + smul(c * 8 + w, 36864), wm = WM_OFF + smul(c * 8 + w, 165888); \
  const int cq = c >> 1, us = (cq & 2) ? 13 : 4, ub = (cq & 1) ? us + 1 : us - 4; \
  const int wo1 = WO_OFF + smul(c, 184320) + smul(w + (c & 2), 18432), wo2 = WO_OFF + smul(c, 184320) + smul(((c & 2) ? 0 : 8) + (w & 1), 18432); \
  const bool first = (G & 15) == 0; \
  const int rowbase = 2 * 64 * (G & 15) + (G >> 4), rot = krot(c), gofs = smul(layer * 32 + G, 73728); \
  const bool ra = w < 4; \
  gu64* mbg = mb + (layer * 256 + G * 8) * 128; \
  (void)rot; (void)gofs; (void)v16s; (void)ra; (void)mbg; \
  (void)g; (void)tl; (void)v16; (void)wq; (void)wm; (void)ub; (void)us; (void)wo1; (void)wo2; (void)first; (void)rowbase;
#define STAMP(i)
#define WST(i)

#if defined(__HIP_DEVICE_COMPILE__)
#define NOPK __attribute__((target("no-packed-fp32-ops")))
#else
#define NOPK
#endif
__global__ NOPK void __launch_bounds__(512, 2) k_main(const u16* __restrict__ x, const u16* __restrict__ wqkv, const u16* __restrict__ wo,
    const u16* __restrict__ w1, const u16* __restrict__ b1, const u16* __restrict__ w2, const u16* __restrict__ b2, u16* __restrict__ out, char* __restrict__ ws) {
  extern __shared__ __attribute__((aligned(16))) char smem[];
  const u32 lb = (u32)(size_t)(LAS char*)smem;
  asm volatile("" :: "s"(x), "s"(wqkv), "s"(wo), "s"(w1), "s"(b2), "s"(ws));
  const int tid = __builtin_amdgcn_workitem_id_x(), bid = __builtin_amdgcn_workgroup_id_x(), lane_ = tid & 63, w_ = __builtin_amdgcn_readfirstlane(tid >> 6);
  int G_, c_;
  {
    const int xc = bid & 7, s = bid >> 3;
    if (XG == 8) { c_ = xc; G_ = s; }
    else if (XG == 1) { G_ = 4 * xc + (s >> 3); c_ = s & 7; }
    else if (XG == 2) { G_ = 8 * (xc >> 1) + (s >> 2); c_ = 4 * (xc & 1) + (s & 3); }
    else { G_ = 16 * (xc >> 2) + (s >> 1); c_ = 2 * (xc & 3) + (s & 1); }
  }
  STAMP(0);
  const rsrc_t rs = __builtin_amdgcn_make_buffer_rsrc((void*)ws, 0, WS_USED, 0x00020000);
  const rsrc_t rx = __builtin_amdgcn_make_buffer_rsrc((void*)x, 0, 2048 * 1152, 0x00020000);
  const rsrc_t ro = __builtin_amdgcn_make_buffer_rsrc((void*)out, 0, 2048 * 1152, 0x00020000);
  gu64* mb = (gu64*)(ws + MB_OFF);
  u64 tag;
  v4u ring[18];
  {
    const int layer = 2;
    FRESH();
    WST(32);
    if (tid < 8) *(LAS u32*)(size_t)(lb + L_CNT + 4 * tid) = 0u;
    gu64* mbw = mb + (2 * 256 + lane * 8 + c) * 128 + G;
    u64 ep;
    const int r8 = lane >> 3, j8 = lane & 7;
    if (ra) {
      const bool isq = G < 18;
      v4u a[4];
      int so[4];
      u32 la, ls, rd[4], rh;
      if (isq) {
        const int kq = G + rot >= 18 ? G + rot - 18 : G + rot, hi = lane >= 27, r = lane - 27 * hi, sec = (r >= 9) + (r >= 18);
        const int vo = (hi * 3456 + sec * (1152 - 144) + r * 16) | (lane < 54 ? 0 : OOB), s0 = smul(32 * kq + 8 * w, 3456) + smul(c, 144);
        const rsrc_t rq = __builtin_amdgcn_make_buffer_rsrc((void*)wqkv, 0, 576 * 3456, 0x00020000);
#pragma unroll
        for (int j = 0; j < 4; ++j) a[j] = ldp(rq, vo, s0 + j * (2 * 3456));
        SB();
        la = lb + L_TQ + w * 3584 + hi * 448 + r * 16 + (lane < 54 ? 0 : 16384); ls = 896;
        const u32 tb = lb + L_TQ + (8 * g + (tl >> 2)) * 448, mo = tb + 72 * (w >> 1) + 40 * (w & 1) + 8 * (tl & 3);
        rd[0] = mo + 144; rd[1] = mo; rd[2] = mo + 288;
        rd[3] = tb + 32 + (w < 2 ? 0 : 288) + (tl & 1) * (w < 2 ? 144 : 72) + (tl & 2) * (w < 2 ? 36 : 0);
        rh = 4 * 448;
        so[0] = wq + G * 2048; so[1] = so[0] + 1024; so[2] = so[0] + 4 * 36864; so[3] = so[2] + 1024;
      } else {
        const int ti = (G - 18) * 4 + w, tk = smul(ti, 43) >> 7, tp = ti - smul(tk, 3);
        const int vo = (((r8 >> 2) * (tk < 16 ? 20 : 36) + (r8 & 3)) * 1152 + j8 * 16) | (ti < 54 ? 0 : OOB);
        const int s0 = smul(tk, tk < 16 ? 36 * 1152 : 288 * 1152) - (tk < 16 ? 0 : (16 * 288 - 16) * 1152) + (((cq & 1) ? us : ub) + 2 * tp) * 64, ss = tk < 16 ? 4 * 1152 : 72 * 1152;
        const rsrc_t rq = __builtin_amdgcn_make_buffer_rsrc((void*)wo, 0, 576 * 1152, 0x00020000);
        a[0] = ldp(rq, vo, s0); a[1] = ldp(rq, vo, s0 + ss); a[2] = ldp(rq, vo, s0 + 2 * ss); a[3] = ldp(rq, vo, s0 + 2 * ss + ss);
        SB();
        la = lb + L_TQ + w * 4608 + r8 * 144 + j8 * 16; ls = 1152;
        rd[0] = lb + L_TQ + w * 4608 + (8 * g + (tl >> 2)) * 144 + (tl & 3) * 16; rd[1] = rd[0] + 8; rd[2] = rd[0] + 64; rd[3] = rd[0] + 72;
        rh = 4 * 144;
        so[0] = WO_OFF + smul(c, 184320) + smul(tp, 4 * 18432) + tk * 1024 + (ti < 54 ? 0 : WS_DUMP); so[1] = so[0] + 18432;
        so[2] = tp < 2 ? so[0] + 2 * 18432 : WS_DUMP; so[3] = so[2] + 18432;
      }
#pragma unroll
      for (int j = 0; j < 4; ++j) so[j] = __builtin_amdgcn_readfirstlane(so[j]);
      ep = __hip_atomic_load((gu64*)(ws + EP_OFF), RLX);
      SB();
      WST(33);
      bar();
#pragma unroll
      for (int j = 0; j < 4; ++j) sts128(la + j * ls, a[j]);
      lds_arrive(lb + L_CNT + 12, lane);
      lds_wait(lb + L_CNT + 12, 4);
      WST(35);
      v4u f[4];
#pragma unroll
      for (int j = 0; j < 4; ++j) { const v2u lo = ldstr(rd[j]), hi = ldstr(rd[j] + rh); f[j] = v4u{lo[0], lo[1], hi[0], hi[1]}; }
#pragma unroll
      for (int j = 0; j < 4; ++j) stx(f[j], rs, so[j] >= WS_DUMP ? OOB : v16, so[j]);
      f32x4 cs[4];
#pragma unroll
      for (int j = 0; j < 4; ++j) cs[j] = mfma(f[j], v4u{0x3f803f80u, 0x3f803f80u, 0x3f803f80u, 0x3f803f80u}, f32x4{0.f, 0.f, 0.f, 0.f});
      const f32x4 d = tl == 0 ? cs[0] : tl == 1 ? cs[1] : tl == 2 ? cs[2] : cs[3];
      stx(__builtin_bit_cast(v4u, d), rs, isq && tl < 4 ? (tl >> 1) * 512 + (tl & 1) * 64 + g * 16 : OOB, CSQ_OFF + smul(c, 18432) + G * 1024 + w * 128);
      lds_arrive(lb + L_CNT + 16, lane);
      WST(36);
      drain();
      WST(37);
    } else {
      const int tt = w - 4;
      const int n = (c & 3) * 162 + tt * 54 + lane, row = (n * 7282) >> 16, ln = n - 9 * row;
      bar();
      const u32 t0 = __builtin_amdgcn_raw_buffer_load_b32(rx, w < 7 && lane < 54 && !(first && row < 8) ? (rowbase + 2 * (row - 8)) * 1152 + ln * 128 : OOB, 0, 0);
      u32 t1 = 0;
      if (w == 7 && lane < CODE_BYTES / 4096) {
        u64 pc;
        asm volatile("s_getpc_b64 %0" : "=s"(pc));
        t1 = *(const GAS u32*)((pc & ~127ull) + ((bid >> 3) + 32 * lane) * 128);
      }
      ep = __hip_atomic_load((gu64*)(ws + EP_OFF), RLX);
      const u32 bv = b2[tid - 256], bv2 = b2[tid], bv3 = b2[512 + lane];
      const int kt = smul(G, 57) >> 9, cb = G - smul(kt, 9), p0 = smul(kt, 6) + 2 * tt;
      const bool ism = G < 27 && w < 7;
      v4u m1[8];
      {
        const rsrc_t r1 = __builtin_amdgcn_make_buffer_rsrc((void*)w1, 0, 576 * 9216, 0x00020000);
        const int v1 = ism ? r8 * 9216 + j8 * 16 : OOB;
        const int s1 = smul(p0 + rot >= 18 ? p0 + rot - 18 : p0 + rot, 32 * 9216) + smul(c, 1152) + cb * 128;
#pragma unroll
        for (int j = 0; j < 8; ++j) m1[j] = ldp(r1, v1, s1 + j * (8 * 9216));
      }
      SB();
      WST(33);
      *(LAS u32*)(size_t)(lb + L_TB2 + 4 * (tid - 256)) = bv << 16;
      *(LAS u32*)(size_t)(lb + L_TB2 + 4 * tid) = bv2 << 16;
      *(LAS u32*)(size_t)(lb + L_TB2 + 2048 + 4 * lane) = bv3 << 16;
      asm volatile("" :: "v"(t0), "v"(t1));
      const int vx = (rowbase + 32 * tt + 2 * r8) * 1152 + j8 * 16, vh = first ? OOB : (rowbase - 16 + 2 * r8) * 1152 + j8 * 16;
      u32 lx = lb + (j8 >> 2) * 5120 + ((j8 & 3) * 16 + (r8 ^ (j8 & 3))) * 16, lh = lx + 4096 + tt * 10240;
      lx += tt * 1024;
      u32 ly = lx + 5 * 10240;
      asm volatile("" : "+v"(lx), "+v"(ly));
      const int rp = rot >> 1;
#define KP(i) (((i) + rp >= 9 ? (i) + rp - 9 : (i) + rp) * 128)
      lds_wait(lb + L_CNT + 16, 4);
      WST(35);
      v4u xv[18], xh[3];
#pragma unroll
      for (int i = 0; i < 9; ++i) { const int so = KP(i); xv[2 * i] = ldp(rx, vx, so); xv[2 * i + 1] = ldp(rx, vx, so + 16 * 1152); }
      xh[0] = ldp(rx, vh, KP(tt)); xh[1] = ldp(rx, vh, KP(tt + 4)); xh[2] = ldp(rx, tt == 0 ? vh : OOB, KP(8));
      SB();
#pragma unroll
      for (int i = 0; i < 18; ++i) sts128((i < 10 ? lx + (i >> 1) * 10240 : ly + ((i >> 1) - 5) * 10240) + (i & 1) * 128, xv[i]);
      sts128(lh, xh[0]); sts128(lh + 128, xh[0]); sts128(lh + 40960, xh[1]); sts128(lh + 40960 + 128, xh[1]);
      if (tt == 0) { sts128(lh + 81920, xh[2]); sts128(lh + 81920 + 128, xh[2]); }
      lds_arrive(lb + L_CNT, lane);
      WST(36);
      if (ism) tile_mlp<0>(m1, lb + L_TW + tt * 4608, rs, lane, c, cb, p0, smul(kt, 3) + tt);
      WST(37);
    }
    tag = ((u64)(u32)__builtin_amdgcn_readfirstlane((u32)(ep >> 32)) << 32 | (u32)__builtin_amdgcn_readfirstlane((u32)ep)) + 1;
    if (ra && lds_ticket(lb + L_CNT + 20, lane) == 3) flag(mbw + MB_WQ, lane < 32, tag);
    STAMP(30);
    WST(38);
    if (w == 0) { poll<0>(mb + (2 * 256 + G * 8 + c) * 128 + MB_WQ + lane, lane < 32, tag); lds_arrive(lb + L_CNT + 8, lane); }
    lds_wait(lb + L_CNT + 8, 1);
    WST(43);
    fill<0, 18>(ring, rs, v16, wq);
    WST(44);
    if (w == 7) {
      v4u p[18];
#pragma unroll
      for (int i = 0; i < 18; ++i) p[i] = ldx(rs, v16, CSQ_OFF + smul(c, 18432) + i * 1024);
      f32x4 cs = __builtin_bit_cast(f32x4, p[0]);
#pragma unroll
      for (int i = 1; i < 18; ++i) cs += __builtin_bit_cast(f32x4, p[i]);
      *(LAS f32x4*)(size_t)(lb + L_TCQ + v16) = cs;
    }
  }

#pragma unroll 1
  for (int layer = 0; layer < 2; ++layer) {
    {
      FRESH();
      const int tt = w & 3;
      const int vh = first ? OOB : v16;
      const int sm = X2_OFF - 32 * 73728 + gofs + tt * 1024, sh = X2_OFF - 33 * 73728 + gofs + 3072;
#define KS(p) (((p) + rot >= 18 ? (p) + rot - 18 : (p) + rot) * 4096)
      if (layer == 1 && ra) {
        v4u xm[6], xh[2];
#pragma unroll
        for (int i = 0; i < 6; ++i) xm[i] = ldx(rs, v16, sm + KS(i));
        xh[0] = ldx(rs, vh, sh + KS(w));
        xh[1] = ldx(rs, w < 2 ? vh : OOB, sh + KS(4 + w));
        SB();
#pragma unroll
        for (int i = 0; i < 6; ++i) sts128(lb + i * 5120 + tt * 1024 + v16s, xm[i]);
        sts128(lb + w * 5120 + 4096 + v16s, xh[0]);
        if (w < 2) sts128(lb + (4 + w) * 5120 + 4096 + v16s, xh[1]);
        lds_arrive(lb + L_CNT, lane);
      }
      if (layer == 1 && !ra) {
        v4u ym[12], yh[3];
        const int h0 = 6 + smul(tt, 3);
        __builtin_amdgcn_s_sleep(1);
#pragma unroll
        for (int i = 0; i < 12; ++i) ym[i] = ldx(rs, v16, sm + KS(6 + i));
#pragma unroll
        for (int i = 0; i < 3; ++i) yh[i] = ldx(rs, vh, sh + KS(h0 + i));
        SB();
#pragma unroll
        for (int i = 0; i < 12; ++i) sts128(lb + (6 + i) * 5120 + tt * 1024 + v16s, ym[i]);
#pragma unroll
        for (int i = 0; i < 3; ++i) sts128(lb + (h0 + i) * 5120 + 4096 + v16s, yh[i]);
      }
      if (ra) lds_wait(lb + L_CNT, 8 * layer + 4);
      if (!ra) {
        bar();
        __builtin_amdgcn_s_setprio(3);
        f32x4 s = {0.f, 0.f, 0.f, 0.f}, q = s, sh_ = s, qh_ = s;
        v4u xm[18], xh[5];
#pragma unroll
        for (int i = 0; i < 18; ++i) xm[i] = lds128(lb + i * 5120 + tt * 1024 + v16s);
#pragma unroll
        for (int i = 0; i < 5; ++i) xh[i] = lds128(lb + (i < 4 ? 4 * i + tt : 16 + (tt & 1)) * 5120 + 4096 + v16s);
        if (tt >= 2) xh[4] = v4u{0u, 0u, 0u, 0u};
#pragma unroll
        for (int i = 0; i < 18; ++i) stat_acc(xm[i], s, q);
#pragma unroll
        for (int i = 0; i < 5; ++i) stat_acc(xh[i], sh_, qh_);
        const float m = s[0] * (1.f / 576.f), var = __builtin_fmaxf(__builtin_fmaf(diag(q, g, tl), 1.f / 576.f, -m * m), 0.f);
        const float r = __builtin_amdgcn_rsqf(var + 1e-5f), qh = diag(qh_, g, tl);
        if (lane < 32) *(LAS f32x2*)(size_t)(lb + L_SS + (tt + 4 * g) * 128 + tl * 8) = g == 0 ? f32x2{r, -m * r} : f32x2{sh_[0], qh};
        __builtin_amdgcn_s_setprio(0);
        lds_arrive(lb + L_CNT + 4, lane);
        if (layer == 0) {
          drain();
          if (lds_ticket(lb + L_CNT + 24, lane) == 3) flag(mb + (2 * 256 + lane * 8 + c) * 128 + G + MB_W1, lane < 32, tag);
        }
      }
    }
    STAMP(1 + 16 * layer);
    {
      FRESH();
      const bool swp = (w == 5) || (w == 7);
      f32x4 a0[5], a1[4];
      float rr[5], mr[5];
#pragma unroll
      for (int t = 0; t < 5; ++t) { rr[t] = 0.f; mr[t] = 0.f; }
#define ROWSTATS() \
      lds_wait(lb + L_CNT + 4, 8 * layer + 4); \
      _Pragma("unroll") for (int t = 0; t < 4; ++t) { const f32x2 p = ldsf2(lb + L_SS + t * 128 + tl * 8); rr[t] = p[0]; mr[t] = p[1]; } \
      { \
        f32x2 p = ldsf2(lb + L_SS + 4 * 128 + tl * 8); \
        _Pragma("unroll") for (int j = 5; j < 8; ++j) p += ldsf2(lb + L_SS + j * 128 + tl * 8); \
        const float m = p[0] * (1.f / 576.f), var = __builtin_fmaxf(__builtin_fmaf(p[1], 1.f / 576.f, -m * m), 0.f); \
        rr[4] = __builtin_amdgcn_rsqf(var + 1e-5f); \
        mr[4] = -m * rr[4]; \
      }
      if (!ra) {
        ROWSTATS();
        if (swp) { float a = rr[0]; rr[0] = rr[4]; rr[4] = a; a = mr[0]; mr[0] = mr[4]; mr[4] = a; }
      }
      groupq(a0, a1, ring, lb + v16s + (swp ? 4096 : 0), lb + v16s + 1024, lb + v16s + (swp ? 0 : 4096), rs, v16, wq + 18432, wo1, ra);
      STAMP(2 + 16 * layer);
      if (layer == 0) WST(45);
      if (ra) { ROWSTATS(); }
      const f32x4 c0 = ldsf4(lb + L_TCQ + w * 128 + g * 16), c1 = ldsf4(lb + L_TCQ + w * 128 + 64 + g * 16);
      const int t0 = w < 4 ? w : 4 + w, t1 = w < 4 ? 4 + w : 12 + ((w - 4) >> 1);
      const u32 e0 = lb + L_E + t0 * 2560 + lane * 8, e1 = lb + L_E + t1 * 2560 + lane * 8;
#pragma unroll
      for (int t = 0; t < 5; ++t) {
        const int slot = t == 0 ? (swp ? 0 : 1) : t == 4 ? (swp ? 1 : 0) : t + 1;
        float y[4];
#pragma unroll
        for (int r = 0; r < 4; ++r) y[r] = __builtin_fmaf(rr[t], a0[t][r], mr[t] * c0[r]);
        sts64(e0 + slot * 512, v2u{pk(y[0], y[1]), pk(y[2], y[3])});
        if (t < 4) {
#pragma unroll
          for (int r = 0; r < 4; ++r) y[r] = __builtin_fmaf(rr[t], a1[t][r], mr[t] * c1[r]);
          sts64(e1 + slot * 512, v2u{pk(y[0], y[1]), pk(y[2], y[3])});
        }
      }
    }
    if (layer == 0 && w_ < 4) {
      FRESH();
      if (w == 3) poll<0>(mb + (2 * 256 + G * 8 + c) * 128 + MB_W1 + lane, lane < 32, tag);
      else if (G < 27) {
        const rsrc_t r2 = __builtin_amdgcn_make_buffer_rsrc((void*)w2, 0, 4608 * 1152, 0x00020000);
        const int kt = smul(G, 57) >> 9, cb = G - smul(kt, 9), p0 = smul(kt, 6) + 2 * w;
        v4u m2[8];
#pragma unroll
        for (int j = 0; j < 8; ++j) m2[j] = ldp(r2, (lane >> 3) * 1152 + (lane & 7) * 16, smul(smul(c, 576) + 32 * p0, 1152) + cb * 128 + j * (8 * 1152));
        tile_mlp<1>(m2, lb + L_TW + w * 4608, rs, lane, c, cb, p0, 0);
      }
    }
    bar();
    STAMP(3 + 16 * layer);
    v4u nring[18], ex[6];
    {
      FRESH();
      fill<0, AUX1>(nring, rs, v16, wm);
      const int p = w & 1, qt = w >> 1;
      const u32 eb = lb + L_E + qt * 512;
      const v2u qr = lds64(eb + 12 * 2560 + 512 + ((2 * p) * 16 + tl) * 8);
      const v2u kp = lds64(eb + 12 * 2560 + ((2 * p + 1) * 16 + tl) * 8), kc = lds64(eb + 12 * 2560 + 512 + ((2 * p + 1) * 16 + tl) * 8);
      const u32 tro = ((lane & 3) * 16 + 4 * g + (tl >> 2)) * 8;
      const v2u r0 = ldstr(eb + 13 * 2560 + tro), r1 = ldstr(eb + 13 * 2560 + 512 + tro);
      const v4u vr = {r0[0], r0[1], r1[0], r1[1]};
      f32x4 m0, m1;
      const bool nohalo = first && qt == 0;
#pragma unroll
      for (int r = 0; r < 4; ++r) {
        const int kl = 4 * g + r;
        m0[r] = (kl >= tl + 9 && !nohalo) ? 0.f : -1e30f;
        m1[r] = (kl <= tl && kl + 7 >= tl) ? 0.f : -1e30f;
      }
      v4u om; v2u orr;
#pragma unroll
      for (int e = 0; e < 2; ++e) {
        const int h = 2 * p + e;
        const v2u k0 = lds64(eb + h * 2560 + lane * 8), k1 = lds64(eb + h * 2560 + 512 + lane * 8);
        const v2u qm = lds64(eb + (4 + h) * 2560 + 512 + lane * 8);
        const v2u v0 = ldstr(eb + (8 + h) * 2560 + tro), v1 = ldstr(eb + (8 + h) * 2560 + 512 + tro);
        const v4u qf = {qm[0], qm[1], g == 0 ? qr[e] : 0u, 0u};
        const f32x4 s0 = mfma(v4u{k0[0], k0[1], kp[e], 0u}, qf, m0), s1 = mfma(v4u{k1[0], k1[1], kc[e], 0u}, qf, m1);
        float mx = __builtin_fmaxf(__builtin_fmaxf(__builtin_fmaxf(s0[0], s0[1]), __builtin_fmaxf(s0[2], s0[3])), __builtin_fmaxf(__builtin_fmaxf(s1[0], s1[1]), __builtin_fmaxf(s1[2], s1[3])));
        mx = xmax(mx);
        constexpr float sc = 0.23570226039551584f * 1.4426950408889634f;
        const float nb = -mx * sc;
        float pe[8], sum = 0.f;
#pragma unroll
        for (int r = 0; r < 4; ++r) {
          pe[r] = __builtin_amdgcn_exp2f(__builtin_fmaf(s0[r], sc, nb));
          pe[4 + r] = __builtin_amdgcn_exp2f(__builtin_fmaf(s1[r], sc, nb));
          sum += pe[r] + pe[4 + r];
        }
        const float inv = __builtin_amdgcn_rcpf(xsum(sum));
        const v4u pf = {pk(pe[0] * inv, pe[1] * inv), pk(pe[2] * inv, pe[3] * inv), pk(pe[4] * inv, pe[5] * inv), pk(pe[6] * inv, pe[7] * inv)};
        const f32x4 z = {0.f, 0.f, 0.f, 0.f};
        const f32x4 o = mfma(v4u{v0[0], v0[1], v1[0], v1[1]}, pf, z), oq = mfma(vr, pf, z);
        om[2 * e] = pk(o[0], o[1]); om[2 * e + 1] = pk(o[2], o[3]);
        orr[e] = pk(oq[2 * e], oq[2 * e + 1]);
      }
      const int ob = OB_OFF + gofs + qt * 1024;
      stx(om, rs, v16, ob + (2 * c + p) * 4096);
      stx2(orr, rs, g == p ? tl * 8 : OOB, ob + (16 + (c >> 2)) * 4096 + (c & 3) * 256 + p * 128);
      STAMP(4 + 16 * layer);
      drain();
      bar();
      flag(mbg + lane * 128 + MB_O + c, w == 7 && lane < 8, tag);
      if (layer == 0) flag(mb + (2 * 256 + lane * 8 + c) * 128 + G + MB_W2, w == 6 && lane < 32, tag);
      STAMP(5 + 16 * layer);
      SB();
#pragma unroll
      for (int i = 0; i < 6; ++i) ex[i] = ldw(rs, w < 6 ? v16 : OOB, wo2 + smul(w >> 1, 6144) + i * 1024);
      fill<AUX1, AUX2>(nring, rs, v16, wm);
      if (layer == 0 && w < 6) {
        const int i = w * 96 + lane;
#pragma unroll
        for (int j = 0; j < 2; ++j) {
          if (j == 0 || lane < 32) {
            const int n = i + 64 * j;
            float cs = 0.f;
#pragma unroll
            for (int k = 0; k < 9; ++k) cs += __builtin_bit_cast(float, __builtin_amdgcn_raw_buffer_load_b32(rs, n * 4, CS1_OFF + smul(c, 20736) + k * 2304, 16));
            u32 h, m, l;
            split3(cs, h, m, l);
            sts64(lb + L_TA + 8 * n, v2u{h | (m << 16), b1[576 * c + colp(n >> 5, (n >> 4) & 1, n & 15)] | (l << 16)});
          }
        }
      }
      if (w == 7) poll<0>(mbg + c * 128 + MB_O + lane, lane < 8, tag);
    }
    bar();
    STAMP(6 + 16 * layer);
    {
      FRESH();
      const int th = c & 1;
      const int ob = OB_OFF + gofs + th * 2048;
      v4u of[5];
#pragma unroll
      for (int i = 0; i < 4; ++i) of[i] = ldx(rs, v16, ob + (4 * i + (w >> 1)) * 4096 + (w & 1) * 1024);
      of[4] = ldx(rs, ra ? v16 : OOB, ob + (16 + (w >> 1)) * 4096 + (w & 1) * 1024);
      fill<AUX2, AUX3>(nring, rs, v16, wm);
#pragma unroll
      for (int i = 0; i < 4; ++i) sts128(lb + L_OI + (8 * i + w) * 1024 + v16, of[i]);
      if (ra) {
        const u32 a = lb + L_OI + (32 + w) * 1024 + (g * 16 + 2 * (lane & 7)) * 16 + ((lane >> 3) & 1) * 8;
        sts64(a, v2u{of[4][0], of[4][1]});
        sts64(a + 16, v2u{of[4][2], of[4][3]});
      }
      bar();
      STAMP(7 + 16 * layer);
      f32x4 a[3];
      groupo(a, ring, ex, lb + L_OI + v16, lb + L_OI + v16 + (cq & 1) * 1024 + (w < 6 ? 6 * (w >> 1) * 2048 : 0));
#define POS(u) ((u) - rot < 0 ? (u) - rot + 18 : (u) - rot)
      const int u1 = ub + (w >> 1), p1 = POS(u1), ps = POS(us);
#pragma unroll
      for (int t = 0; t < 2; ++t) {
        const u32 ad = lb + p1 * 5120 + (2 * th + t) * 1024 + v16s + (w & 1) * 8;
        const v2u xv = lds64(ad);
        sts64(ad, v2u{pk(lo2f(xv[0]) + a[t][0], hi2f(xv[0]) + a[t][1]), pk(lo2f(xv[1]) + a[t][2], hi2f(xv[1]) + a[t][3])});
      }
      if (w < 6) *(LAS f32x4*)(size_t)(lb + L_PX + w * 1024 + v16) = a[2];
      bar();
      const int xb = X1_OFF + gofs;
      stx(lds128(lb + p1 * 5120 + (2 * th + (w & 1)) * 1024 + v16s), rs, v16, xb + u1 * 4096 + (2 * th + (w & 1)) * 1024);
      if (w == 6) {
        const v4u xv = lds128(lb + ps * 5120 + (2 * th + (cq & 1)) * 1024 + v16s);
        const u32 pa = lb + L_PX + v16;
        const f32x4 e0 = ldsf4(pa) + ldsf4(pa + 2048) + ldsf4(pa + 4096), e1 = ldsf4(pa + 1024) + ldsf4(pa + 3072) + ldsf4(pa + 5120);
        stx(v4u{pk(lo2f(xv[0]) + e0[0], hi2f(xv[0]) + e0[1]), pk(lo2f(xv[1]) + e0[2], hi2f(xv[1]) + e0[3]),
                pk(lo2f(xv[2]) + e1[0], hi2f(xv[2]) + e1[1]), pk(lo2f(xv[3]) + e1[2], hi2f(xv[3]) + e1[3])},
            rs, v16, xb + us * 4096 + (2 * th + (cq & 1)) * 1024);
      }
      STAMP(8 + 16 * layer);
      drain();
      bar();
      flag(mbg + lane * 128 + MB_X1 + c, w == 7 && lane < 8, tag);
      fill<AUX3, 18>(nring, rs, v16, wm);
      if (w == 7) poll<0>(lane < 8 ? mbg + c * 128 + MB_X1 + lane : mb + (2 * 256 + G * 8 + c) * 128 + MB_W2 + lane - 8, lane < (layer == 0 ? 40 : 8), tag);
    }
    bar();
    STAMP(9 + 16 * layer);
#pragma unroll
    for (int i = 0; i < 18; ++i) ring[i] = nring[i];
    {
      FRESH();
      const int tt = w & 3;
      const int sm = X1_OFF + gofs + tt * 1024;
      if (ra) {
        v4u xm[6];
#pragma unroll
        for (int i = 0; i < 6; ++i) xm[i] = ldx(rs, v16, sm + KS(i));
        SB();
#pragma unroll
        for (int i = 0; i < 6; ++i) sts128(lb + i * 4096 + tt * 1024 + v16, xm[i]);
        lds_arrive(lb + L_CNT, lane);
        lds_wait(lb + L_CNT, 8 * layer + 8);
      } else {
        v4u ym[12];
        __builtin_amdgcn_s_sleep(1);
#pragma unroll
        for (int i = 0; i < 12; ++i) ym[i] = ldx(rs, v16, sm + KS(6 + i));
        SB();
#pragma unroll
        for (int i = 0; i < 12; ++i) sts128(lb + (6 + i) * 4096 + tt * 1024 + v16, ym[i]);
        bar();
        __builtin_amdgcn_s_setprio(3);
        v4u xm[6];
#pragma unroll
        for (int i = 0; i < 6; ++i) xm[i] = lds128(lb + i * 4096 + tt * 1024 + v16);
        f32x4 s = {0.f, 0.f, 0.f, 0.f}, q = s;
#pragma unroll
        for (int i = 0; i < 12; ++i) stat_acc(ym[i], s, q);
#pragma unroll
        for (int i = 0; i < 6; ++i) stat_acc(xm[i], s, q);
        const float m = s[0] * (1.f / 576.f), var = __builtin_fmaxf(__builtin_fmaf(diag(q, g, tl), 1.f / 576.f, -m * m), 0.f);
        const float r1 = __builtin_amdgcn_rsqf(var + 1e-5f), r = r1 * __builtin_amdgcn_rsqf(var * r1 * r1 + 1e-5f), ri = __builtin_amdgcn_rcpf(r);
        constexpr float k1 = -2.f * 0.7978845608028654f * 1.4426950408889634f, k2 = k1 * 0.044715f;
        u32 mh, mm_, ml, rh, rm, rl;
        split3(-m, mh, mm_, ml); split3(ri, rh, rm, rl);
        const u32 mv = g == 0 ? mh : g == 1 ? mm_ : g == 2 ? ml : 0u, rv = g == 0 ? rh : g == 1 ? rm : g == 2 ? rl : 0u;
        sts64(lb + L_RK + tt * 512 + lane * 8, v2u{mv | (mv << 16), rv | (mv << 16)});
        if (lane < 16) *(LAS f32x4*)(size_t)(lb + L_SS + tt * 256 + tl * 16) = f32x4{k1 * r, k2 * r * r * r, ri, 0.f};
        __builtin_amdgcn_s_setprio(0);
        lds_arrive(lb + L_CNT + 4, lane);
      }
    }
    STAMP(10 + 16 * layer);
    {
      FRESH();
      RowK rk;
#pragma unroll
      for (int t = 0; t < 4; ++t) { rk.k1[t] = 0.f; rk.k2[t] = 0.f; rk.ri[t] = 0.f; rk.aug[t] = v2u{0u, 0u}; }
      const int i = w & 3, ot = c >> 1, hb = c & 1;
      const int pb = PB_OFF + gofs * 8 + smul((i >> 1) * 8 + c, 9216);
      const int town = (i >> 1) == hb ? ot : -1;
      const u32 xa = lb + v16, ha = lb + L_H + v16, ta = lb + L_TA + tl * 8, lo = xa + (ot ^ 1) * 1024;
      int sn = wm + 18432;
      WST(0);
#pragma unroll 1
      for (int gi = 0; gi < (ra ? 2 : 3); ++gi) {
        if (!ra && gi == 1) {
          f32x4 a[3][4];
          const v2u t0 = lds64(ta + (12 + i) * 256), t1 = lds64(ta + (12 + i) * 256 + 128), t2 = lds64(ta + (16 + (i >> 1)) * 256 + (i & 1) * 128);
          group<3, 99, true>(a, ring, xa, rs, v16, sn, v16, sn + 2 * 18432, false);
          sn += 3 * 18432;
          aug_tile(a[0], t0, rk); aug_tile(a[1], t1, rk); aug_tile(a[2], t2, rk);
          mm_end();
          WST(7);
          bar();
          WST(8);
          gelu_pair(a[0], a[1], 12 + i, lb, lane, rk);
          v2u o2[4];
          gelu_tile(a[2], rk, o2);
#pragma unroll
          for (int t = 0; t < 4; ++t) sts64(ha + (16 + (i >> 1)) * 4096 + t * 1024 + (i & 1) * 8, o2[t]);
          WST(9);
          bar();
          WST(10);
        }
        const bool g1 = ra || gi == 0;
        f32x4 a[2][4];
        group<2, 6, true>(a, ring, g1 ? xa : ha, rs, v16, sn, gi == 2 ? OOB : v16, sn + 18432, ra && gi == 0);
        sn += 2 * 18432;
        WST(1 + 2 * gi);
        if (gi == 0) {
          lds_wait(lb + L_CNT + 4, 8 * layer + 8);
#pragma unroll
          for (int t = 0; t < 4; ++t) {
            const f32x4 p = ldsf4(lb + L_SS + t * 256 + tl * 16);
            rk.k1[t] = p[0]; rk.k2[t] = p[1]; rk.ri[t] = p[2];
            rk.aug[t] = lds64(lb + L_RK + t * 512 + lane * 8);
          }
        }
        if (g1) {
          const int u = ra ? 8 * gi + i : 4 + i;
          __builtin_amdgcn_s_setprio(3);
          aug_tile(a[0], lds64(ta + u * 256), rk); aug_tile(a[1], lds64(ta + u * 256 + 128), rk);
          __builtin_amdgcn_s_setprio(0);
          mm_end();
          gelu_pair(a[0], a[1], u, lb, lane, rk);
        } else {
          const int u = 4 * i + 1 + gi;
          part_pair(a[0], a[1], rs, v16, pb + (u & 7) * 1024, town, lo + u * 4096);
        }
        WST(2 + 2 * gi);
      }
      if (ra) {
        f32x4 a[5][4];
        bar();
        STAMP(11 + 16 * layer);
        WST(8);
        group<5, 12, false>(a, ring, ha, rs, v16, sn, v16, 0, true);
        WST(11);
        part_pair(a[0], a[1], rs, v16, pb + ((4 * i) & 7) * 1024, town, lo + (4 * i) * 4096);
        part_pair(a[2], a[3], rs, v16, pb + ((4 * i + 1) & 7) * 1024, town, lo + (4 * i + 1) * 4096);
#pragma unroll
        for (int t = 0; t < 4; ++t) {
          const v2u d = {pk(a[4][t][0], a[4][t][1]), pk(a[4][t][2], a[4][t][3])};
          if (t == town) sts64(lo + (16 + hb) * 4096 + (i & 1) * 8, d);
          else stx2(d, rs, lane * 8, pb + 8192 + (i & 1) * 512 + t * (2 * 8 * 9216));
        }
      }
      WST(12);
      STAMP(12 + 16 * layer);
      drain();
      flag(mbg + (2 * lane + (i >> 1)) * 128 + (ra ? MB_A : MB_B) + 2 * c + (i & 1), lane < 4, tag);
      WST(13);
      if (!ra) bar();
      fill<0, BF1>(ring, rs, ra || layer == 1 ? OOB : v16, wq);
    }
    if (w_ < 4) {
      FRESH();
      const int tt = c >> 1, hb = c & 1;
      const int pb = PB_OFF + gofs * 8 + smul(c, 8 * 9216);
#pragma unroll 1
      for (int rep = (w == 0 ? 0 : 1); rep < 3; ++rep) {
        if (rep == 2) bar();
        if (rep != 1 || w != 0) {
          __builtin_amdgcn_s_sleep(PSL);
          poll<4>(mbg + c * 128 + (rep == 2 ? MB_B : MB_A) + lane, lane < 16, tag);
        }
        if (rep == 2) { WST(14); STAMP(13 + 16 * layer); }
        const int slot = (w >> 1) * 4 + (w & 1) + (rep == 2 ? 2 : 0), u = rep == 0 ? 16 + hb : 8 * hb + slot;
        v4u pv[7];
        if (rep == 0) {
#pragma unroll
          for (int j = 0; j < 7; ++j) {
            const int o = pb + smul((c + 1 + j) & 7, 9216) + 8192;
            const v2u a = ldx2(rs, lane * 8, o), b = ldx2(rs, lane * 8, o + 512);
            pv[j] = v4u{a[0], a[1], b[0], b[1]};
          }
        } else {
#pragma unroll
          for (int j = 0; j < 7; ++j) pv[j] = ldx(rs, v16, pb + smul((c + 1 + j) & 7, 9216) + slot * 1024);
        }
        SB();
        const v4u xv = lds128(lb + POS(u) * 4096 + tt * 1024 + v16), ov = lds128(lb + u * 4096 + (tt ^ 1) * 1024 + v16);
        const f32x4 ba = ldsf4(lb + L_TB2 + u * 128 + g * 32), bb = ldsf4(lb + L_TB2 + u * 128 + g * 32 + 16);
        float y[8];
#pragma unroll
        for (int k = 0; k < 4; ++k) {
          y[2 * k] = lo2f(xv[k]) + lo2f(ov[k]) + (k < 2 ? ba[2 * k] : bb[2 * k - 4]);
          y[2 * k + 1] = hi2f(xv[k]) + hi2f(ov[k]) + (k < 2 ? ba[2 * k + 1] : bb[2 * k - 3]);
        }
#pragma unroll
        for (int j = 0; j < 7; ++j)
#pragma unroll
          for (int k = 0; k < 4; ++k) { y[2 * k] += lo2f(pv[j][k]); y[2 * k + 1] += hi2f(pv[j][k]); }
        const v4u o = {pk(y[0], y[1]), pk(y[2], y[3]), pk(y[4], y[5]), pk(y[6], y[7])};
        if (layer == 0) stx(o, rs, v16, X2_OFF + gofs + u * 4096 + tt * 1024);
        else st128<0>(o, ro, (rowbase + 2 * (16 * tt + tl)) * 1152 + 16 * g, 64 * u);
        if (rep == 1) WST(15);
      }
      if (layer == 0) STAMP(14);
      if (layer == 0) {
        drain();
        const bool nxt = c >= 6 && (G & 15) != 15;
        flag(mbg + lane * 128 + MB_X2 + (lane < 8 ? 4 * c : 4 * c + 8) + w, lane < 8 || (nxt && lane < 16), tag);
      }
      WST(16);
    }
    STAMP(15 + 16 * layer);
    if (layer == 0) {
      FRESH();
      if (ra) fill<0, BF1>(ring, rs, v16, wq);
      else __builtin_amdgcn_s_sleep(BSL);
      fill<BF1, 18>(ring, rs, v16, wq);
      if (w == 7) poll<1>(mbg + c * 128 + MB_X2 + lane, lane < (first ? 32 : 40), tag);
      bar();
      STAMP(16);
    }
  }
  if (bid == 0 && tid == 448) __hip_atomic_store((gu64*)(ws + EP_OFF), tag, RLX);
}

extern "C" void kernel_launch(void* const* d_in, const int* in_sizes, int n_in, void* d_out, int out_size, void* d_ws, size_t ws_size,
                              hipStream_t stream) {
  const u16* x = (const u16*)d_in[0];
  const u16* wqkv = (const u16*)d_in[1];
  const u16* wo = (const u16*)d_in[2];
  const u16* w1 = (const u16*)d_in[4];
  const u16* b1 = (const u16*)d_in[5];
  const u16* w2 = (const u16*)d_in[6];
  const u16* b2 = (const u16*)d_in[7];
  (void)hipFuncSetAttribute((const void*)k_main, hipFuncAttributeMaxDynamicSharedMemorySize, L_TOTAL);
  k_main<<<256, 512, L_TOTAL, stream>>>(x, wqkv, wo, w1, b1, w2, b2, (u16*)d_out, (char*)d_ws);
}
```

```cpp
#include <hip/hip_runtime.h>
#include <hip/hip_bf16.h>

typedef unsigned short u16;
typedef unsigned u32;
typedef unsigned long long u64;
typedef u32 v4u __attribute__((ext_vector_type(4)));
typedef u32 v2u __attribute__((ext_vector_type(2)));
typedef float f32x4 __attribute__((ext_vector_type(4)));
typedef float f32x2 __attribute__((ext_vector_type(2)));
typedef short s16x4 __attribute__((ext_vector_type(4)));
typedef __bf16 bf16x2 __attribute__((ext_vector_type(2)));
typedef __bf16 bf16x8 __attribute__((ext_vector_type(8)));
#define GAS __attribute__((address_space(1)))
#define LAS __attribute__((address_space(3)))
#define DI __device__ __forceinline__
#define RLX __ATOMIC_RELAXED, __HIP_MEMORY_SCOPE_AGENT

#define XG 2
#define AUX1 6
#define AUX2 6
#define AUX3 12
#define BF1 12
#define PSL 6
#define BSL 24
#define B1LATE 1
#define HOLD 0
#define FW 1
#define APRIO 3
#define APOLL 1
#define PD0 0
#define MBW 1
#define PS1 8
#define NOEARLY 0
#define CH1 6
#define SIGK 14
#define STSL 3
#define STAMPS 0

#define CODE_BYTES 55296
constexpr int EP_OFF = 0;
constexpr int MB_OFF = 4096, MB_BYTES = 3 * 256 * 1024;
constexpr int MB_O = 0, MB_X1 = 16, MB_A = 32, MB_B = 48, MB_X2 = 80, MB_WQ = 0, MB_W1 = 32, MB_W2 = 64;
constexpr int CSQ_OFF = MB_OFF + MB_BYTES;
constexpr int CS1_OFF = CSQ_OFF + 8 * 18432;
constexpr int WQ_OFF = CS1_OFF + 8 * 20736;
constexpr int WO_OFF = WQ_OFF + 64 * 36864;
constexpr int WM_OFF = WO_OFF + 80 * 18432;
constexpr int OB_OFF = WM_OFF + 64 * 165888;
constexpr int X1_OFF = OB_OFF + 64 * 73728;
constexpr int X2_OFF = X1_OFF + 64 * 73728;
constexpr int PB_OFF = X2_OFF + 32 * 73728;
constexpr int ST_OFF = PB_OFF + 2 * 32 * 64 * 9216;
constexpr int WS_USED = ST_OFF + 2 * 256 * 32 * 8 + 256 * 2048;
constexpr int WS_DUMP = WS_USED;
constexpr int L_E = 92160, L_OI = 92160, L_H = 73728;
constexpr int L_TQ = 92160, L_TC = L_TQ + 18432 + 6 * 4608;
constexpr int L_PX = 129024;
constexpr int L_SS = 147456;
constexpr int L_RK = L_SS + 1024;
constexpr int L_CNT = L_RK + 2048;
constexpr int L_TA = L_CNT + 96;
constexpr int L_TB2 = L_TA + 4608;
constexpr int L_TCQ = L_TB2 + 2304;
constexpr int L_DBG = L_TCQ + 1024;
constexpr int L_TOTAL = L_DBG + (STAMPS ? 2048 : 0);

DI float bf2f(u16 b) { return __builtin_bit_cast(float, (u32)((u32)b) << 16); }
DI float lo2f(u32 x) { return __builtin_bit_cast(float, (u32)x << 16); }
DI float hi2f(u32 x) { return __builtin_bit_cast(float, (u32)x & 0xffff0000u); }
DI u32 pk(float a, float b) { f32x2 f = {a, b}; return __builtin_bit_cast(u32, __builtin_convertvector(f, bf16x2)); }
DI f32x4 mfma(v4u a, v4u b, f32x4 c) {
  return __builtin_amdgcn_mfma_f32_16x16x32_bf16(__builtin_bit_cast(bf16x8, a), __builtin_bit_cast(bf16x8, b), c, 0, 0, 0);
}
DI void mm(f32x4& c, v4u a, v4u b) { asm volatile("v_mfma_f32_16x16x32_bf16 %0, %1, %2, %0" : "+v"(c) : "v"(a), "v"(b)); }
DI void mm0(f32x4& c, v4u a, v4u b) { asm volatile("v_mfma_f32_16x16x32_bf16 %0, %1, %2, 0" : "=&v"(c) : "v"(a), "v"(b)); }
DI void mmk16(f32x4& c, v2u a, v2u b) { asm volatile("v_mfma_f32_16x16x16_bf16 %0, %1, %2, %0" : "+v"(c) : "v"(a), "v"(b)); }
DI void mm_end() { __builtin_amdgcn_sched_barrier(0); asm volatile("s_nop 7\n\ts_nop 1" ::: "memory"); __builtin_amdgcn_sched_barrier(0); }
DI float xsum(float v) {
  u32 x = __builtin_bit_cast(u32, v);
  auto a = __builtin_amdgcn_permlane16_swap(x, x, false, false);
  v = __builtin_bit_cast(float, (u32)a[0]) + __builtin_bit_cast(float, (u32)a[1]);
  x = __builtin_bit_cast(u32, v);
  auto b = __builtin_amdgcn_permlane32_swap(x, x, false, false);
  return __builtin_bit_cast(float, (u32)b[0]) + __builtin_bit_cast(float, (u32)b[1]);
}
DI float xmax(float v) {
  u32 x = __builtin_bit_cast(u32, v);
  auto a = __builtin_amdgcn_permlane16_swap(x, x, false, false);
  v = __builtin_fmaxf(__builtin_bit_cast(float, (u32)a[0]), __builtin_bit_cast(float, (u32)a[1]));
  x = __builtin_bit_cast(u32, v);
  auto b = __builtin_amdgcn_permlane32_swap(x, x, false, false);
  return __builtin_fmaxf(__builtin_bit_cast(float, (u32)b[0]), __builtin_bit_cast(float, (u32)b[1]));
}
#define SB() __builtin_amdgcn_sched_barrier(0)
#define INL __attribute__((always_inline))
DI int smul(int a, int k) { int r; asm("s_mul_i32 %0, %1, %2" : "=s"(r) : "s"(a), "s"(k)); return r; }
DI void bar() {
  asm volatile("s_waitcnt lgkmcnt(0)" ::: "memory");
  __builtin_amdgcn_s_barrier();
  asm volatile("" ::: "memory");
}
typedef __amdgpu_buffer_rsrc_t rsrc_t;
#define WAUX 16
DI v4u ldw(rsrc_t rs, int v, int s) { return __builtin_amdgcn_raw_buffer_load_b128(rs, v, s, WAUX); }
DI v4u ldp(rsrc_t rs, int v, int s) { return __builtin_amdgcn_raw_buffer_load_b128(rs, v, s, 0); }
constexpr int OOB = 0x70000000;
DI v4u ldx(rsrc_t rs, int v, int s) { return __builtin_amdgcn_raw_buffer_load_b128(rs, v, s, 16); }
DI v2u ldx2(rsrc_t rs, int v, int s) { return __builtin_amdgcn_raw_buffer_load_b64(rs, v, s, 16); }
template <int AUX> DI void st128(v4u d, rsrc_t rs, int v, int s) { __builtin_amdgcn_raw_buffer_store_b128(d, rs, v, s, AUX); asm volatile("s_nop 1" :: "v"(d)); }
DI void stx(v4u d, rsrc_t rs, int v, int s) { st128<16>(d, rs, v, s); }
DI void stx2(v2u d, rsrc_t rs, int v, int s) { __builtin_amdgcn_raw_buffer_store_b64(d, rs, v, s, 16); }
DI v4u lds128(u32 a) { return *(const LAS v4u*)(size_t)a; }
DI v2u lds64(u32 a) { return *(const LAS v2u*)(size_t)a; }
DI f32x4 ldsf4(u32 a) { return *(const LAS f32x4*)(size_t)a; }
DI f32x2 ldsf2(u32 a) { return *(const LAS f32x2*)(size_t)a; }
DI void sts128(u32 a, v4u v) { *(LAS v4u*)(size_t)a = v; }
DI void sts64(u32 a, v2u v) { *(LAS v2u*)(size_t)a = v; }
DI v2u ldstr(u32 a) { return __builtin_bit_cast(v2u, __builtin_amdgcn_ds_read_tr16_b64_v4i16((LAS s16x4*)(size_t)a)); }

__host__ __device__ __forceinline__ int colq(int c, int w, int t, int m) {
  if (w < 4 || t == 0) {
    const int h = w & 3, sec = w < 4 ? (t == 0 ? 1 : 0) : 2;
    return sec * 576 + 72 * c + 36 * (h >> 1) + 20 * (h & 1) + m;
  }
  const int qd = m >> 2;
  if (w < 6) return (qd & 1) * 576 + 72 * c + 36 * (qd >> 1) + 16 + (m & 3);
  return 2 * 576 + 72 * c + 36 * (qd & 1) + 16 + (m & 3);
}
__host__ __device__ __forceinline__ int krot(int c) { return 0; }
__host__ __device__ __forceinline__ int colp(int u, int e, int m) { return 32 * u + 8 * (m >> 2) + 4 * e + (m & 3); }
__host__ __device__ __forceinline__ int rowo(int ks, int g, int j) {
  if (ks < 16) return 72 * (ks >> 1) + 36 * (ks & 1) + 20 * (j >> 2) + 4 * g + (j & 3);
  return 72 * (4 * (ks - 16) + g) + 36 * (j >> 2) + 16 + (j & 3);
}
DI void lds_arrive(u32 a, int lane) {
  asm volatile("s_waitcnt lgkmcnt(0)" ::: "memory");
  if (lane == 0) __hip_atomic_fetch_add((LAS u32*)(size_t)a, 1u, __ATOMIC_RELAXED, __HIP_MEMORY_SCOPE_WORKGROUP);
}
DI void lds_waits(u32 a, u32 n) {
  while ((u32)__builtin_amdgcn_readfirstlane(*(volatile LAS u32*)(size_t)a) < n) __builtin_amdgcn_s_sleep(2);
  asm volatile("" ::: "memory");
}
DI void lds_wait(u32 a, u32 n) {
  while ((u32)__builtin_amdgcn_readfirstlane(*(volatile LAS u32*)(size_t)a) < n) {}
  asm volatile("" ::: "memory");
}
#define REFILL(f_, NU) do { const int q_ = (f_) % 18, u_ = (f_) / 18; \
    if (u_ < (NU) - 1) ring[q_] = ldw(rs, v16 + (q_ & 3) * 1024, s0 + u_ * 18432 + (q_ >> 2) * 4096); \
    else if (LAST == 1) ring[q_] = ldw(rs, v16 + (q_ & 3) * 1024, slast + (q_ >> 2) * 4096); } while (0)
template <int NT, int MIDBAR, int LAST, class MID, class PRE>
DI void group(f32x4 (&acc)[NT][4], v4u (&ring)[18], u32 xa, rsrc_t rs, int v16, int s0, int slast, MID&& mid, PRE&& pre) {
  v4u xf[2][4];
  u32 xb = xa + 9 * 4096;
  asm volatile("" : "+v"(xa), "+v"(xb));
#pragma unroll
  for (int t = 0; t < 4; ++t) xf[0][t] = lds128(xa + t * 1024);
  SB();
#pragma unroll
  for (int ks = 0; ks < 18; ++ks) {
    const int kn = ks + 1;
#pragma unroll
    for (int n = 0; n < NT; ++n) {
      const int f = ks * NT + n;
#pragma unroll
      for (int t = 0; t < 4; ++t) {
        if (ks == 0) mm0(acc[n][t], ring[f % 18], xf[0][t]); else mm(acc[n][t], ring[f % 18], xf[ks & 1][t]);
        if (t == 0 && f > 0) REFILL(f - 1, NT);
        const int n0 = kn == MIDBAR ? NT - 2 : 0;
        const int q = n == n0 ? t - 1 : (n == n0 + 1 && t == 1 ? 3 : -1);
        if (q == 0 && kn == MIDBAR) { mid(); SB(); }
        if (q >= 0 && kn < 18) xf[kn & 1][q] = kn < 9 ? lds128(xa + kn * 4096 + q * 1024) : lds128(xb + (kn - 9) * 4096 + q * 1024);
        if (kn == 18 && n == 0 && t == 1) pre();
        SB();
      }
    }
  }
  REFILL(18 * NT - 1, NT);
  SB();
}
template <bool REF, class MID>
DI void group36(f32x4 (&acc)[2][4], v4u (&r0)[18], v4u (&r1)[18], u32 xa, rsrc_t rs, int v16, int sn, MID&& mid) {
  v4u xf[2][4];
  u32 xb = xa + 9 * 4096;
  asm volatile("" : "+v"(xa), "+v"(xb));
#pragma unroll
  for (int t = 0; t < 4; ++t) xf[0][t] = lds128(xa + t * 1024);
  SB();
#pragma unroll
  for (int f = 0; f < 36; ++f) {
    const int ks = f >> 1, n = f & 1, kn = ks + 1;
#pragma unroll
    for (int t = 0; t < 4; ++t) {
      if (ks == 0) mm0(acc[n][t], r0[f], xf[0][t]); else mm(acc[n][t], f < 18 ? r0[f % 18] : r1[f % 18], xf[ks & 1][t]);
      if (REF && t == 0 && f > 0) (f - 1 < 18 ? r0[(f - 1) % 18] : r1[(f - 1) % 18]) = ldw(rs, v16 + ((f - 1) & 3) * 1024, sn + ((f - 1) >> 2) * 4096);
      const int q = n == 0 ? t - 1 : (t == 1 ? 3 : -1);
      if (q >= 0 && kn < 18) xf[kn & 1][q] = kn < 9 ? lds128(xa + kn * 4096 + q * 1024) : lds128(xb + (kn - 9) * 4096 + q * 1024);
      if (f == 2 * SIGK + 1 && t == 2) mid();
      SB();
    }
  }
  mm_end();
  if (REF) r1[17] = ldw(rs, v16 + 3072, sn + 8 * 4096);
  SB();
}
DI void groupq(f32x4 (&a0)[5], f32x4 (&a1)[4], v4u (&ring)[18], u32 x0, u32 x1, u32 x4, rsrc_t rs, int v16, int s0, int slast, u32 cnt, u32 want) {
  constexpr int LAST = 1;
  v4u xf[2][5];
  u32 y0 = x0 + 46080, y1 = x1 + 46080, y4 = x4 + 46080;
  asm volatile("" : "+v"(y0), "+v"(y1), "+v"(y4));
  xf[0][0] = lds128(x0); xf[0][1] = lds128(x1); xf[0][2] = lds128(x1 + 1024); xf[0][3] = lds128(x1 + 2048); xf[0][4] = lds128(x4);
  SB();
#pragma unroll
  for (int ks = 0; ks < 18; ++ks) {
    const int sl = (2 * ks) % 18, kn = ks + 1, o = (kn % 9) * 5120;
#pragma unroll
    for (int t = 0; t < 5; ++t) {
      if (ks == 0) mm0(a0[t], ring[sl], xf[0][t]); else mm(a0[t], ring[sl], xf[ks & 1][t]);
      if (t == 0 && ks > 0) REFILL(2 * ks - 1, 2);
      if (t == 1 && kn == CH1) { lds_wait(cnt, want); SB(); }
      if (t > 0 && kn < 18) xf[kn & 1][t - 1] = lds128(t == 1 ? (kn < 9 ? x0 : y0) + o : (kn < 9 ? x1 : y1) + o + (t - 2) * 1024);
      SB();
    }
#pragma unroll
    for (int t = 0; t < 4; ++t) {
      if (ks == 0) mm0(a1[t], ring[sl + 1], xf[0][t]); else mm(a1[t], ring[sl + 1], xf[ks & 1][t]);
      if (t == 0) REFILL(2 * ks, 2);
      if (t == 1 && kn < 18) xf[kn & 1][4] = lds128((kn < 9 ? x4 : y4) + o);
      SB();
    }
  }
  mm_end();
  REFILL(35, 2);
  SB();
}
DI void groupo(f32x4 (&a)[5], const v4u (&ring)[18], const v4u (&ex)[6], u32 x0, u32 x1, u32 xe) {
  v4u xf[3][2], ef[6];
  asm volatile("" : "+v"(x0), "+v"(x1), "+v"(xe));
#pragma unroll
  for (int k = 0; k < 2; ++k) { xf[k][0] = lds128(x0 + k * 2048); xf[k][1] = lds128(x1 + k * 2048); }
  SB();
#pragma unroll
  for (int ks = 0; ks < 9; ++ks) {
    const int kn = ks + 2;
#pragma unroll
    for (int j = 0; j < 4; ++j) {
      if (ks == 0) mm0(a[j], ring[2 * ks + (j >> 1)], xf[0][j & 1]); else mm(a[j], ring[2 * ks + (j >> 1)], xf[ks % 3][j & 1]);
      if (kn < 9 && j < 2) xf[kn % 3][j] = lds128((j ? x1 : x0) + kn * 2048);
      if (kn >= 9 && j < 3) ef[3 * (kn - 9) + j] = lds128(xe + (3 * (kn - 9) + j) * 2048);
      SB();
    }
  }
#pragma unroll
  for (int k = 0; k < 6; ++k) { if (k == 0) mm0(a[4], ex[0], ef[0]); else mm(a[4], ex[k], ef[k]); SB(); }
  mm_end();
}
template <int LO, int HI> DI void fill(v4u (&ring)[18], rsrc_t rs, int v16, int s) {
  SB();
#pragma unroll
  for (int i = LO; i < HI; ++i) ring[i] = ldw(rs, v16 + (i & 3) * 1024, s + (i >> 2) * 4096);
  SB();
}
template <class T> DI void undef(T& v) { asm volatile("" : "=v"(v)); }
DI void stat_acc(v4u v, f32x4& s, f32x4& q) { s = mfma(v4u{0x3f803f80u, 0x3f803f80u, 0x3f803f80u, 0x3f803f80u}, v, s); q = mfma(v, v, q); }
DI float diag(f32x4 q, int g, int tl) {
  const float a = (tl & 1) ? q[1] : q[0], b = (tl & 1) ? q[3] : q[2];
  return xsum(g == (tl >> 2) ? ((tl & 2) ? b : a) : 0.f);
}
DI void split3(float x, u32& h, u32& m, u32& l) {
  h = pk(x, 0.f); x -= lo2f(h);
  m = pk(x, 0.f); x -= lo2f(m);
  l = pk(x, 0.f);
}
struct RowK { f32x4 k[4]; v2u aug[4]; };
template <int N> DI void gelu(float (&v)[N], f32x4 k) {
  float y[N];
#pragma unroll
  for (int r = 0; r < N; ++r) y[r] = v[r] * v[r];
  SB();
#pragma unroll
  for (int r = 0; r < N; ++r) y[r] = __builtin_fmaf(y[r], k[1], k[0]);
  SB();
#pragma unroll
  for (int r = 0; r < N; ++r) y[r] = v[r] * y[r];
  SB();
#pragma unroll
  for (int r = 0; r < N; ++r) y[r] = __builtin_amdgcn_exp2f(y[r]);
  SB();
#pragma unroll
  for (int r = 0; r < N; ++r) y[r] = __builtin_fmaf(y[r], k[2], k[2]);
  SB();
#pragma unroll
  for (int r = 0; r < N; ++r) y[r] = __builtin_amdgcn_rcpf(y[r]);
  SB();
#pragma unroll
  for (int r = 0; r < N; ++r) v[r] = v[r] * y[r];
  SB();
}
DI v2u gelu4(f32x4 a, f32x4 k) {
  float v[4] = {a[0], a[1], a[2], a[3]};
  gelu<4>(v, k);
  return v2u{pk(v[0], v[1]), pk(v[2], v[3])};
}
DI void aug_tile(f32x4 (&a)[4], v2u ta, const RowK& k) {
#pragma unroll
  for (int t = 0; t < 4; ++t) mmk16(a[t], ta, k.aug[t]);
}
DI void gelu_pair(f32x4 (&a0)[4], f32x4 (&a1)[4], u32 hp, const RowK& k) {
#pragma unroll
  for (int t = 0; t < 4; ++t) {
    float v[8] = {a0[t][0], a0[t][1], a0[t][2], a0[t][3], a1[t][0], a1[t][1], a1[t][2], a1[t][3]};
    gelu<8>(v, k.k[t]);
    sts128(hp + t * 1024, v4u{pk(v[0], v[1]), pk(v[2], v[3]), pk(v[4], v[5]), pk(v[6], v[7])});
  }
}
DI v4u pk8(f32x4 a, f32x4 b) { return v4u{pk(a[0], a[1]), pk(a[2], a[3]), pk(b[0], b[1]), pk(b[2], b[3])}; }
DI void part_pair(const f32x4 (&a0)[4], const f32x4 (&a1)[4], rsrc_t rs, int v16, int sb, int town, u32 lown) {
#pragma unroll
  for (int t = 0; t < 4; ++t) {
    const v4u d = pk8(a0[t], a1[t]);
    if (t == town) sts128(lown, d); else stx(d, rs, v16, sb + t * (2 * 8 * 9216));
  }
}
DI void make_sel(v4u (&sel)[2], int g, int tl) {
  const u32 one = g == (tl >> 2) ? ((tl & 1) ? 0x3f800000u : 0x00003f80u) : 0u, lo = (tl & 2) ? 0u : one, hi = (tl & 2) ? one : 0u;
  sel[0] = v4u{lo, hi, 0u, 0u};
  sel[1] = v4u{0u, 0u, lo, hi};
}
typedef GAS u64 gu64;
DI void drain() { asm volatile("s_waitcnt vmcnt(0) lgkmcnt(0)" ::: "memory"); }
DI void flag(rsrc_t rs, int v, int s, bool on, v2u tag) { if (on) stx2(tag, rs, v, s); }
template <int SL> DI void poll(rsrc_t rs, int lane, int s, int n, v2u tag) {
  if (lane < n) {
#pragma unroll 1
    for (u32 sp = 0; sp < (1u << 16); ++sp) {
      v2u x = {0u, 0u};
      asm volatile("buffer_atomic_or_x2 %0, %1, %2, %3 offen sc0\n\ts_waitcnt vmcnt(0)" : "+v"(x) : "v"(lane * 8), "s"(rs), "s"(s) : "memory");
      if (__builtin_amdgcn_ballot_w64(((x[0] ^ tag[0]) | (x[1] ^ tag[1])) != 0u) == 0) break;
      if (SL) __builtin_amdgcn_s_sleep(SL);
      asm volatile("" ::: "memory");
    }
  }
  asm volatile("" ::: "memory");
}
DI u32 lds_ticket(u32 a, int lane) {
  u32 r = 0;
  if (lane == 0) r = __hip_atomic_fetch_add((LAS u32*)(size_t)a, 1u, __ATOMIC_RELAXED, __HIP_MEMORY_SCOPE_WORKGROUP);
  return __builtin_amdgcn_readfirstlane(r);
}
DI void tile_tr(const v4u* m, v4u (&f)[4], u32 lw, int lane) {
  const u32 wr = lw + (lane >> 3) * 144 + (lane & 7) * 16, tr = lw + (8 * (lane >> 4) + ((lane & 15) >> 2)) * 144 + (lane & 3) * 16;
#pragma unroll
  for (int j = 0; j < 4; ++j) sts128(wr + j * 8 * 144, m[j]);
  asm volatile("s_waitcnt lgkmcnt(0)" ::: "memory");
#pragma unroll
  for (int q = 0; q < 4; ++q) {
    const v2u lo = ldstr(tr + (q >> 1) * 64 + (q & 1) * 8), hi = ldstr(tr + (q >> 1) * 64 + (q & 1) * 8 + 4 * 144);
    f[q] = v4u{lo[0], lo[1], hi[0], hi[1]};
  }
  asm volatile("s_waitcnt lgkmcnt(0)" : "+v"(f[0]), "+v"(f[1]), "+v"(f[2]), "+v"(f[3]) :: "memory");
}
template <int MT> DI void mlp_dest(int c, int cb, int q, int& so, int& st) {
  const int u = 2 * cb + (q >> 1), e = q & 1;
  int wd, y0;
  if (MT == 0) {
    wd = u < 8 ? u : u < 16 ? u - 8 : 4 + q;
    y0 = u < 8 ? e : u < 16 ? 36 + e : 38;
    st = u < 12 ? 2048 : 3072;
  } else {
    const bool sp = cb == 8, fa = (cb & 1) == 0;
    wd = sp ? q : fa ? cb >> 1 : 4 + (cb >> 1);
    y0 = sp ? 76 : fa ? 72 + q : 90 + 36 * (q >> 1) + e;
    st = fa ? 5120 : 2048;
  }
  so = WM_OFF + smul(c * 8 + wd, 165888) + y0 * 1024;
}
#define FRESH() \
  int lane = lane_, w = w_, G = G_, c = c_; \
  asm volatile("" : "+s"(w)); asm volatile("" : "+s"(G)); asm volatile("" : "+s"(c)); asm volatile("" : "+v"(lane)); \
  const int g = lane >> 4, tl = lane & 15, v16 = lane * 16, v16s = (lane ^ g) * 16; \
  const int wq = WQ_OFF + smul(c * 8 + w, 36864), wm = WM_OFF + smul(c * 8 + w, 165888), woc = WO_OFF + smul(c, 184320); \
  const int us = (c & 4) ? 13 : 4, ub = (c & 2) ? us + 1 : us - 4; \
  const bool first = (G & 15) == 0, ra = w < 4; \
  const int rowbase = 2 * 64 * (G & 15) + (G >> 4), gofs = smul(layer * 32 + G, 73728), mbg = MB_OFF + (layer * 256 + G * 8) * 1024; \
  (void)g; (void)tl; (void)v16; (void)v16s; (void)wq; (void)wm; (void)woc; (void)ub; (void)us; (void)first; (void)ra; (void)rowbase; (void)gofs; (void)mbg;
#define STAMP(i)
#define WST(i)

#if defined(__HIP_DEVICE_COMPILE__)
#define NOPK __attribute__((target("no-packed-fp32-ops")))
#else
#define NOPK
#endif
__global__ NOPK void __launch_bounds__(512, 2) k_main(const u16* __restrict__ x, const u16* __restrict__ wqkv, const u16* __restrict__ wo,
    const u16* __restrict__ w1, const u16* __restrict__ b1, const u16* __restrict__ w2, const u16* __restrict__ b2, u16* __restrict__ out, char* __restrict__ ws) {
  extern __shared__ __attribute__((aligned(16))) char smem[];
  const u32 lb = (u32)(size_t)(LAS char*)smem;
  u32 codew;
  {
    u64 pc;
    asm volatile("s_getpc_b64 %0" : "=s"(pc));
    const u32 t = __builtin_amdgcn_workitem_id_x(), ln = (__builtin_amdgcn_workgroup_id_x() & ~7u) + (t >> 6) + 256 * (t & 1);
    const GAS u32* cp = (const GAS u32*)((pc & ~255ull) + (ln * 128 < CODE_BYTES ? ln * 128 : 0));
    asm volatile("global_load_dword %0, %1, off" : "=v"(codew) : "v"(cp) : "memory");
  }
  asm volatile("" :: "s"(x), "s"(wqkv), "s"(wo), "s"(w1), "s"(b2), "s"(ws));
  const int tid = __builtin_amdgcn_workitem_id_x(), bid = __builtin_amdgcn_workgroup_id_x(), lane_ = tid & 63, w_ = __builtin_amdgcn_readfirstlane(tid >> 6);
  int G_, c_;
  {
    const int xc = bid & 7, s = bid >> 3;
    if (XG == 8) { c_ = xc; G_ = s; }
    else if (XG == 1) { G_ = 4 * xc + (s >> 3); c_ = s & 7; }
    else if (XG == 2) { G_ = 8 * (xc >> 1) + (s >> 2); c_ = 4 * (xc & 1) + (s & 3); }
    else { G_ = 16 * (xc >> 2) + (s >> 1); c_ = 2 * (xc & 3) + (s & 1); }
  }
  STAMP(0);
  const rsrc_t rs = __builtin_amdgcn_make_buffer_rsrc((void*)ws, 0, WS_USED, 0x00020000);
  const rsrc_t rx = __builtin_amdgcn_make_buffer_rsrc((void*)x, 0, 2048 * 1152, 0x00020000);
  const rsrc_t ro = __builtin_amdgcn_make_buffer_rsrc((void*)out, 0, 2048 * 1152, 0x00020000);
  v2u tag;
  v4u ring[18], mt[4];
  {
    const int layer = 2;
    FRESH();
    WST(32);
    if (tid < 24) *(LAS u32*)(size_t)(lb + L_CNT + 4 * tid) = 0u;
    const int r8 = lane >> 3, j8 = lane & 7, tt = w & 3;
    const bool isq = G < 18;
    const v4u ones = {0x3f803f80u, 0x3f803f80u, 0x3f803f80u, 0x3f803f80u};
    const u32 lw = lb + L_TQ + 18432 + w * 4608;
    if (ra && isq) {
      const int hi = lane >= 27, r = lane - 27 * hi, sec = (r >= 9) + (r >= 18);
      const int vo = (hi * 3456 + sec * (1152 - 144) + r * 16) | (lane < 54 ? 0 : OOB), s0 = smul(32 * G + 8 * w, 3456) + smul(c, 144);
      const rsrc_t rq = __builtin_amdgcn_make_buffer_rsrc((void*)wqkv, 0, 576 * 3456, 0x00020000);
      v4u a[4], f[4];
#pragma unroll
      for (int j = 0; j < 4; ++j) a[j] = ldp(rq, vo, s0 + j * (2 * 3456));
      SB();
      WST(33);
      bar();
      const u32 la = lb + L_TQ + (lane < 54 ? w * 3584 : 14336) + hi * 448 + r * 16;
      const u32 tb = lb + L_TQ + (8 * g + (tl >> 2)) * 448, mo = tb + 72 * (w >> 1) + 40 * (w & 1) + 8 * (tl & 3);
      const u32 ro_ = tb + 32 + (w < 2 ? 0 : 288) + (tl & 1) * (w < 2 ? 144 : 72) + (tl & 2) * (w < 2 ? 36 : 0);
#pragma unroll
      for (int j = 0; j < 4; ++j) sts128(la + j * 896, a[j]);
      lds_arrive(lb + L_CNT + 12, lane);
      lds_wait(lb + L_CNT + 12, 4);
      WST(35);
#pragma unroll
      for (int j = 0; j < 4; ++j) {
        const u32 ad = j == 0 ? mo + 144 : j == 1 ? mo : j == 2 ? mo + 288 : ro_;
        const v2u lo = ldstr(ad), hi_ = ldstr(ad + 4 * 448);
        f[j] = v4u{lo[0], lo[1], hi_[0], hi_[1]};
      }
#pragma unroll
      for (int j = 0; j < 4; ++j) stx(f[j], rs, v16, wq + G * 2048 + (j & 1) * 1024 + (j >> 1) * (4 * 36864));
      f32x4 cs[4];
#pragma unroll
      for (int j = 0; j < 4; ++j) cs[j] = mfma(f[j], ones, f32x4{0.f, 0.f, 0.f, 0.f});
      const f32x4 d = tl == 0 ? cs[0] : tl == 1 ? cs[1] : tl == 2 ? cs[2] : cs[3];
      stx(__builtin_bit_cast(v4u, d), rs, tl < 4 ? (tl >> 1) * 512 + (tl & 1) * 64 + g * 16 : OOB, CSQ_OFF + smul(c, 18432) + G * 1024 + w * 128);
    }
    if (ra && !isq) {
      const int ti = (G - 18) * 4 + w, tk = smul(ti, 43) >> 7, tp = ti - smul(tk, 3);
      bar();
      if (ti < 54) {
        const int vo = ((r8 >> 2) * (tk < 16 ? 20 : 36) + (r8 & 3)) * 1152 + j8 * 16;
        const int s0 = smul(tk, tk < 16 ? 36 * 1152 : 288 * 1152) - (tk < 16 ? 0 : (16 * 288 - 16) * 1152) + (tp < 2 ? ub + 2 * tp : us) * 64, ss = tk < 16 ? 4 * 1152 : 72 * 1152;
        const rsrc_t rw = __builtin_amdgcn_make_buffer_rsrc((void*)wo, 0, 576 * 1152, 0x00020000);
        v4u a[4], f[4];
        a[0] = ldp(rw, vo, s0); a[1] = ldp(rw, vo, s0 + ss); a[2] = ldp(rw, vo, s0 + 2 * ss); a[3] = ldp(rw, vo, s0 + 2 * ss + ss);
        SB();
        WST(33);
        tile_tr(a, f, lw, lane);
        const int so = woc + (tp < 2 ? smul(tp, 2 * 18432) + tk * 2048 + (tk < 9 ? 0 : 3 * 18432) : 8 * 18432 + tk * 1024);
        stx(f[0], rs, v16, so); stx(f[1], rs, v16, so + (tp < 2 ? 1024 : 18432));
        if (tp < 2) { stx(f[2], rs, v16, so + 18432); stx(f[3], rs, v16, so + 18432 + 1024); }
      }
    }
    if (!ra) {
      const int n = (c & 3) * 162 + tt * 54 + lane, row = (n * 7282) >> 16, ln = n - 9 * row;
      bar();
      u32 t0 = 0;
      if (w < 7) t0 = __builtin_amdgcn_raw_buffer_load_b32(rx, lane < 54 && !(first && row < 8) ? (rowbase + 2 * (row - 8)) * 1152 + ln * 128 : OOB, 0, 0);
      else {
        const u64 ep = __hip_atomic_load((gu64*)(ws + EP_OFF), RLX);
        {
          const u32 z = 0, mo = lane < 48 ? (lane >> 4) * (256 * 1024) + (lane & 15) * 64 : OOB;
          asm volatile("buffer_atomic_or %0, %1, %2, %3 offen" :: "v"(z), "v"(mo), "s"(rs), "s"(MB_OFF + (G * 8 + c) * 1024) : "memory");
        }
        *(LAS u64*)(size_t)(lb + L_CNT + 32) = ep + 1;
        lds_arrive(lb + L_CNT + 28, lane);
      }
      const u32 bv = b2[tid - 256], bv2 = b2[tid], bv3 = b2[512 + lane];
      SB();
      WST(33);
      const int vx = (rowbase + 32 * tt + 2 * r8) * 1152 + j8 * 16, vh = first ? OOB : (rowbase - 16 + 2 * r8) * 1152 + j8 * 16 + tt * 128;
      u32 lx = lb + (j8 >> 2) * 5120 + ((j8 & 3) * 16 + (r8 ^ (j8 & 3))) * 16, lh = lx + 4096 + tt * 10240;
      lx += tt * 1024;
      u32 ly = lx + 5 * 10240;
      asm volatile("" : "+v"(lx), "+v"(ly));
      *(LAS u32*)(size_t)(lb + L_TB2 + 4 * (tid - 256)) = bv << 16;
      *(LAS u32*)(size_t)(lb + L_TB2 + 4 * tid) = bv2 << 16;
      *(LAS u32*)(size_t)(lb + L_TB2 + 2048 + 4 * lane) = bv3 << 16;
      asm volatile("" :: "v"(t0) : "memory");
      SB();
      WST(35);
      v4u xv[18], xh[3];
#pragma unroll
      for (int i = 0; i < 9; ++i) { xv[2 * i] = ldp(rx, vx + i * 128, 0); xv[2 * i + 1] = ldp(rx, vx + i * 128, 16 * 1152); }
      xh[0] = ldp(rx, vh, 0); xh[1] = ldp(rx, vh + 512, 0);
      undef(xh[2]);
      if (tt == 0) xh[2] = ldp(rx, vh + 1024, 0);
      SB();
#pragma unroll
      for (int i = 0; i < 18; ++i) sts128((i < 10 ? lx + (i >> 1) * 10240 : ly + ((i >> 1) - 5) * 10240) + (i & 1) * 128, xv[i]);
      sts128(lh, xh[0]); sts128(lh + 128, xh[0]); sts128(lh + 40960, xh[1]); sts128(lh + 40960 + 128, xh[1]);
      if (tt == 0) { sts128(lh + 81920, xh[2]); sts128(lh + 81920 + 128, xh[2]); }
      lds_arrive(lb + L_CNT, lane);
      lds_arrive(lb + L_CNT + 40, lane);
    }
    WST(36);
    drain();
    asm volatile("" :: "v"(codew));
    WST(37);
    lds_wait(lb + L_CNT + 28, 1);
    tag = lds64(lb + L_CNT + 32);
    if (ra && lds_ticket(lb + L_CNT + 20, lane) == 3) {
      flag(rs, lane * 8192, MB_OFF + (2 * 256 + c) * 1024 + (G + MB_WQ) * 8, lane < 32, tag);
      lds_arrive(lb + L_CNT + 16, lane);
    }
    STAMP(30);
    WST(38);
#pragma unroll
    for (int j = 0; j < 4; ++j) undef(mt[j]);
    if (G < 27 && w < 6) {
      const int kt = smul(G, 57) >> 9, cb = G - smul(kt, 9), p0 = smul(kt, 6) + w;
      const rsrc_t r1 = __builtin_amdgcn_make_buffer_rsrc((void*)w1, 0, 576 * 9216, 0x00020000);
      const int s1 = smul(p0, 32 * 9216) + smul(c, 1152) + cb * 128;
      v4u m1[4];
      lds_wait(lb + L_CNT + 16, 1);
#pragma unroll
      for (int j = 0; j < 4; ++j) m1[j] = ldp(r1, r8 * 9216 + j8 * 16, s1 + j * (8 * 9216));
      tile_tr(m1, mt, lw, lane);
      f32x4 cs[4];
#pragma unroll
      for (int q = 0; q < 4; ++q) cs[q] = mfma(mt[q], ones, f32x4{0.f, 0.f, 0.f, 0.f});
      const f32x4 d = tl == 0 ? cs[0] : tl == 1 ? cs[1] : tl == 2 ? cs[2] : cs[3];
      if (tl < 4) *(LAS f32x4*)(size_t)(lb + L_TC + w * 256 + tl * 64 + g * 16) = d;
    }
    if (w == 7) { poll<0>(rs, lane, mbg + c * 1024 + MB_WQ * 8, 32, tag); lds_arrive(lb + L_CNT + 8, lane); }
    lds_wait(lb + L_CNT + 8, 1);
    WST(43);
    if (!ra) __builtin_amdgcn_s_sleep(8);
    fill<0, 18>(ring, rs, v16, wq);
    WST(44);
    if (w == 7) {
      v4u p[18];
#pragma unroll
      for (int i = 0; i < 18; ++i) p[i] = ldx(rs, v16, CSQ_OFF + smul(c, 18432) + i * 1024);
      f32x4 cs = __builtin_bit_cast(f32x4, p[0]);
#pragma unroll
      for (int i = 1; i < 18; ++i) cs += __builtin_bit_cast(f32x4, p[i]);
      *(LAS f32x4*)(size_t)(lb + L_TCQ + v16) = cs;
    }
  }

#pragma unroll 1
  for (int layer = 0; layer < 2; ++layer) {
    f32x4 m0, m1;
    {
      FRESH();
      const int tt = w & 3;
      const bool swp = (w == 5) || (w == 7);
      f32x4 a0[5], a1[4];
      float rr[5], mr[5];
#pragma unroll
      for (int t = 0; t < 5; ++t) { undef(rr[t]); undef(mr[t]); }
      undef(m0); undef(m1);
#define ROWSTATS() \
      { \
        const bool nohalo = first && w < 2; \
        _Pragma("unroll") for (int r = 0; r < 4; ++r) { \
          const int kl = 4 * g + r; \
          m0[r] = (kl >= tl + 9 && !nohalo) ? 0.f : -1e30f; \
          m1[r] = (kl <= tl && kl + 7 >= tl) ? 0.f : -1e30f; \
        } \
        lds_wait(lb + L_CNT + 4, 8 * layer + 4); \
        _Pragma("unroll") for (int t = 0; t < 4; ++t) { const f32x2 p = ldsf2(lb + L_SS + t * 128 + tl * 8); rr[t] = p[0]; mr[t] = p[1]; } \
        f32x2 p = ldsf2(lb + L_SS + 4 * 128 + tl * 8); \
        _Pragma("unroll") for (int j = 5; j < 8; ++j) p += ldsf2(lb + L_SS + j * 128 + tl * 8); \
        const float m = p[0] * (1.f / 576.f), var = __builtin_fmaxf(__builtin_fmaf(p[1], 1.f / 576.f, -m * m), 0.f); \
        rr[4] = __builtin_amdgcn_rsqf(var + 1e-5f); \
        mr[4] = -m * rr[4]; \
      }
      lds_wait(lb + L_CNT, 8 * layer + 4);
      if (!ra) {
        lds_wait(lb + L_CNT + 40, 8 * layer + 4);
        __builtin_amdgcn_s_setprio(3);
        f32x4 s = {0.f, 0.f, 0.f, 0.f}, q = s, sh_ = s, qh_ = s;
        v4u xm[18], xh[5];
        u32 xa = lb + tt * 1024 + v16s, xb = xa + 46080, xc = lb + tt * 5120 + 4096 + v16s;
        asm volatile("" : "+v"(xa), "+v"(xb), "+v"(xc));
#pragma unroll
        for (int i = 0; i < 18; ++i) xm[i] = lds128((i < 9 ? xa : xb) + (i % 9) * 5120);
#pragma unroll
        for (int i = 0; i < 4; ++i) xh[i] = lds128(xc + i * 20480);
        xh[4] = v4u{0u, 0u, 0u, 0u};
        if (tt < 2) xh[4] = lds128(xc + 81920);
#pragma unroll
        for (int i = 0; i < 18; ++i) stat_acc(xm[i], s, q);
#pragma unroll
        for (int i = 0; i < 5; ++i) stat_acc(xh[i], sh_, qh_);
        const float m = s[0] * (1.f / 576.f), var = __builtin_fmaxf(__builtin_fmaf(diag(q, g, tl), 1.f / 576.f, -m * m), 0.f);
        const float r = __builtin_amdgcn_rsqf(var + 1e-5f), qh = diag(qh_, g, tl);
        if (lane < 32) *(LAS f32x2*)(size_t)(lb + L_SS + (tt + 4 * g) * 128 + tl * 8) = g == 0 ? f32x2{r, -m * r} : f32x2{sh_[0], qh};
        __builtin_amdgcn_s_setprio(0);
        lds_arrive(lb + L_CNT + 4, lane);
        ROWSTATS();
        if (swp) { float a = rr[0]; rr[0] = rr[4]; rr[4] = a; a = mr[0]; mr[0] = mr[4]; mr[4] = a; }
      }
      WST(18);
      STAMP(1 + 16 * layer);
      groupq(a0, a1, ring, lb + v16s + (swp ? 4096 : 0), lb + v16s + 1024, lb + v16s + (swp ? 0 : 4096), rs, v16, wq + 18432, woc + smul(w, 18432), lb + L_CNT + 40, 8 * layer + 4);
      WST(19);
      STAMP(2 + 16 * layer);
      if (layer == 0) WST(45);
      if (ra) ROWSTATS();
      const f32x4 c0 = ldsf4(lb + L_TCQ + w * 128 + g * 16), c1 = ldsf4(lb + L_TCQ + w * 128 + 64 + g * 16);
      const int t0 = w < 4 ? w : 4 + w, t1 = w < 4 ? 4 + w : 12 + ((w - 4) >> 1);
      const u32 e0 = lb + L_E + t0 * 2560 + lane * 8, e1 = lb + L_E + t1 * 2560 + lane * 8;
#pragma unroll
      for (int t = 0; t < 5; ++t) {
        const int slot = t == 0 ? (swp ? 0 : 1) : t == 4 ? (swp ? 1 : 0) : t + 1;
        float y[4];
#pragma unroll
        for (int r = 0; r < 4; ++r) y[r] = __builtin_fmaf(rr[t], a0[t][r], mr[t] * c0[r]);
        sts64(e0 + slot * 512, v2u{pk(y[0], y[1]), pk(y[2], y[3])});
        if (t < 4) {
#pragma unroll
          for (int r = 0; r < 4; ++r) y[r] = __builtin_fmaf(rr[t], a1[t][r], mr[t] * c1[r]);
          sts64(e1 + slot * 512, v2u{pk(y[0], y[1]), pk(y[2], y[3])});
        }
      }
    }
    v4u nring[18], ex[6];
    {
      FRESH();
      const int p = w & 1, qt = w >> 1;
      u32 el = lb + L_E + qt * 512 + p * 5120 + lane * 8, et = lb + L_E + qt * 512 + ((lane & 3) * 16 + 4 * g + (tl >> 2)) * 8, er = lb + L_E + qt * 512 + p * 256 + tl * 8;
      u32 ev = et + p * 5120;
      asm volatile("" : "+v"(el), "+v"(et), "+v"(er), "+v"(ev));
      WST(20);
      bar();
      WST(21);
      STAMP(3 + 16 * layer);
      const v2u qr = lds64(er + 12 * 2560 + 512), kp = lds64(er + 12 * 2560 + 128), kc = lds64(er + 12 * 2560 + 512 + 128);
      v2u k0[2], k1[2], qm[2], v0[2], v1[2];
#pragma unroll
      for (int e = 0; e < 2; ++e) { k0[e] = lds64(el + e * 2560); k1[e] = lds64(el + e * 2560 + 512); qm[e] = lds64(el + (4 + e) * 2560 + 512); }
#pragma unroll
      for (int e = 0; e < 2; ++e) { v0[e] = ldstr(ev + (8 + e) * 2560); v1[e] = ldstr(ev + (8 + e) * 2560 + 512); }
      const v2u r0 = ldstr(et + 13 * 2560), r1 = ldstr(et + 13 * 2560 + 512);
      SB();
#pragma unroll
      for (int i = 0; i < 18; ++i) undef(nring[i]);
#pragma unroll
      for (int i = 0; i < 6; ++i) undef(ex[i]);
      if (layer == 0 && G < 27) {
        const int kt = smul(G, 57) >> 9, cb = G - smul(kt, 9), p0 = smul(kt, 6) + w;
        if (w < 6) {
          const rsrc_t r2 = __builtin_amdgcn_make_buffer_rsrc((void*)w2, 0, 4608 * 1152, 0x00020000);
#pragma unroll
          for (int q = 0; q < 4; ++q) { int so, st; mlp_dest<0>(c, cb, q, so, st); stx(mt[q], rs, v16, so + smul(st, p0)); }
#pragma unroll
          for (int j = 0; j < 4; ++j) mt[j] = ldp(r2, (lane >> 3) * 1152 + (lane & 7) * 16, smul(smul(c, 576) + 32 * p0, 1152) + cb * 128 + j * (8 * 1152));
        } else if (w == 6) {
          f32x4 cs = ldsf4(lb + L_TC + tl * 16);
#pragma unroll
          for (int j = 1; j < 6; ++j) cs += ldsf4(lb + L_TC + j * 256 + tl * 16);
          stx(__builtin_bit_cast(v4u, cs), rs, lane < 16 ? tl * 16 : OOB, CS1_OFF + smul(c, 20736) + smul(kt, 2304) + cb * 256);
        }
      }
      if (w < 6) {
#pragma unroll
        for (int i = 0; i < 6; ++i) ex[i] = ldw(rs, v16 + (i & 3) * 1024, woc + smul(8 + (w & 1), 18432) + smul(w >> 1, 6144) + (i >> 2) * 4096);
      }
      if (layer == 1) fill<0, AUX1>(nring, rs, v16, wm);
      SB();
      const v4u vr = {r0[0], r0[1], r1[0], r1[1]};
      v4u om; v2u orr;
#pragma unroll
      for (int e = 0; e < 2; ++e) {
        const v4u qf = {qm[e][0], qm[e][1], g == 0 ? qr[e] : 0u, 0u};
        const f32x4 s0 = mfma(v4u{k0[e][0], k0[e][1], kp[e], 0u}, qf, m0), s1 = mfma(v4u{k1[e][0], k1[e][1], kc[e], 0u}, qf, m1);
        float mx = __builtin_fmaxf(__builtin_fmaxf(__builtin_fmaxf(s0[0], s0[1]), __builtin_fmaxf(s0[2], s0[3])),
                                   __builtin_fmaxf(__builtin_fmaxf(s1[0], s1[1]), __builtin_fmaxf(s1[2], s1[3])));
        mx = xmax(mx);
        constexpr float sc = 0.23570226039551584f * 1.4426950408889634f;
        const float nb = -mx * sc;
        float pe[8], sum = 0.f;
#pragma unroll
        for (int r = 0; r < 4; ++r) {
          pe[r] = __builtin_amdgcn_exp2f(__builtin_fmaf(s0[r], sc, nb));
          pe[4 + r] = __builtin_amdgcn_exp2f(__builtin_fmaf(s1[r], sc, nb));
          sum += pe[r] + pe[4 + r];
        }
        const v4u pf = {pk(pe[0], pe[1]), pk(pe[2], pe[3]), pk(pe[4], pe[5]), pk(pe[6], pe[7])};
        const f32x4 z = {0.f, 0.f, 0.f, 0.f};
        const f32x4 o = mfma(v4u{v0[e][0], v0[e][1], v1[e][0], v1[e][1]}, pf, z), oq = mfma(vr, pf, z);
        const float inv = __builtin_amdgcn_rcpf(xsum(sum));
        om[2 * e] = pk(o[0] * inv, o[1] * inv); om[2 * e + 1] = pk(o[2] * inv, o[3] * inv);
        orr[e] = pk(oq[2 * e] * inv, oq[2 * e + 1] * inv);
      }
      const int ob = OB_OFF + gofs + qt * 1024;
      stx(om, rs, v16, ob + (2 * c + p) * 4096);
      stx2(orr, rs, g == p ? tl * 8 : OOB, ob + (16 + (c >> 2)) * 4096 + (c & 3) * 256 + p * 128);
      WST(22);
      STAMP(4 + 16 * layer);
      int fs = mbg + (MB_O + c) * 8, ps = mbg + c * 1024 + MB_O * 8, fv = lane * 1024;
      asm volatile("" : "+s"(fs)); asm volatile("" : "+s"(ps)); asm volatile("" : "+v"(fv));
      drain();
      WST(23);
      bar();
      flag(rs, fv, fs, w == 7 && lane < 8, tag);
      WST(24);
      STAMP(5 + 16 * layer);
      if (layer == 0) {
        flag(rs, lane * 8192, MB_OFF + (2 * 256 + c) * 1024 + (G + MB_W1) * 8, w == 6 && lane < 32, tag);
        if (w < 6) { v4u f[4]; tile_tr(mt, f, lb + L_E + w * 4608, lane); mt[0] = f[0]; mt[1] = f[1]; mt[2] = f[2]; mt[3] = f[3]; }
      }
      if (layer == 1) fill<AUX1, AUX2>(nring, rs, v16, wm);
      if (w == 7) { __builtin_amdgcn_s_sleep(PD0); poll<0>(rs, lane, ps, 8, tag); }
    }
    {
      FRESH();
      const int th = c & 1, kh = w >> 2, sw = (c >> 1) & 1, u1 = ub + (w & 3);
      int ob = OB_OFF + gofs + th * 2048 + (w >> 1) * 4096 + (w & 1) * 1024;
      u32 lo = lb + L_OI + w * 1024 + v16, l4 = lb + L_OI + (32 + w) * 1024 + (g * 16 + 2 * (lane & 7)) * 16 + ((lane >> 3) & 1) * 8;
      u32 x0 = lb + L_OI + v16 + kh * (9 * 2048 + 1024), x1 = lb + L_OI + v16 + kh * (9 * 2048) + (kh ^ 1) * 1024;
      u32 xe = lb + L_OI + v16 + sw * 1024 + (w < 6 ? smul(w >> 1, 6 * 2048) : 0);
      const v4u xr = lds128(lb + smul(u1, 5120) + (2 * th + kh) * 1024 + v16s);
      asm volatile("" : "+s"(ob)); asm volatile("" : "+v"(lo), "+v"(l4), "+v"(x0), "+v"(x1), "+v"(xe));
      bar();
      WST(25);
      STAMP(6 + 16 * layer);
      v4u of[5];
#pragma unroll
      for (int i = 0; i < 4; ++i) of[i] = ldx(rs, v16, ob + i * 16384);
      undef(of[4]);
      if (ra) of[4] = ldx(rs, v16, ob + 65536);
      SB();
#pragma unroll
      for (int i = 0; i < 4; ++i) sts128(lo + i * 8192, of[i]);
      if (ra) { sts64(l4, v2u{of[4][0], of[4][1]}); sts64(l4 + 16, v2u{of[4][2], of[4][3]}); }
      bar();
      WST(26);
      STAMP(7 + 16 * layer);
      if (layer == 0 && G < 27 && w < 6) {
        const int kt = smul(G, 57) >> 9, cb = G - smul(kt, 9), p0 = smul(kt, 6) + w;
#pragma unroll
        for (int q = 0; q < 4; ++q) { int so, st; mlp_dest<1>(c, cb, q, so, st); stx(mt[q], rs, v16, so + smul(st, p0)); }
      }
      if (layer == 1) fill<AUX2, AUX3>(nring, rs, v16, wm);
      f32x4 a[5];
      groupo(a, ring, ex, x0, x1, xe);
      WST(27);
      *(LAS f32x4*)(size_t)(lb + L_PX + w * 2048 + v16) = a[1];
      *(LAS f32x4*)(size_t)(lb + L_PX + w * 2048 + 1024 + v16) = a[3];
      if (w < 6) *(LAS f32x4*)(size_t)(lb + smul(w, 5120) + 4096 + v16) = a[4];
      a[0] += f32x4{lo2f(xr[0]), hi2f(xr[0]), lo2f(xr[1]), hi2f(xr[1])};
      a[2] += f32x4{lo2f(xr[2]), hi2f(xr[2]), lo2f(xr[3]), hi2f(xr[3])};
      int xb = X1_OFF + gofs + u1 * 4096 + (2 * th + kh) * 1024, fs = mbg + (MB_X1 + c) * 8, ps = mbg + c * 1024 + MB_X1 * 8, fv = lane * 1024;
      u32 pa = lb + L_PX + (w ^ 4) * 2048 + v16;
      asm volatile("" : "+s"(xb)); asm volatile("" : "+s"(fs)); asm volatile("" : "+s"(ps)); asm volatile("" : "+v"(fv), "+v"(pa), "+v"(a[0]), "+v"(a[2]));
      bar();
      stx(pk8(a[0] + ldsf4(pa), a[2] + ldsf4(pa + 1024)), rs, v16, xb);
      if (w == 6) {
        const v4u xv = lds128(lb + smul(us, 5120) + (2 * th + sw) * 1024 + v16s);
        const u32 pb = lb + 4096 + v16;
        const f32x4 e0 = ldsf4(pb) + ldsf4(pb + 10240) + ldsf4(pb + 20480) + f32x4{lo2f(xv[0]), hi2f(xv[0]), lo2f(xv[1]), hi2f(xv[1])};
        const f32x4 e1 = ldsf4(pb + 5120) + ldsf4(pb + 15360) + ldsf4(pb + 25600) + f32x4{lo2f(xv[2]), hi2f(xv[2]), lo2f(xv[3]), hi2f(xv[3])};
        stx(pk8(e0, e1), rs, v16, X1_OFF + gofs + us * 4096 + (2 * th + sw) * 1024);
      }
      WST(28);
      STAMP(8 + 16 * layer);
      if (layer == 0 && w == 7) poll<0>(rs, lane, MB_OFF + (2 * 256 + G * 8 + c) * 1024 + MB_W1 * 8, 32, tag);
      drain();
      WST(29);
      bar();
      flag(rs, fv, fs, w == 7 && lane < 8, tag);
      WST(30);
      if (layer == 0) {
        flag(rs, lane * 8192, MB_OFF + (2 * 256 + c) * 1024 + (G + MB_W2) * 8, w == 6 && lane < 32, tag);
        fill<0, AUX3>(nring, rs, v16, wm);
      }
      fill<AUX3, 18>(nring, rs, v16, wm);
      if (layer == 0 && w < 6) {
#pragma unroll
        for (int j = 0; j < 2; ++j) {
          if (j == 0 || lane < 32) {
            const int n = w * 96 + lane + 64 * j;
            float cs = 0.f;
#pragma unroll
            for (int k = 0; k < 3; ++k) cs += __builtin_bit_cast(float, __builtin_amdgcn_raw_buffer_load_b32(rs, n * 4, CS1_OFF + smul(c, 20736) + k * 2304, 16));
            u32 h, m, l;
            split3(cs, h, m, l);
            sts64(lb + L_TA + 8 * n, v2u{h | (m << 16), b1[576 * c + colp(n >> 5, (n >> 4) & 1, n & 15)] | (l << 16)});
          }
        }
      }
      if (w == 7) { __builtin_amdgcn_s_sleep(PD0); poll<0>(rs, lane, ps, 8, tag); }
    }
#pragma unroll
    for (int i = 0; i < 18; ++i) ring[i] = nring[i];
    {
      FRESH();
      const int tt = w & 3;
      int sm = X1_OFF + gofs + tt * 1024;
      u32 la = lb + tt * 1024 + v16, lc = la + CH1 * 4096;
      asm volatile("" : "+s"(sm)); asm volatile("" : "+v"(la), "+v"(lc));
      bar();
      WST(31);
      STAMP(9 + 16 * layer);
      if (ra) {
        v4u xm[CH1];
#pragma unroll
        for (int i = 0; i < CH1; ++i) xm[i] = ldx(rs, v16, sm + i * 4096);
        SB();
#pragma unroll
        for (int i = 0; i < CH1; ++i) sts128(la + i * 4096, xm[i]);
        lds_arrive(lb + L_CNT, lane);
        lds_wait(lb + L_CNT, 8 * layer + 8);
      }
      int wb = w;
      asm volatile("" : "+s"(wb));
      if (wb >= 4) {
        __builtin_amdgcn_s_sleep(STSL);
        v4u xm[18 - CH1], ym[CH1];
#pragma unroll
        for (int i = 0; i < 18 - CH1; ++i) xm[i] = ldx(rs, v16, sm + (CH1 + i) * 4096);
        SB();
#pragma unroll
        for (int i = 0; i < 18 - CH1; ++i) sts128(lc + i * 4096, xm[i]);
        lds_arrive(lb + L_CNT + 40, lane);
        lds_wait(lb + L_CNT, 8 * layer + 8);
        __builtin_amdgcn_s_setprio(3);
#pragma unroll
        for (int i = 0; i < CH1; ++i) ym[i] = lds128(la + i * 4096);
        f32x4 s = {0.f, 0.f, 0.f, 0.f}, q = s;
#pragma unroll
        for (int i = 0; i < 18 - CH1; ++i) stat_acc(xm[i], s, q);
#pragma unroll
        for (int i = 0; i < CH1; ++i) stat_acc(ym[i], s, q);
        const float m = s[0] * (1.f / 576.f), var = __builtin_fmaxf(__builtin_fmaf(diag(q, g, tl), 1.f / 576.f, -m * m), 0.f);
        const float r1 = __builtin_amdgcn_rsqf(var + 1e-5f), r = r1 * __builtin_amdgcn_rsqf(var * r1 * r1 + 1e-5f), ri = __builtin_amdgcn_rcpf(r);
        constexpr float k1 = -2.f * 0.7978845608028654f * 1.4426950408889634f, k2 = k1 * 0.044715f;
        u32 mh, mm_, ml, rh, rm, rl;
        split3(-m, mh, mm_, ml); split3(ri, rh, rm, rl);
        const u32 mv = g == 0 ? mh : g == 1 ? mm_ : g == 2 ? ml : 0u, rv = g == 0 ? rh : g == 1 ? rm : g == 2 ? rl : 0u;
        sts64(lb + L_RK + tt * 512 + lane * 8, v2u{mv | (mv << 16), rv | (mv << 16)});
        if (lane < 16) *(LAS f32x4*)(size_t)(lb + L_SS + tt * 256 + tl * 16) = f32x4{k1 * r, k2 * r * r * r, ri, 0.f};
        __builtin_amdgcn_s_setprio(0);
        if (layer == 0 && w == 7) poll<0>(rs, lane, MB_OFF + (2 * 256 + G * 8 + c) * 1024 + MB_W2 * 8, 32, tag);
        lds_arrive(lb + L_CNT + 4, lane);
      }
    }
    STAMP(10 + 16 * layer);
    {
      FRESH();
      const int i = w & 3, ot = c >> 1, hb = c & 1;
      const int pb = PB_OFF + gofs * 8 + smul((i >> 1) * 8 + c, 9216);
      const int town = (i >> 1) == hb ? ot : -1;
      const u32 xa = lb + v16, ha = lb + L_H + v16, lo = xa + (ot ^ 1) * 1024;
      u32 ta = lb + L_TA + tl * 8, ka = lb + L_SS + tl * 16, ga = lb + L_RK + lane * 8;
      asm volatile("" : "+v"(ta), "+v"(ka), "+v"(ga));
#define LOADRK() _Pragma("unroll") for (int t = 0; t < 4; ++t) { rk.aug[t] = lds64(ga + t * 512); rk.k[t] = ldsf4(ka + t * 256); }
      int sn = wm + 18432;
      WST(0);
#pragma unroll 1
      for (int gi = 0; gi < (ra ? 2 : 1); ++gi) {
        const int u = ra ? 8 * gi + i : 4 + i;
        f32x4 a[2][4];
        RowK rk;
        v2u t0, t1;
        group<2, CH1, 1>(a, ring, xa, rs, v16, sn, sn + 18432, [&]() INL { if (gi == 0) lds_wait(lb + L_CNT + 40, 8 * layer + 8); }, [&]() INL {
          if (gi == 0) lds_wait(lb + L_CNT + 4, 8 * layer + 8);
          t0 = lds64(ta + u * 256); t1 = lds64(ta + u * 256 + 128);
          LOADRK();
        });
        sn += 2 * 18432;
        __builtin_amdgcn_s_setprio(3);
        aug_tile(a[0], t0, rk); aug_tile(a[1], t1, rk);
        __builtin_amdgcn_s_setprio(0);
        mm_end();
        WST(1 + 2 * gi);
        gelu_pair(a[0], a[1], ha + u * 4096, rk);
        WST(2 + 2 * gi);
      }
      if (ra) {
        f32x4 a[5][4];
        bar();
        STAMP(11 + 16 * layer);
        WST(8);
        group<5, 12, 0>(a, ring, ha, rs, v16, sn, 0, []() INL { bar(); }, []() INL {});
        mm_end();
        WST(11);
        __builtin_amdgcn_s_setprio(APRIO);
        const u32 ls = xa + (ot ^ 2) * 1024 + (i >> 1) * 16384;
#pragma unroll
        for (int t = 0; t < 4; ++t) sts64((t == town ? lo + (16 + hb) * 4096 : ls + t * 4096) + (i & 1) * 8, v2u{pk(a[4][t][0], a[4][t][1]), pk(a[4][t][2], a[4][t][3])});
        lds_arrive(lb + L_CNT + 48 + (i >> 1) * 4, lane);
        part_pair(a[0], a[1], rs, v16, pb + ((4 * i) & 7) * 1024, town, lo + (4 * i) * 4096);
        part_pair(a[2], a[3], rs, v16, pb + ((4 * i + 1) & 7) * 1024, town, lo + (4 * i + 1) * 4096);
        lds_wait(lb + L_CNT + 48 + (i >> 1) * 4, 2 * layer + 2);
#pragma unroll
        for (int k = 0; k < 2; ++k) {
          const int t = 2 * (i & 1) + k;
          if (t != town) stx(lds128(ls + t * 4096), rs, v16, pb + 8192 + smul(t, 2 * 8 * 9216));
        }
      }
      int wb = w;
      asm volatile("" : "+s"(wb));
      if (wb >= 4) {
        v4u aux[18];
        {
          f32x4 a[3][4];
          RowK rk;
          v2u t0, t1, t2;
          group<3, 99, 1>(a, ring, xa, rs, v16, sn, sn + 2 * 18432, []() INL {}, [&]() INL {
            t0 = lds64(ta + (12 + i) * 256); t1 = lds64(ta + (12 + i) * 256 + 128); t2 = lds64(ta + (16 + (i >> 1)) * 256 + (i & 1) * 128);
            LOADRK();
          });
          sn += 3 * 18432;
          aug_tile(a[0], t0, rk); aug_tile(a[1], t1, rk); aug_tile(a[2], t2, rk);
          mm_end();
          WST(7);
          bar();
          WST(8);
          gelu_pair(a[0], a[1], ha + (12 + i) * 4096, rk);
#pragma unroll
          for (int t = 0; t < 4; ++t) sts64(ha + (16 + (i >> 1)) * 4096 + t * 1024 + (i & 1) * 8, gelu4(a[2][t], rk.k[t]));
          WST(9);
          bar();
          WST(10);
        }
        fill<0, 18>(aux, rs, v16, sn);
        f32x4 a[2][4];
        group36<true>(a, ring, aux, ha, rs, v16, sn + 18432, []() INL {});
        WST(3);
#pragma unroll
        for (int t = 0; t < 4; ++t) sts128(t == town ? lo + (4 * i + 2) * 4096 : xa + (ot ^ 3) * 1024 + (4 * i + t) * 4096, pk8(a[0][t], a[1][t]));
        lds_arrive(lb + L_CNT + 64 + 4 * i, lane);
        WST(4);
        group36<false>(a, ring, aux, ha, rs, v16, 0, []() INL {});
        WST(5);
        part_pair(a[0], a[1], rs, v16, pb + ((4 * i + 3) & 7) * 1024, town, lo + (4 * i + 3) * 4096);
      }
      WST(12);
      STAMP(12 + 16 * layer);
    }
    {
      FRESH();
      const int tt = c >> 1, hb = c & 1;
      int po[7];
#pragma unroll
      for (int j = 0; j < 7; ++j) { po[j] = PB_OFF + gofs * 8 + smul(c * 8 + ((c + 1 + j) & 7), 9216); asm volatile("" : "+s"(po[j])); }
      int vo = layer == 0 ? v16 : (rowbase + 2 * (16 * tt + tl)) * 1152 + 16 * g;
      v4u sel[2];
      make_sel(sel, g, tl);
      int slot = (0x62854100u >> (4 * w)) & 15, vs = v16 + slot * 1024, fv = lane * 2048;
      int fs = mbg + ((w >> 1) & 1) * 1024 + ((ra ? MB_A : MB_B) + 2 * c + (w & 1)) * 8;
      asm volatile("" : "+s"(fs)); asm volatile("" : "+s"(slot));
      asm volatile("" : "+v"(vo), "+v"(vs), "+v"(fv), "+v"(sel[0]), "+v"(sel[1]));
      drain();
      flag(rs, fv, fs, lane < 4, tag);
      __builtin_amdgcn_s_setprio(0);
      if (!ra) lds_arrive(lb + L_CNT + 44, lane);
      WST(13);
      if (ra) {
        const int pb = PB_OFF + gofs * 8 + smul((w >> 1) * 8 + c, 9216) + ((4 * w + 2) & 7) * 1024, town = (w >> 1) == hb ? tt : -1;
        lds_waits(lb + L_CNT + 64 + 4 * w, layer + 1);
#pragma unroll
        for (int t = 0; t < 4; ++t)
          if (t != town) stx(lds128(lb + v16 + (tt ^ 3) * 1024 + (4 * w + t) * 4096), rs, v16, pb + t * (2 * 8 * 9216));
        WST(5);
        if (w == 0) { poll<2>(rs, lane, mbg + c * 1024 + MB_A * 8, 16, tag); lds_arrive(lb + L_CNT + 24, lane); }
        drain();
        flag(rs, lane * 2048, mbg + (w >> 1) * 1024 + (MB_B + 16 + 2 * c + (w & 1)) * 8, lane < 4, tag);
        WST(7);
        if (w == 0) {
          __builtin_amdgcn_s_sleep(PS1);
          poll<4>(rs, lane, mbg + c * 1024 + (MB_B + 16) * 8, 16, tag);
          lds_arrive(lb + L_CNT + 24, lane);
          WST(9);
          lds_waits(lb + L_CNT + 44, 4 * layer + 4);
          __builtin_amdgcn_s_sleep(PSL);
          poll<1>(rs, lane, mbg + c * 1024 + MB_B * 8, 16, tag);
          lds_arrive(lb + L_CNT + 24, lane);
        }
      }
#pragma unroll 1
      for (int rep = ra ? 0 : 1; rep < 2; ++rep) {
        if (ra && rep == 1) { slot = 3 + 4 * w; vs = v16 + slot * 1024; }
        const int u = slot == 8 ? 16 + hb : 8 * hb + slot;
        const bool on = !ra || (rep == 0 ? w > 0 : w < 2);
        v4u pv[7];
#pragma unroll
        for (int j = 0; j < 7; ++j) undef(pv[j]);
        if (on) {
          if (!ra || (NOEARLY && rep == 0)) lds_wait(lb + L_CNT + 44, 4 * layer + 4);
          lds_waits(lb + L_CNT + 24, 3 * layer + (ra ? 1 + 2 * rep : w < 6 ? 1 : 2));
          if (rep == 1) { WST(14); STAMP(13 + 16 * layer); } else WST(6);
#pragma unroll
          for (int j = 0; j < 7; ++j) pv[j] = ldx(rs, vs, po[j]);
        }
        SB();
        if (on) {
          const v4u xv = lds128(lb + u * 4096 + tt * 1024 + v16), ov = lds128(lb + u * 4096 + (tt ^ 1) * 1024 + v16);
          f32x4 ya = mfma(sel[0], ov, mfma(sel[0], xv, ldsf4(lb + L_TB2 + u * 128 + g * 32)));
          f32x4 yb = mfma(sel[1], ov, mfma(sel[1], xv, ldsf4(lb + L_TB2 + u * 128 + g * 32 + 16)));
#pragma unroll
          for (int j = 0; j < 7; ++j) { ya = mfma(sel[0], pv[j], ya); yb = mfma(sel[1], pv[j], yb); }
          if (HOLD && w != 1) lds_waits(lb + L_CNT + 24, 3 * layer + 3);
          if (layer == 0) stx(pk8(ya, yb), rs, vo, X2_OFF + gofs + u * 4096 + tt * 1024);
          else st128<0>(pk8(ya, yb), ro, vo, 64 * u);
        }
      }
      WST(15);
      if (layer == 0) {
        STAMP(14);
        drain();
        const bool nxt = c >= 6 && (G & 15) != 15;
        if (lds_ticket(lb + L_CNT + 56, lane) == 7) flag(rs, lane * 1024 + (lane & 8) * 2, mbg + (MB_X2 + c) * 8, lane < 8 || (nxt && lane < 16), tag);
      }
      WST(16);
    }
    STAMP(15 + 16 * layer);
    if (layer == 0) {
      FRESH();
      static_assert(CH1 == 6, "halo split");
      const int tt = w & 3;
      if (FW) lds_waits(lb + L_CNT + 56, 8);
      fill<0, 18>(ring, rs, v16, wq);
      int sm = X2_OFF + gofs + tt * 1024, sh = X2_OFF - 73728 + gofs + 3072 + tt * 4096;
      u32 la = lb + tt * 1024 + v16s + (ra ? 0 : 6 * 5120), lh = lb + 4096 + smul(tt + (ra ? 0 : 6), 5120) + v16s;
      asm volatile("" : "+s"(sm)); asm volatile("" : "+s"(sh)); asm volatile("" : "+v"(la), "+v"(lh));
      if (w == 7) poll<1>(rs, lane, mbg + c * 1024 + MB_X2 * 8, first ? 8 : 10, tag);
      bar();
      WST(17);
      STAMP(16);
      v4u xh[3];
#pragma unroll
      for (int i = 0; i < 3; ++i) xh[i] = v4u{0u, 0u, 0u, 0u};
      if (ra) {
        v4u xm[6];
#pragma unroll
        for (int i = 0; i < 6; ++i) xm[i] = ldx(rs, v16, sm + i * 4096);
        if (!first) { xh[0] = ldx(rs, v16, sh); if (tt < 2) xh[1] = ldx(rs, v16, sh + 16384); }
        SB();
#pragma unroll
        for (int i = 0; i < 6; ++i) sts128(la + i * 5120, xm[i]);
        sts128(lh, xh[0]);
        if (tt < 2) sts128(lh + 20480, xh[1]);
        lds_arrive(lb + L_CNT, lane);
      }
      int wb = w;
      asm volatile("" : "+s"(wb));
      if (wb >= 4) {
        __builtin_amdgcn_s_sleep(STSL);
        v4u xm[12];
#pragma unroll
        for (int i = 0; i < 12; ++i) xm[i] = ldx(rs, v16, sm + (6 + i) * 4096);
        if (!first) {
#pragma unroll
          for (int i = 0; i < 3; ++i) xh[i] = ldx(rs, v16, sh + (6 + 4 * i) * 4096);
        }
        SB();
#pragma unroll
        for (int i = 0; i < 12; ++i) sts128(la + i * 5120, xm[i]);
#pragma unroll
        for (int i = 0; i < 3; ++i) sts128(lh + i * 20480, xh[i]);
        lds_arrive(lb + L_CNT + 40, lane);
      }
    }
#pragma unroll
    for (int j = 0; j < 4; ++j) undef(mt[j]);
  }
  if (bid == 0 && tid == 448) stx2(tag, rs, 0, EP_OFF);
}

extern "C" void kernel_launch(void* const* d_in, const int* in_sizes, int n_in, void* d_out, int out_size, void* d_ws, size_t ws_size,
                              hipStream_t stream) {
  const u16* x = (const u16*)d_in[0];
  const u16* wqkv = (const u16*)d_in[1];
  const u16* wo = (const u16*)d_in[2];
  const u16* w1 = (const u16*)d_in[4];
  const u16* b1 = (const u16*)d_in[5];
  const u16* w2 = (const u16*)d_in[6];
  const u16* b2 = (const u16*)d_in[7];
  (void)hipFuncSetAttribute((const void*)k_main, hipFuncAttributeMaxDynamicSharedMemorySize, L_TOTAL);
  k_main<<<256, 512, L_TOTAL, stream>>>(x, wqkv, wo, w1, b1, w2, b2, (u16*)d_out, (char*)d_ws + ((size_t)190 << 20));
}
```

```cpp
#define PROBE 0
#include <hip/hip_runtime.h>
#include <hip/hip_bf16.h>

typedef unsigned short u16;
typedef unsigned u32;
typedef unsigned long long u64;
typedef u32 v4u __attribute__((ext_vector_type(4)));
typedef u32 v2u __attribute__((ext_vector_type(2)));
typedef float f32x4 __attribute__((ext_vector_type(4)));
typedef float f32x2 __attribute__((ext_vector_type(2)));
typedef short s16x4 __attribute__((ext_vector_type(4)));
typedef __bf16 bf16x2 __attribute__((ext_vector_type(2)));
typedef __bf16 bf16x8 __attribute__((ext_vector_type(8)));
#define GAS __attribute__((address_space(1)))
#define LAS __attribute__((address_space(3)))
#define DI __device__ __forceinline__
#define RLX __ATOMIC_RELAXED, __HIP_MEMORY_SCOPE_AGENT

#define XG 2
#define AUX1 6
#define AUX2 6
#define AUX3 12
#define BF1 12
#define PSL 6
#define BSL 24
#define B1LATE 1
#define HOLD 0
#define FW 1
#define APRIO 3
#define APOLL 1
#define PD0 0
#define MBW 1
#define PS1 8
#define NOEARLY 0
#define CH1 6
#define SIGK 14
#define STSL 3
#define STAMPS 0

#define CODE_BYTES 55296
constexpr int EP_OFF = 0;
constexpr int MB_OFF = 4096, MB_BYTES = 3 * 256 * 1024;
constexpr int MB_O = 0, MB_X1 = 16, MB_A = 32, MB_B = 48, MB_X2 = 80, MB_WQ = 0, MB_W1 = 32, MB_W2 = 64;
constexpr int CSQ_OFF = MB_OFF + MB_BYTES;
constexpr int CS1_OFF = CSQ_OFF + 8 * 18432;
constexpr int WQ_OFF = CS1_OFF + 8 * 20736;
constexpr int WO_OFF = WQ_OFF + 64 * 36864;
constexpr int WM_OFF = WO_OFF + 80 * 18432;
constexpr int OB_OFF = WM_OFF + 64 * 165888;
constexpr int X1_OFF = OB_OFF + 64 * 73728;
constexpr int X2_OFF = X1_OFF + 64 * 73728;
constexpr int PB_OFF = X2_OFF + 32 * 73728;
constexpr int ST_OFF = PB_OFF + 2 * 32 * 64 * 9216;
constexpr int WS_USED = ST_OFF + 2 * 256 * 32 * 8 + 256 * 2048;
constexpr int WS_DUMP = WS_USED;
constexpr int L_E = 92160, L_OI = 92160, L_H = 73728;
constexpr int L_TQ = 92160, L_TC = L_TQ + 18432 + 6 * 4608;
constexpr int L_PX = 129024;
constexpr int L_SS = 147456;
constexpr int L_RK = L_SS + 1024;
constexpr int L_CNT = L_RK + 2048;
constexpr int L_TA = L_CNT + 96;
constexpr int L_TB2 = L_TA + 4608;
constexpr int L_TCQ = L_TB2 + 2304;
constexpr int L_DBG = L_TCQ + 1024;
constexpr int L_TOTAL = L_DBG + (STAMPS ? 2048 : 0);

DI float bf2f(u16 b) { return __builtin_bit_cast(float, (u32)((u32)b) << 16); }
DI float lo2f(u32 x) { return __builtin_bit_cast(float, (u32)x << 16); }
DI float hi2f(u32 x) { return __builtin_bit_cast(float, (u32)x & 0xffff0000u); }
DI u32 pk(float a, float b) { f32x2 f = {a, b}; return __builtin_bit_cast(u32, __builtin_convertvector(f, bf16x2)); }
DI f32x4 mfma(v4u a, v4u b, f32x4 c) {
  return __builtin_amdgcn_mfma_f32_16x16x32_bf16(__builtin_bit_cast(bf16x8, a), __builtin_bit_cast(bf16x8, b), c, 0, 0, 0);
}
DI void mm(f32x4& c, v4u a, v4u b) { asm volatile("v_mfma_f32_16x16x32_bf16 %0, %1, %2, %0" : "+v"(c) : "v"(a), "v"(b)); }
DI void mm0(f32x4& c, v4u a, v4u b) { asm volatile("v_mfma_f32_16x16x32_bf16 %0, %1, %2, 0" : "=&v"(c) : "v"(a), "v"(b)); }
DI void mmk16(f32x4& c, v2u a, v2u b) { asm volatile("v_mfma_f32_16x16x16_bf16 %0, %1, %2, %0" : "+v"(c) : "v"(a), "v"(b)); }
DI void mm_end() { __builtin_amdgcn_sched_barrier(0); asm volatile("s_nop 7\n\ts_nop 1" ::: "memory"); __builtin_amdgcn_sched_barrier(0); }
DI float xsum(float v) {
  u32 x = __builtin_bit_cast(u32, v);
  auto a = __builtin_amdgcn_permlane16_swap(x, x, false, false);
  v = __builtin_bit_cast(float, (u32)a[0]) + __builtin_bit_cast(float, (u32)a[1]);
  x = __builtin_bit_cast(u32, v);
  auto b = __builtin_amdgcn_permlane32_swap(x, x, false, false);
  return __builtin_bit_cast(float, (u32)b[0]) + __builtin_bit_cast(float, (u32)b[1]);
}
DI float xmax(float v) {
  u32 x = __builtin_bit_cast(u32, v);
  auto a = __builtin_amdgcn_permlane16_swap(x, x, false, false);
  v = __builtin_fmaxf(__builtin_bit_cast(float, (u32)a[0]), __builtin_bit_cast(float, (u32)a[1]));
  x = __builtin_bit_cast(u32, v);
  auto b = __builtin_amdgcn_permlane32_swap(x, x, false, false);
  return __builtin_fmaxf(__builtin_bit_cast(float, (u32)b[0]), __builtin_bit_cast(float, (u32)b[1]));
}
#define SB() __builtin_amdgcn_sched_barrier(0)
#define INL __attribute__((always_inline))
DI int smul(int a, int k) { int r; asm("s_mul_i32 %0, %1, %2" : "=s"(r) : "s"(a), "s"(k)); return r; }
DI void bar() {
  asm volatile("s_waitcnt lgkmcnt(0)" ::: "memory");
  __builtin_amdgcn_s_barrier();
  asm volatile("" ::: "memory");
}
typedef __amdgpu_buffer_rsrc_t rsrc_t;
#define WAUX 16
DI v4u ldw(rsrc_t rs, int v, int s) { return __builtin_amdgcn_raw_buffer_load_b128(rs, v, s, WAUX); }
DI v4u ldp(rsrc_t rs, int v, int s) { return __builtin_amdgcn_raw_buffer_load_b128(rs, v, s, 0); }
constexpr int OOB = 0x70000000;
DI v4u ldx(rsrc_t rs, int v, int s) { return __builtin_amdgcn_raw_buffer_load_b128(rs, v, s, 16); }
DI v2u ldx2(rsrc_t rs, int v, int s) { return __builtin_amdgcn_raw_buffer_load_b64(rs, v, s, 16); }
template <int AUX> DI void st128(v4u d, rsrc_t rs, int v, int s) { __builtin_amdgcn_raw_buffer_store_b128(d, rs, v, s, AUX); asm volatile("s_nop 1" :: "v"(d)); }
DI void stx(v4u d, rsrc_t rs, int v, int s) { st128<16>(d, rs, v, s); }
DI void stx2(v2u d, rsrc_t rs, int v, int s) { __builtin_amdgcn_raw_buffer_store_b64(d, rs, v, s, 16); }
DI v4u lds128(u32 a) { return *(const LAS v4u*)(size_t)a; }
DI v2u lds64(u32 a) { return *(const LAS v2u*)(size_t)a; }
DI f32x4 ldsf4(u32 a) { return *(const LAS f32x4*)(size_t)a; }
DI f32x2 ldsf2(u32 a) { return *(const LAS f32x2*)(size_t)a; }
DI void sts128(u32 a, v4u v) { *(LAS v4u*)(size_t)a = v; }
DI void sts64(u32 a, v2u v) { *(LAS v2u*)(size_t)a = v; }
DI v2u ldstr(u32 a) { return __builtin_bit_cast(v2u, __builtin_amdgcn_ds_read_tr16_b64_v4i16((LAS s16x4*)(size_t)a)); }

__host__ __device__ __forceinline__ int colq(int c, int w, int t, int m) {
  if (w < 4 || t == 0) {
    const int h = w & 3, sec = w < 4 ? (t == 0 ? 1 : 0) : 2;
    return sec * 576 + 72 * c + 36 * (h >> 1) + 20 * (h & 1) + m;
  }
  const int qd = m >> 2;
  if (w < 6) return (qd & 1) * 576 + 72 * c + 36 * (qd >> 1) + 16 + (m & 3);
  return 2 * 576 + 72 * c + 36 * (qd & 1) + 16 + (m & 3);
}
__host__ __device__ __forceinline__ int krot(int c) { return 0; }
__host__ __device__ __forceinline__ int colp(int u, int e, int m) { return 32 * u + 8 * (m >> 2) + 4 * e + (m & 3); }
__host__ __device__ __forceinline__ int rowo(int ks, int g, int j) {
  if (ks < 16) return 72 * (ks >> 1) + 36 * (ks & 1) + 20 * (j >> 2) + 4 * g + (j & 3);
  return 72 * (4 * (ks - 16) + g) + 36 * (j >> 2) + 16 + (j & 3);
}
DI void lds_arrive(u32 a, int lane) {
  asm volatile("s_waitcnt lgkmcnt(0)" ::: "memory");
  if (lane == 0) __hip_atomic_fetch_add((LAS u32*)(size_t)a, 1u, __ATOMIC_RELAXED, __HIP_MEMORY_SCOPE_WORKGROUP);
}
DI void lds_waits(u32 a, u32 n) {
  while ((u32)__builtin_amdgcn_readfirstlane(*(volatile LAS u32*)(size_t)a) < n) __builtin_amdgcn_s_sleep(2);
  asm volatile("" ::: "memory");
}
DI void lds_wait(u32 a, u32 n) {
  while ((u32)__builtin_amdgcn_readfirstlane(*(volatile LAS u32*)(size_t)a) < n) {}
  asm volatile("" ::: "memory");
}
#define REFILL(f_, NU) do { const int q_ = (f_) % 18, u_ = (f_) / 18; \
    if (u_ < (NU) - 1) ring[q_] = ldw(rs, v16 + (q_ & 3) * 1024, s0 + u_ * 18432 + (q_ >> 2) * 4096); \
    else if (LAST == 1) ring[q_] = ldw(rs, v16 + (q_ & 3) * 1024, slast + (q_ >> 2) * 4096); } while (0)
template <int NT, int MIDBAR, int LAST, class MID, class PRE>
DI void group(f32x4 (&acc)[NT][4], v4u (&ring)[18], u32 xa, rsrc_t rs, int v16, int s0, int slast, MID&& mid, PRE&& pre) {
  v4u xf[2][4];
  u32 xb = xa + 9 * 4096;
  asm volatile("" : "+v"(xa), "+v"(xb));
#pragma unroll
  for (int t = 0; t < 4; ++t) xf[0][t] = lds128(xa + t * 1024);
  SB();
#pragma unroll
  for (int ks = 0; ks < 18; ++ks) {
    const int kn = ks + 1;
#pragma unroll
    for (int n = 0; n < NT; ++n) {
      const int f = ks * NT + n;
#pragma unroll
      for (int t = 0; t < 4; ++t) {
        if (ks == 0) mm0(acc[n][t], ring[f % 18], xf[0][t]); else mm(acc[n][t], ring[f % 18], xf[ks & 1][t]);
        if (t == 0 && f > 0) REFILL(f - 1, NT);
        const int n0 = kn == MIDBAR ? NT - 2 : 0;
        const int q = n == n0 ? t - 1 : (n == n0 + 1 && t == 1 ? 3 : -1);
        if (q == 0 && kn == MIDBAR) { mid(); SB(); }
        if (q >= 0 && kn < 18) xf[kn & 1][q] = kn < 9 ? lds128(xa + kn * 4096 + q * 1024) : lds128(xb + (kn - 9) * 4096 + q * 1024);
        if (kn == 18 && n == 0 && t == 1) pre();
        SB();
      }
    }
  }
  REFILL(18 * NT - 1, NT);
  SB();
}
template <bool REF, class MID>
DI void group36(f32x4 (&acc)[2][4], v4u (&r0)[18], v4u (&r1)[18], u32 xa, rsrc_t rs, int v16, int sn, MID&& mid) {
  v4u xf[2][4];
  u32 xb = xa + 9 * 4096;
  asm volatile("" : "+v"(xa), "+v"(xb));
#pragma unroll
  for (int t = 0; t < 4; ++t) xf[0][t] = lds128(xa + t * 1024);
  SB();
#pragma unroll
  for (int f = 0; f < 36; ++f) {
    const int ks = f >> 1, n = f & 1, kn = ks + 1;
#pragma unroll
    for (int t = 0; t < 4; ++t) {
      if (ks == 0) mm0(acc[n][t], r0[f], xf[0][t]); else mm(acc[n][t], f < 18 ? r0[f % 18] : r1[f % 18], xf[ks & 1][t]);
      if (REF && t == 0 && f > 0) (f - 1 < 18 ? r0[(f - 1) % 18] : r1[(f - 1) % 18]) = ldw(rs, v16 + ((f - 1) & 3) * 1024, sn + ((f - 1) >> 2) * 4096);
      const int q = n == 0 ? t - 1 : (t == 1 ? 3 : -1);
      if (q >= 0 && kn < 18) xf[kn & 1][q] = kn < 9 ? lds128(xa + kn * 4096 + q * 1024) : lds128(xb + (kn - 9) * 4096 + q * 1024);
      if (f == 2 * SIGK + 1 && t == 2) mid();
      SB();
    }
  }
  mm_end();
  if (REF) r1[17] = ldw(rs, v16 + 3072, sn + 8 * 4096);
  SB();
}
DI void groupq(f32x4 (&a0)[5], f32x4 (&a1)[4], v4u (&ring)[18], u32 x0, u32 x1, u32 x4, rsrc_t rs, int v16, int s0, int slast, u32 cnt, u32 want) {
  constexpr int LAST = 1;
  v4u xf[2][5];
  u32 y0 = x0 + 46080, y1 = x1 + 46080, y4 = x4 + 46080;
  asm volatile("" : "+v"(y0), "+v"(y1), "+v"(y4));
  xf[0][0] = lds128(x0); xf[0][1] = lds128(x1); xf[0][2] = lds128(x1 + 1024); xf[0][3] = lds128(x1 + 2048); xf[0][4] = lds128(x4);
  SB();
#pragma unroll
  for (int ks = 0; ks < 18; ++ks) {
    const int sl = (2 * ks) % 18, kn = ks + 1, o = (kn % 9) * 5120;
#pragma unroll
    for (int t = 0; t < 5; ++t) {
      if (ks == 0) mm0(a0[t], ring[sl], xf[0][t]); else mm(a0[t], ring[sl], xf[ks & 1][t]);
      if (t == 0 && ks > 0) REFILL(2 * ks - 1, 2);
      if (t == 1 && kn == CH1) { lds_wait(cnt, want); SB(); }
      if (t > 0 && kn < 18) xf[kn & 1][t - 1] = lds128(t == 1 ? (kn < 9 ? x0 : y0) + o : (kn < 9 ? x1 : y1) + o + (t - 2) * 1024);
      SB();
    }
#pragma unroll
    for (int t = 0; t < 4; ++t) {
      if (ks == 0) mm0(a1[t], ring[sl + 1], xf[0][t]); else mm(a1[t], ring[sl + 1], xf[ks & 1][t]);
      if (t == 0) REFILL(2 * ks, 2);
      if (t == 1 && kn < 18) xf[kn & 1][4] = lds128((kn < 9 ? x4 : y4) + o);
      SB();
    }
  }
  mm_end();
  REFILL(35, 2);
  SB();
}
DI void groupo(f32x4 (&a)[5], const v4u (&ring)[18], const v4u (&ex)[6], u32 x0, u32 x1, u32 xe) {
  v4u xf[3][2], ef[6];
  asm volatile("" : "+v"(x0), "+v"(x1), "+v"(xe));
#pragma unroll
  for (int k = 0; k < 2; ++k) { xf[k][0] = lds128(x0 + k * 2048); xf[k][1] = lds128(x1 + k * 2048); }
  SB();
#pragma unroll
  for (int ks = 0; ks < 9; ++ks) {
    const int kn = ks + 2;
#pragma unroll
    for (int j = 0; j < 4; ++j) {
      if (ks == 0) mm0(a[j], ring[2 * ks + (j >> 1)], xf[0][j & 1]); else mm(a[j], ring[2 * ks + (j >> 1)], xf[ks % 3][j & 1]);
      if (kn < 9 && j < 2) xf[kn % 3][j] = lds128((j ? x1 : x0) + kn * 2048);
      if (kn >= 9 && j < 3) ef[3 * (kn - 9) + j] = lds128(xe + (3 * (kn - 9) + j) * 2048);
      SB();
    }
  }
#pragma unroll
  for (int k = 0; k < 6; ++k) { if (k == 0) mm0(a[4], ex[0], ef[0]); else mm(a[4], ex[k], ef[k]); SB(); }
  mm_end();
}
template <int LO, int HI> DI void fill(v4u (&ring)[18], rsrc_t rs, int v16, int s) {
  SB();
#pragma unroll
  for (int i = LO; i < HI; ++i) ring[i] = ldw(rs, v16 + (i & 3) * 1024, s + (i >> 2) * 4096);
  SB();
}
template <class T> DI void undef(T& v) { asm volatile("" : "=v"(v)); }
DI void stat_acc(v4u v, f32x4& s, f32x4& q) { s = mfma(v4u{0x3f803f80u, 0x3f803f80u, 0x3f803f80u, 0x3f803f80u}, v, s); q = mfma(v, v, q); }
DI float diag(f32x4 q, int g, int tl) {
  const float a = (tl & 1) ? q[1] : q[0], b = (tl & 1) ? q[3] : q[2];
  return xsum(g == (tl >> 2) ? ((tl & 2) ? b : a) : 0.f);
}
DI void split3(float x, u32& h, u32& m, u32& l) {
  h = pk(x, 0.f); x -= lo2f(h);
  m = pk(x, 0.f); x -= lo2f(m);
  l = pk(x, 0.f);
}
struct RowK { f32x4 k[4]; v2u aug[4]; };
template <int N> DI void gelu(float (&v)[N], f32x4 k) {
  float y[N];
#pragma unroll
  for (int r = 0; r < N; ++r) y[r] = v[r] * v[r];
  SB();
#pragma unroll
  for (int r = 0; r < N; ++r) y[r] = __builtin_fmaf(y[r], k[1], k[0]);
  SB();
#pragma unroll
  for (int r = 0; r < N; ++r) y[r] = v[r] * y[r];
  SB();
#pragma unroll
  for (int r = 0; r < N; ++r) y[r] = __builtin_amdgcn_exp2f(y[r]);
  SB();
#pragma unroll
  for (int r = 0; r < N; ++r) y[r] = __builtin_fmaf(y[r], k[2], k[2]);
  SB();
#pragma unroll
  for (int r = 0; r < N; ++r) y[r] = __builtin_amdgcn_rcpf(y[r]);
  SB();
#pragma unroll
  for (int r = 0; r < N; ++r) v[r] = v[r] * y[r];
  SB();
}
DI v2u gelu4(f32x4 a, f32x4 k) {
  float v[4] = {a[0], a[1], a[2], a[3]};
  gelu<4>(v, k);
  return v2u{pk(v[0], v[1]), pk(v[2], v[3])};
}
DI void aug_tile(f32x4 (&a)[4], v2u ta, const RowK& k) {
#pragma unroll
  for (int t = 0; t < 4; ++t) mmk16(a[t], ta, k.aug[t]);
}
DI void gelu_pair(f32x4 (&a0)[4], f32x4 (&a1)[4], u32 hp, const RowK& k) {
#pragma unroll
  for (int t = 0; t < 4; ++t) {
    float v[8] = {a0[t][0], a0[t][1], a0[t][2], a0[t][3], a1[t][0], a1[t][1], a1[t][2], a1[t][3]};
    gelu<8>(v, k.k[t]);
    sts128(hp + t * 1024, v4u{pk(v[0], v[1]), pk(v[2], v[3]), pk(v[4], v[5]), pk(v[6], v[7])});
  }
}
DI v4u pk8(f32x4 a, f32x4 b) { return v4u{pk(a[0], a[1]), pk(a[2], a[3]), pk(b[0], b[1]), pk(b[2], b[3])}; }
DI void part_pair(const f32x4 (&a0)[4], const f32x4 (&a1)[4], rsrc_t rs, int v16, int sb, int town, u32 lown) {
#pragma unroll
  for (int t = 0; t < 4; ++t) {
    const v4u d = pk8(a0[t], a1[t]);
    if (t == town) sts128(lown, d); else stx(d, rs, v16, sb + t * (2 * 8 * 9216));
  }
}
DI void make_sel(v4u (&sel)[2], int g, int tl) {
  const u32 one = g == (tl >> 2) ? ((tl & 1) ? 0x3f800000u : 0x00003f80u) : 0u, lo = (tl & 2) ? 0u : one, hi = (tl & 2) ? one : 0u;
  sel[0] = v4u{lo, hi, 0u, 0u};
  sel[1] = v4u{0u, 0u, lo, hi};
}
typedef GAS u64 gu64;
DI void drain() { asm volatile("s_waitcnt vmcnt(0) lgkmcnt(0)" ::: "memory"); }
DI void flag(rsrc_t rs, int v, int s, bool on, v2u tag) { if (on) stx2(tag, rs, v, s); }
template <int SL> DI void poll(rsrc_t rs, int lane, int s, int n, v2u tag) {
  if (lane < n) {
#pragma unroll 1
    for (u32 sp = 0; sp < (1u << 16); ++sp) {
      v2u x = {0u, 0u};
      asm volatile("buffer_atomic_or_x2 %0, %1, %2, %3 offen sc0\n\ts_waitcnt vmcnt(0)" : "+v"(x) : "v"(lane * 8), "s"(rs), "s"(s) : "memory");
      if (__builtin_amdgcn_ballot_w64(((x[0] ^ tag[0]) | (x[1] ^ tag[1])) != 0u) == 0) break;
      if (SL) __builtin_amdgcn_s_sleep(SL);
      asm volatile("" ::: "memory");
    }
  }
  asm volatile("" ::: "memory");
}
DI u32 lds_ticket(u32 a, int lane) {
  u32 r = 0;
  if (lane == 0) r = __hip_atomic_fetch_add((LAS u32*)(size_t)a, 1u, __ATOMIC_RELAXED, __HIP_MEMORY_SCOPE_WORKGROUP);
  return __builtin_amdgcn_readfirstlane(r);
}
DI void tile_tr(const v4u* m, v4u (&f)[4], u32 lw, int lane) {
  const u32 wr = lw + (lane >> 3) * 144 + (lane & 7) * 16, tr = lw + (8 * (lane >> 4) + ((lane & 15) >> 2)) * 144 + (lane & 3) * 16;
#pragma unroll
  for (int j = 0; j < 4; ++j) sts128(wr + j * 8 * 144, m[j]);
  asm volatile("s_waitcnt lgkmcnt(0)" ::: "memory");
#pragma unroll
  for (int q = 0; q < 4; ++q) {
    const v2u lo = ldstr(tr + (q >> 1) * 64 + (q & 1) * 8), hi = ldstr(tr + (q >> 1) * 64 + (q & 1) * 8 + 4 * 144);
    f[q] = v4u{lo[0], lo[1], hi[0], hi[1]};
  }
  asm volatile("s_waitcnt lgkmcnt(0)" : "+v"(f[0]), "+v"(f[1]), "+v"(f[2]), "+v"(f[3]) :: "memory");
}
template <int MT> DI void mlp_dest(int c, int cb, int q, int& so, int& st) {
  const int u = 2 * cb + (q >> 1), e = q & 1;
  int wd, y0;
  if (MT == 0) {
    wd = u < 8 ? u : u < 16 ? u - 8 : 4 + q;
    y0 = u < 8 ? e : u < 16 ? 36 + e : 38;
    st = u < 12 ? 2048 : 3072;
  } else {
    const bool sp = cb == 8, fa = (cb & 1) == 0;
    wd = sp ? q : fa ? cb >> 1 : 4 + (cb >> 1);
    y0 = sp ? 76 : fa ? 72 + q : 90 + 36 * (q >> 1) + e;
    st = fa ? 5120 : 2048;
  }
  so = WM_OFF + smul(c * 8 + wd, 165888) + y0 * 1024;
}
#define FRESH() \
  int lane = lane_, w = w_, G = G_, c = c_; \
  asm volatile("" : "+s"(w)); asm volatile("" : "+s"(G)); asm volatile("" : "+s"(c)); asm volatile("" : "+v"(lane)); \
  const int g = lane >> 4, tl = lane & 15, v16 = lane * 16, v16s = (lane ^ g) * 16; \
  const int wq = WQ_OFF + smul(c * 8 + w, 36864), wm = WM_OFF + smul(c * 8 + w, 165888), woc = WO_OFF + smul(c, 184320); \
  const int us = (c & 4) ? 13 : 4, ub = (c & 2) ? us + 1 : us - 4; \
  const bool first = (G & 15) == 0, ra = w < 4; \
  const int rowbase = 2 * 64 * (G & 15) + (G >> 4), gofs = smul(layer * 32 + G, 73728), mbg = MB_OFF + (layer * 256 + G * 8) * 1024; \
  (void)g; (void)tl; (void)v16; (void)v16s; (void)wq; (void)wm; (void)woc; (void)ub; (void)us; (void)first; (void)ra; (void)rowbase; (void)gofs; (void)mbg;
#define STAMP(i) do { if ((i) == PROBE) prb = __builtin_amdgcn_s_memrealtime(); } while (0)
#define WST(i)

#if defined(__HIP_DEVICE_COMPILE__)
#define NOPK __attribute__((target("no-packed-fp32-ops")))
#else
#define NOPK
#endif
__global__ NOPK void __launch_bounds__(512, 2) k_main(const u16* __restrict__ x, const u16* __restrict__ wqkv, const u16* __restrict__ wo,
    const u16* __restrict__ w1, const u16* __restrict__ b1, const u16* __restrict__ w2, const u16* __restrict__ b2, u16* __restrict__ out, char* __restrict__ ws) {
  extern __shared__ __attribute__((aligned(16))) char smem[];
  const u32 lb = (u32)(size_t)(LAS char*)smem;
  u32 codew;
  {
    u64 pc;
    asm volatile("s_getpc_b64 %0" : "=s"(pc));
    const u32 t = __builtin_amdgcn_workitem_id_x(), ln = (__builtin_amdgcn_workgroup_id_x() & ~7u) + (t >> 6) + 256 * (t & 1);
    const GAS u32* cp = (const GAS u32*)((pc & ~255ull) + (ln * 128 < CODE_BYTES ? ln * 128 : 0));
    asm volatile("global_load_dword %0, %1, off" : "=v"(codew) : "v"(cp) : "memory");
  }
  asm volatile("" :: "s"(x), "s"(wqkv), "s"(wo), "s"(w1), "s"(b2), "s"(ws));
  const int tid = __builtin_amdgcn_workitem_id_x(), bid = __builtin_amdgcn_workgroup_id_x(), lane_ = tid & 63, w_ = __builtin_amdgcn_readfirstlane(tid >> 6);
  int G_, c_;
  {
    const int xc = bid & 7, s = bid >> 3;
    if (XG == 8) { c_ = xc; G_ = s; }
    else if (XG == 1) { G_ = 4 * xc + (s >> 3); c_ = s & 7; }
    else if (XG == 2) { G_ = 8 * (xc >> 1) + (s >> 2); c_ = 4 * (xc & 1) + (s & 3); }
    else { G_ = 16 * (xc >> 2) + (s >> 1); c_ = 2 * (xc & 3) + (s & 1); }
  }
  u64 prb = __builtin_amdgcn_s_memrealtime();
  STAMP(0);
  const rsrc_t rs = __builtin_amdgcn_make_buffer_rsrc((void*)ws, 0, WS_USED, 0x00020000);
  const rsrc_t rx = __builtin_amdgcn_make_buffer_rsrc((void*)x, 0, 2048 * 1152, 0x00020000);
  const rsrc_t ro = __builtin_amdgcn_make_buffer_rsrc((void*)out, 0, 2048 * 1152, 0x00020000);
  v2u tag;
  v4u ring[18], mt[4];
  {
    const int layer = 2;
    FRESH();
    WST(32);
    if (tid < 24) *(LAS u32*)(size_t)(lb + L_CNT + 4 * tid) = 0u;
    const int r8 = lane >> 3, j8 = lane & 7, tt = w & 3;
    const bool isq = G < 18;
    const v4u ones = {0x3f803f80u, 0x3f803f80u, 0x3f803f80u, 0x3f803f80u};
    const u32 lw = lb + L_TQ + 18432 + w * 4608;
    if (ra && isq) {
      const int hi = lane >= 27, r = lane - 27 * hi, sec = (r >= 9) + (r >= 18);
      const int vo = (hi * 3456 + sec * (1152 - 144) + r * 16) | (lane < 54 ? 0 : OOB), s0 = smul(32 * G + 8 * w, 3456) + smul(c, 144);
      const rsrc_t rq = __builtin_amdgcn_make_buffer_rsrc((void*)wqkv, 0, 576 * 3456, 0x00020000);
      v4u a[4], f[4];
#pragma unroll
      for (int j = 0; j < 4; ++j) a[j] = ldp(rq, vo, s0 + j * (2 * 3456));
      SB();
      WST(33);
      bar();
      const u32 la = lb + L_TQ + (lane < 54 ? w * 3584 : 14336) + hi * 448 + r * 16;
      const u32 tb = lb + L_TQ + (8 * g + (tl >> 2)) * 448, mo = tb + 72 * (w >> 1) + 40 * (w & 1) + 8 * (tl & 3);
      const u32 ro_ = tb + 32 + (w < 2 ? 0 : 288) + (tl & 1) * (w < 2 ? 144 : 72) + (tl & 2) * (w < 2 ? 36 : 0);
#pragma unroll
      for (int j = 0; j < 4; ++j) sts128(la + j * 896, a[j]);
      lds_arrive(lb + L_CNT + 12, lane);
      lds_wait(lb + L_CNT + 12, 4);
      WST(35);
#pragma unroll
      for (int j = 0; j < 4; ++j) {
        const u32 ad = j == 0 ? mo + 144 : j == 1 ? mo : j == 2 ? mo + 288 : ro_;
        const v2u lo = ldstr(ad), hi_ = ldstr(ad + 4 * 448);
        f[j] = v4u{lo[0], lo[1], hi_[0], hi_[1]};
      }
#pragma unroll
      for (int j = 0; j < 4; ++j) stx(f[j], rs, v16, wq + G * 2048 + (j & 1) * 1024 + (j >> 1) * (4 * 36864));
      f32x4 cs[4];
#pragma unroll
      for (int j = 0; j < 4; ++j) cs[j] = mfma(f[j], ones, f32x4{0.f, 0.f, 0.f, 0.f});
      const f32x4 d = tl == 0 ? cs[0] : tl == 1 ? cs[1] : tl == 2 ? cs[2] : cs[3];
      stx(__builtin_bit_cast(v4u, d), rs, tl < 4 ? (tl >> 1) * 512 + (tl & 1) * 64 + g * 16 : OOB, CSQ_OFF + smul(c, 18432) + G * 1024 + w * 128);
    }
    if (ra && !isq) {
      const int ti = (G - 18) * 4 + w, tk = smul(ti, 43) >> 7, tp = ti - smul(tk, 3);
      bar();
      if (ti < 54) {
        const int vo = ((r8 >> 2) * (tk < 16 ? 20 : 36) + (r8 & 3)) * 1152 + j8 * 16;
        const int s0 = smul(tk, tk < 16 ? 36 * 1152 : 288 * 1152) - (tk < 16 ? 0 : (16 * 288 - 16) * 1152) + (tp < 2 ? ub + 2 * tp : us) * 64, ss = tk < 16 ? 4 * 1152 : 72 * 1152;
        const rsrc_t rw = __builtin_amdgcn_make_buffer_rsrc((void*)wo, 0, 576 * 1152, 0x00020000);
        v4u a[4], f[4];
        a[0] = ldp(rw, vo, s0); a[1] = ldp(rw, vo, s0 + ss); a[2] = ldp(rw, vo, s0 + 2 * ss); a[3] = ldp(rw, vo, s0 + 2 * ss + ss);
        SB();
        WST(33);
        tile_tr(a, f, lw, lane);
        const int so = woc + (tp < 2 ? smul(tp, 2 * 18432) + tk * 2048 + (tk < 9 ? 0 : 3 * 18432) : 8 * 18432 + tk * 1024);
        stx(f[0], rs, v16, so); stx(f[1], rs, v16, so + (tp < 2 ? 1024 : 18432));
        if (tp < 2) { stx(f[2], rs, v16, so + 18432); stx(f[3], rs, v16, so + 18432 + 1024); }
      }
    }
    if (!ra) {
      const int n = (c & 3) * 162 + tt * 54 + lane, row = (n * 7282) >> 16, ln = n - 9 * row;
      bar();
      u32 t0 = 0;
      if (w < 7) t0 = __builtin_amdgcn_raw_buffer_load_b32(rx, lane < 54 && !(first && row < 8) ? (rowbase + 2 * (row - 8)) * 1152 + ln * 128 : OOB, 0, 0);
      else {
        const u64 ep = __hip_atomic_load((gu64*)(ws + EP_OFF), RLX);
        {
          const u32 z = 0, mo = lane < 48 ? (lane >> 4) * (256 * 1024) + (lane & 15) * 64 : OOB;
          asm volatile("buffer_atomic_or %0, %1, %2, %3 offen" :: "v"(z), "v"(mo), "s"(rs), "s"(MB_OFF + (G * 8 + c) * 1024) : "memory");
        }
        *(LAS u64*)(size_t)(lb + L_CNT + 32) = ep + 1;
        lds_arrive(lb + L_CNT + 28, lane);
      }
      const u32 bv = b2[tid - 256], bv2 = b2[tid], bv3 = b2[512 + lane];
      SB();
      WST(33);
      const int vx = (rowbase + 32 * tt + 2 * r8) * 1152 + j8 * 16, vh = first ? OOB : (rowbase - 16 + 2 * r8) * 1152 + j8 * 16 + tt * 128;
      u32 lx = lb + (j8 >> 2) * 5120 + ((j8 & 3) * 16 + (r8 ^ (j8 & 3))) * 16, lh = lx + 4096 + tt * 10240;
      lx += tt * 1024;
      u32 ly = lx + 5 * 10240;
      asm volatile("" : "+v"(lx), "+v"(ly));
      *(LAS u32*)(size_t)(lb + L_TB2 + 4 * (tid - 256)) = bv << 16;
      *(LAS u32*)(size_t)(lb + L_TB2 + 4 * tid) = bv2 << 16;
      *(LAS u32*)(size_t)(lb + L_TB2 + 2048 + 4 * lane) = bv3 << 16;
      asm volatile("" :: "v"(t0) : "memory");
      SB();
      WST(35);
      v4u xv[18], xh[3];
#pragma unroll
      for (int i = 0; i < 9; ++i) { xv[2 * i] = ldp(rx, vx + i * 128, 0); xv[2 * i + 1] = ldp(rx, vx + i * 128, 16 * 1152); }
      xh[0] = ldp(rx, vh, 0); xh[1] = ldp(rx, vh + 512, 0);
      undef(xh[2]);
      if (tt == 0) xh[2] = ldp(rx, vh + 1024, 0);
      SB();
#pragma unroll
      for (int i = 0; i < 18; ++i) sts128((i < 10 ? lx + (i >> 1) * 10240 : ly + ((i >> 1) - 5) * 10240) + (i & 1) * 128, xv[i]);
      sts128(lh, xh[0]); sts128(lh + 128, xh[0]); sts128(lh + 40960, xh[1]); sts128(lh + 40960 + 128, xh[1]);
      if (tt == 0) { sts128(lh + 81920, xh[2]); sts128(lh + 81920 + 128, xh[2]); }
      lds_arrive(lb + L_CNT, lane);
      lds_arrive(lb + L_CNT + 40, lane);
    }
    WST(36);
    drain();
    asm volatile("" :: "v"(codew));
    WST(37);
    lds_wait(lb + L_CNT + 28, 1);
    tag = lds64(lb + L_CNT + 32);
    if (ra && lds_ticket(lb + L_CNT + 20, lane) == 3) {
      flag(rs, lane * 8192, MB_OFF + (2 * 256 + c) * 1024 + (G + MB_WQ) * 8, lane < 32, tag);
      lds_arrive(lb + L_CNT + 16, lane);
    }
    STAMP(30);
    WST(38);
#pragma unroll
    for (int j = 0; j < 4; ++j) undef(mt[j]);
    if (G < 27 && w < 6) {
      const int kt = smul(G, 57) >> 9, cb = G - smul(kt, 9), p0 = smul(kt, 6) + w;
      const rsrc_t r1 = __builtin_amdgcn_make_buffer_rsrc((void*)w1, 0, 576 * 9216, 0x00020000);
      const int s1 = smul(p0, 32 * 9216) + smul(c, 1152) + cb * 128;
      v4u m1[4];
      lds_wait(lb + L_CNT + 16, 1);
#pragma unroll
      for (int j = 0; j < 4; ++j) m1[j] = ldp(r1, r8 * 9216 + j8 * 16, s1 + j * (8 * 9216));
      tile_tr(m1, mt, lw, lane);
      f32x4 cs[4];
#pragma unroll
      for (int q = 0; q < 4; ++q) cs[q] = mfma(mt[q], ones, f32x4{0.f, 0.f, 0.f, 0.f});
      const f32x4 d = tl == 0 ? cs[0] : tl == 1 ? cs[1] : tl == 2 ? cs[2] : cs[3];
      if (tl < 4) *(LAS f32x4*)(size_t)(lb + L_TC + w * 256 + tl * 64 + g * 16) = d;
    }
    if (w == 7) { poll<0>(rs, lane, mbg + c * 1024 + MB_WQ * 8, 32, tag); lds_arrive(lb + L_CNT + 8, lane); }
    lds_wait(lb + L_CNT + 8, 1);
    WST(43);
    if (!ra) __builtin_amdgcn_s_sleep(8);
    fill<0, 18>(ring, rs, v16, wq);
    WST(44);
    if (w == 7) {
      v4u p[18];
#pragma unroll
      for (int i = 0; i < 18; ++i) p[i] = ldx(rs, v16, CSQ_OFF + smul(c, 18432) + i * 1024);
      f32x4 cs = __builtin_bit_cast(f32x4, p[0]);
#pragma unroll
      for (int i = 1; i < 18; ++i) cs += __builtin_bit_cast(f32x4, p[i]);
      *(LAS f32x4*)(size_t)(lb + L_TCQ + v16) = cs;
    }
  }

#pragma unroll 1
  for (int layer = 0; layer < 2; ++layer) {
    f32x4 m0, m1;
    {
      FRESH();
      const int tt = w & 3;
      const bool swp = (w == 5) || (w == 7);
      f32x4 a0[5], a1[4];
      float rr[5], mr[5];
#pragma unroll
      for (int t = 0; t < 5; ++t) { undef(rr[t]); undef(mr[t]); }
      undef(m0); undef(m1);
#define ROWSTATS() \
      { \
        const bool nohalo = first && w < 2; \
        _Pragma("unroll") for (int r = 0; r < 4; ++r) { \
          const int kl = 4 * g + r; \
          m0[r] = (kl >= tl + 9 && !nohalo) ? 0.f : -1e30f; \
          m1[r] = (kl <= tl && kl + 7 >= tl) ? 0.f : -1e30f; \
        } \
        lds_wait(lb + L_CNT + 4, 8 * layer + 4); \
        _Pragma("unroll") for (int t = 0; t < 4; ++t) { const f32x2 p = ldsf2(lb + L_SS + t * 128 + tl * 8); rr[t] = p[0]; mr[t] = p[1]; } \
        f32x2 p = ldsf2(lb + L_SS + 4 * 128 + tl * 8); \
        _Pragma("unroll") for (int j = 5; j < 8; ++j) p += ldsf2(lb + L_SS + j * 128 + tl * 8); \
        const float m = p[0] * (1.f / 576.f), var = __builtin_fmaxf(__builtin_fmaf(p[1], 1.f / 576.f, -m * m), 0.f); \
        rr[4] = __builtin_amdgcn_rsqf(var + 1e-5f); \
        mr[4] = -m * rr[4]; \
      }
      lds_wait(lb + L_CNT, 8 * layer + 4);
      if (!ra) {
        lds_wait(lb + L_CNT + 40, 8 * layer + 4);
        __builtin_amdgcn_s_setprio(3);
        f32x4 s = {0.f, 0.f, 0.f, 0.f}, q = s, sh_ = s, qh_ = s;
        v4u xm[18], xh[5];
        u32 xa = lb + tt * 1024 + v16s, xb = xa + 46080, xc = lb + tt * 5120 + 4096 + v16s;
        asm volatile("" : "+v"(xa), "+v"(xb), "+v"(xc));
#pragma unroll
        for (int i = 0; i < 18; ++i) xm[i] = lds128((i < 9 ? xa : xb) + (i % 9) * 5120);
#pragma unroll
        for (int i = 0; i < 4; ++i) xh[i] = lds128(xc + i * 20480);
        xh[4] = v4u{0u, 0u, 0u, 0u};
        if (tt < 2) xh[4] = lds128(xc + 81920);
#pragma unroll
        for (int i = 0; i < 18; ++i) stat_acc(xm[i], s, q);
#pragma unroll
        for (int i = 0; i < 5; ++i) stat_acc(xh[i], sh_, qh_);
        const float m = s[0] * (1.f / 576.f), var = __builtin_fmaxf(__builtin_fmaf(diag(q, g, tl), 1.f / 576.f, -m * m), 0.f);
        const float r = __builtin_amdgcn_rsqf(var + 1e-5f), qh = diag(qh_, g, tl);
        if (lane < 32) *(LAS f32x2*)(size_t)(lb + L_SS + (tt + 4 * g) * 128 + tl * 8) = g == 0 ? f32x2{r, -m * r} : f32x2{sh_[0], qh};
        __builtin_amdgcn_s_setprio(0);
        lds_arrive(lb + L_CNT + 4, lane);
        ROWSTATS();
        if (swp) { float a = rr[0]; rr[0] = rr[4]; rr[4] = a; a = mr[0]; mr[0] = mr[4]; mr[4] = a; }
      }
      WST(18);
      STAMP(1 + 16 * layer);
      groupq(a0, a1, ring, lb + v16s + (swp ? 4096 : 0), lb + v16s + 1024, lb + v16s + (swp ? 0 : 4096), rs, v16, wq + 18432, woc + smul(w, 18432), lb + L_CNT + 40, 8 * layer + 4);
      WST(19);
      STAMP(2 + 16 * layer);
      if (layer == 0) WST(45);
      if (ra) ROWSTATS();
      const f32x4 c0 = ldsf4(lb + L_TCQ + w * 128 + g * 16), c1 = ldsf4(lb + L_TCQ + w * 128 + 64 + g * 16);
      const int t0 = w < 4 ? w : 4 + w, t1 = w < 4 ? 4 + w : 12 + ((w - 4) >> 1);
      const u32 e0 = lb + L_E + t0 * 2560 + lane * 8, e1 = lb + L_E + t1 * 2560 + lane * 8;
#pragma unroll
      for (int t = 0; t < 5; ++t) {
        const int slot = t == 0 ? (swp ? 0 : 1) : t == 4 ? (swp ? 1 : 0) : t + 1;
        float y[4];
#pragma unroll
        for (int r = 0; r < 4; ++r) y[r] = __builtin_fmaf(rr[t], a0[t][r], mr[t] * c0[r]);
        sts64(e0 + slot * 512, v2u{pk(y[0], y[1]), pk(y[2], y[3])});
        if (t < 4) {
#pragma unroll
          for (int r = 0; r < 4; ++r) y[r] = __builtin_fmaf(rr[t], a1[t][r], mr[t] * c1[r]);
          sts64(e1 + slot * 512, v2u{pk(y[0], y[1]), pk(y[2], y[3])});
        }
      }
    }
    v4u nring[18], ex[6];
    {
      FRESH();
      const int p = w & 1, qt = w >> 1;
      u32 el = lb + L_E + qt * 512 + p * 5120 + lane * 8, et = lb + L_E + qt * 512 + ((lane & 3) * 16 + 4 * g + (tl >> 2)) * 8, er = lb + L_E + qt * 512 + p * 256 + tl * 8;
      u32 ev = et + p * 5120;
      asm volatile("" : "+v"(el), "+v"(et), "+v"(er), "+v"(ev));
      WST(20);
      bar();
      WST(21);
      STAMP(3 + 16 * layer);
      const v2u qr = lds64(er + 12 * 2560 + 512), kp = lds64(er + 12 * 2560 + 128), kc = lds64(er + 12 * 2560 + 512 + 128);
      v2u k0[2], k1[2], qm[2], v0[2], v1[2];
#pragma unroll
      for (int e = 0; e < 2; ++e) { k0[e] = lds64(el + e * 2560); k1[e] = lds64(el + e * 2560 + 512); qm[e] = lds64(el + (4 + e) * 2560 + 512); }
#pragma unroll
      for (int e = 0; e < 2; ++e) { v0[e] = ldstr(ev + (8 + e) * 2560); v1[e] = ldstr(ev + (8 + e) * 2560 + 512); }
      const v2u r0 = ldstr(et + 13 * 2560), r1 = ldstr(et + 13 * 2560 + 512);
      SB();
#pragma unroll
      for (int i = 0; i < 18; ++i) undef(nring[i]);
#pragma unroll
      for (int i = 0; i < 6; ++i) undef(ex[i]);
      if (layer == 0 && G < 27) {
        const int kt = smul(G, 57) >> 9, cb = G - smul(kt, 9), p0 = smul(kt, 6) + w;
        if (w < 6) {
          const rsrc_t r2 = __builtin_amdgcn_make_buffer_rsrc((void*)w2, 0, 4608 * 1152, 0x00020000);
#pragma unroll
          for (int q = 0; q < 4; ++q) { int so, st; mlp_dest<0>(c, cb, q, so, st); stx(mt[q], rs, v16, so + smul(st, p0)); }
#pragma unroll
          for (int j = 0; j < 4; ++j) mt[j] = ldp(r2, (lane >> 3) * 1152 + (lane & 7) * 16, smul(smul(c, 576) + 32 * p0, 1152) + cb * 128 + j * (8 * 1152));
        } else if (w == 6) {
          f32x4 cs = ldsf4(lb + L_TC + tl * 16);
#pragma unroll
          for (int j = 1; j < 6; ++j) cs += ldsf4(lb + L_TC + j * 256 + tl * 16);
          stx(__builtin_bit_cast(v4u, cs), rs, lane < 16 ? tl * 16 : OOB, CS1_OFF + smul(c, 20736) + smul(kt, 2304) + cb * 256);
        }
      }
      if (w < 6) {
#pragma unroll
        for (int i = 0; i < 6; ++i) ex[i] = ldw(rs, v16 + (i & 3) * 1024, woc + smul(8 + (w & 1), 18432) + smul(w >> 1, 6144) + (i >> 2) * 4096);
      }
      if (layer == 1) fill<0, AUX1>(nring, rs, v16, wm);
      SB();
      const v4u vr = {r0[0], r0[1], r1[0], r1[1]};
      v4u om; v2u orr;
#pragma unroll
      for (int e = 0; e < 2; ++e) {
        const v4u qf = {qm[e][0], qm[e][1], g == 0 ? qr[e] : 0u, 0u};
        const f32x4 s0 = mfma(v4u{k0[e][0], k0[e][1], kp[e], 0u}, qf, m0), s1 = mfma(v4u{k1[e][0], k1[e][1], kc[e], 0u}, qf, m1);
        float mx = __builtin_fmaxf(__builtin_fmaxf(__builtin_fmaxf(s0[0], s0[1]), __builtin_fmaxf(s0[2], s0[3])),
                                   __builtin_fmaxf(__builtin_fmaxf(s1[0], s1[1]), __builtin_fmaxf(s1[2], s1[3])));
        mx = xmax(mx);
        constexpr float sc = 0.23570226039551584f * 1.4426950408889634f;
        const float nb = -mx * sc;
        float pe[8], sum = 0.f;
#pragma unroll
        for (int r = 0; r < 4; ++r) {
          pe[r] = __builtin_amdgcn_exp2f(__builtin_fmaf(s0[r], sc, nb));
          pe[4 + r] = __builtin_amdgcn_exp2f(__builtin_fmaf(s1[r], sc, nb));
          sum += pe[r] + pe[4 + r];
        }
        const v4u pf = {pk(pe[0], pe[1]), pk(pe[2], pe[3]), pk(pe[4], pe[5]), pk(pe[6], pe[7])};
        const f32x4 z = {0.f, 0.f, 0.f, 0.f};
        const f32x4 o = mfma(v4u{v0[e][0], v0[e][1], v1[e][0], v1[e][1]}, pf, z), oq = mfma(vr, pf, z);
        const float inv = __builtin_amdgcn_rcpf(xsum(sum));
        om[2 * e] = pk(o[0] * inv, o[1] * inv); om[2 * e + 1] = pk(o[2] * inv, o[3] * inv);
        orr[e] = pk(oq[2 * e] * inv, oq[2 * e + 1] * inv);
      }
      const int ob = OB_OFF + gofs + qt * 1024;
      stx(om, rs, v16, ob + (2 * c + p) * 4096);
      stx2(orr, rs, g == p ? tl * 8 : OOB, ob + (16 + (c >> 2)) * 4096 + (c & 3) * 256 + p * 128);
      WST(22);
      STAMP(4 + 16 * layer);
      int fs = mbg + (MB_O + c) * 8, ps = mbg + c * 1024 + MB_O * 8, fv = lane * 1024;
      asm volatile("" : "+s"(fs)); asm volatile("" : "+s"(ps)); asm volatile("" : "+v"(fv));
      drain();
      WST(23);
      bar();
      flag(rs, fv, fs, w == 7 && lane < 8, tag);
      WST(24);
      STAMP(5 + 16 * layer);
      if (layer == 0) {
        flag(rs, lane * 8192, MB_OFF + (2 * 256 + c) * 1024 + (G + MB_W1) * 8, w == 6 && lane < 32, tag);
        if (w < 6) { v4u f[4]; tile_tr(mt, f, lb + L_E + w * 4608, lane); mt[0] = f[0]; mt[1] = f[1]; mt[2] = f[2]; mt[3] = f[3]; }
      }
      if (layer == 1) fill<AUX1, AUX2>(nring, rs, v16, wm);
      if (w == 7) { __builtin_amdgcn_s_sleep(PD0); poll<0>(rs, lane, ps, 8, tag); }
    }
    {
      FRESH();
      const int th = c & 1, kh = w >> 2, sw = (c >> 1) & 1, u1 = ub + (w & 3);
      int ob = OB_OFF + gofs + th * 2048 + (w >> 1) * 4096 + (w & 1) * 1024;
      u32 lo = lb + L_OI + w * 1024 + v16, l4 = lb + L_OI + (32 + w) * 1024 + (g * 16 + 2 * (lane & 7)) * 16 + ((lane >> 3) & 1) * 8;
      u32 x0 = lb + L_OI + v16 + kh * (9 * 2048 + 1024), x1 = lb + L_OI + v16 + kh * (9 * 2048) + (kh ^ 1) * 1024;
      u32 xe = lb + L_OI + v16 + sw * 1024 + (w < 6 ? smul(w >> 1, 6 * 2048) : 0);
      const v4u xr = lds128(lb + smul(u1, 5120) + (2 * th + kh) * 1024 + v16s);
      asm volatile("" : "+s"(ob)); asm volatile("" : "+v"(lo), "+v"(l4), "+v"(x0), "+v"(x1), "+v"(xe));
      bar();
      WST(25);
      STAMP(6 + 16 * layer);
      v4u of[5];
#pragma unroll
      for (int i = 0; i < 4; ++i) of[i] = ldx(rs, v16, ob + i * 16384);
      undef(of[4]);
      if (ra) of[4] = ldx(rs, v16, ob + 65536);
      SB();
#pragma unroll
      for (int i = 0; i < 4; ++i) sts128(lo + i * 8192, of[i]);
      if (ra) { sts64(l4, v2u{of[4][0], of[4][1]}); sts64(l4 + 16, v2u{of[4][2], of[4][3]}); }
      bar();
      WST(26);
      STAMP(7 + 16 * layer);
      if (layer == 0 && G < 27 && w < 6) {
        const int kt = smul(G, 57) >> 9, cb = G - smul(kt, 9), p0 = smul(kt, 6) + w;
#pragma unroll
        for (int q = 0; q < 4; ++q) { int so, st; mlp_dest<1>(c, cb, q, so, st); stx(mt[q], rs, v16, so + smul(st, p0)); }
      }
      if (layer == 1) fill<AUX2, AUX3>(nring, rs, v16, wm);
      f32x4 a[5];
      groupo(a, ring, ex, x0, x1, xe);
      WST(27);
      *(LAS f32x4*)(size_t)(lb + L_PX + w * 2048 + v16) = a[1];
      *(LAS f32x4*)(size_t)(lb + L_PX + w * 2048 + 1024 + v16) = a[3];
      if (w < 6) *(LAS f32x4*)(size_t)(lb + smul(w, 5120) + 4096 + v16) = a[4];
      a[0] += f32x4{lo2f(xr[0]), hi2f(xr[0]), lo2f(xr[1]), hi2f(xr[1])};
      a[2] += f32x4{lo2f(xr[2]), hi2f(xr[2]), lo2f(xr[3]), hi2f(xr[3])};
      int xb = X1_OFF + gofs + u1 * 4096 + (2 * th + kh) * 1024, fs = mbg + (MB_X1 + c) * 8, ps = mbg + c * 1024 + MB_X1 * 8, fv = lane * 1024;
      u32 pa = lb + L_PX + (w ^ 4) * 2048 + v16;
      asm volatile("" : "+s"(xb)); asm volatile("" : "+s"(fs)); asm volatile("" : "+s"(ps)); asm volatile("" : "+v"(fv), "+v"(pa), "+v"(a[0]), "+v"(a[2]));
      bar();
      stx(pk8(a[0] + ldsf4(pa), a[2] + ldsf4(pa + 1024)), rs, v16, xb);
      if (w == 6) {
        const v4u xv = lds128(lb + smul(us, 5120) + (2 * th + sw) * 1024 + v16s);
        const u32 pb = lb + 4096 + v16;
        const f32x4 e0 = ldsf4(pb) + ldsf4(pb + 10240) + ldsf4(pb + 20480) + f32x4{lo2f(xv[0]), hi2f(xv[0]), lo2f(xv[1]), hi2f(xv[1])};
        const f32x4 e1 = ldsf4(pb + 5120) + ldsf4(pb + 15360) + ldsf4(pb + 25600) + f32x4{lo2f(xv[2]), hi2f(xv[2]), lo2f(xv[3]), hi2f(xv[3])};
        stx(pk8(e0, e1), rs, v16, X1_OFF + gofs + us * 4096 + (2 * th + sw) * 1024);
      }
      WST(28);
      STAMP(8 + 16 * layer);
      if (layer == 0 && w == 7) poll<0>(rs, lane, MB_OFF + (2 * 256 + G * 8 + c) * 1024 + MB_W1 * 8, 32, tag);
      drain();
      WST(29);
      bar();
      flag(rs, fv, fs, w == 7 && lane < 8, tag);
      WST(30);
      if (layer == 0) {
        flag(rs, lane * 8192, MB_OFF + (2 * 256 + c) * 1024 + (G + MB_W2) * 8, w == 6 && lane < 32, tag);
        fill<0, AUX3>(nring, rs, v16, wm);
      }
      fill<AUX3, 18>(nring, rs, v16, wm);
      if (layer == 0 && w < 6) {
#pragma unroll
        for (int j = 0; j < 2; ++j) {
          if (j == 0 || lane < 32) {
            const int n = w * 96 + lane + 64 * j;
            float cs = 0.f;
#pragma unroll
            for (int k = 0; k < 3; ++k) cs += __builtin_bit_cast(float, __builtin_amdgcn_raw_buffer_load_b32(rs, n * 4, CS1_OFF + smul(c, 20736) + k * 2304, 16));
            u32 h, m, l;
            split3(cs, h, m, l);
            sts64(lb + L_TA + 8 * n, v2u{h | (m << 16), b1[576 * c + colp(n >> 5, (n >> 4) & 1, n & 15)] | (l << 16)});
          }
        }
      }
      if (w == 7) { __builtin_amdgcn_s_sleep(PD0); poll<0>(rs, lane, ps, 8, tag); }
    }
#pragma unroll
    for (int i = 0; i < 18; ++i) ring[i] = nring[i];
    {
      FRESH();
      const int tt = w & 3;
      int sm = X1_OFF + gofs + tt * 1024;
      u32 la = lb + tt * 1024 + v16, lc = la + CH1 * 4096;
      asm volatile("" : "+s"(sm)); asm volatile("" : "+v"(la), "+v"(lc));
      bar();
      WST(31);
      STAMP(9 + 16 * layer);
      if (ra) {
        v4u xm[CH1];
#pragma unroll
        for (int i = 0; i < CH1; ++i) xm[i] = ldx(rs, v16, sm + i * 4096);
        SB();
#pragma unroll
        for (int i = 0; i < CH1; ++i) sts128(la + i * 4096, xm[i]);
        lds_arrive(lb + L_CNT, lane);
        lds_wait(lb + L_CNT, 8 * layer + 8);
      }
      int wb = w;
      asm volatile("" : "+s"(wb));
      if (wb >= 4) {
        __builtin_amdgcn_s_sleep(STSL);
        v4u xm[18 - CH1], ym[CH1];
#pragma unroll
        for (int i = 0; i < 18 - CH1; ++i) xm[i] = ldx(rs, v16, sm + (CH1 + i) * 4096);
        SB();
#pragma unroll
        for (int i = 0; i < 18 - CH1; ++i) sts128(lc + i * 4096, xm[i]);
        lds_arrive(lb + L_CNT + 40, lane);
        lds_wait(lb + L_CNT, 8 * layer + 8);
        __builtin_amdgcn_s_setprio(3);
#pragma unroll
        for (int i = 0; i < CH1; ++i) ym[i] = lds128(la + i * 4096);
        f32x4 s = {0.f, 0.f, 0.f, 0.f}, q = s;
#pragma unroll
        for (int i = 0; i < 18 - CH1; ++i) stat_acc(xm[i], s, q);
#pragma unroll
        for (int i = 0; i < CH1; ++i) stat_acc(ym[i], s, q);
        const float m = s[0] * (1.f / 576.f), var = __builtin_fmaxf(__builtin_fmaf(diag(q, g, tl), 1.f / 576.f, -m * m), 0.f);
        const float r1 = __builtin_amdgcn_rsqf(var + 1e-5f), r = r1 * __builtin_amdgcn_rsqf(var * r1 * r1 + 1e-5f), ri = __builtin_amdgcn_rcpf(r);
        constexpr float k1 = -2.f * 0.7978845608028654f * 1.4426950408889634f, k2 = k1 * 0.044715f;
        u32 mh, mm_, ml, rh, rm, rl;
        split3(-m, mh, mm_, ml); split3(ri, rh, rm, rl);
        const u32 mv = g == 0 ? mh : g == 1 ? mm_ : g == 2 ? ml : 0u, rv = g == 0 ? rh : g == 1 ? rm : g == 2 ? rl : 0u;
        sts64(lb + L_RK + tt * 512 + lane * 8, v2u{mv | (mv << 16), rv | (mv << 16)});
        if (lane < 16) *(LAS f32x4*)(size_t)(lb + L_SS + tt * 256 + tl * 16) = f32x4{k1 * r, k2 * r * r * r, ri, 0.f};
        __builtin_amdgcn_s_setprio(0);
        if (layer == 0 && w == 7) poll<0>(rs, lane, MB_OFF + (2 * 256 + G * 8 + c) * 1024 + MB_W2 * 8, 32, tag);
        lds_arrive(lb + L_CNT + 4, lane);
      }
    }
    STAMP(10 + 16 * layer);
    {
      FRESH();
      const int i = w & 3, ot = c >> 1, hb = c & 1;
      const int pb = PB_OFF + gofs * 8 + smul((i >> 1) * 8 + c, 9216);
      const int town = (i >> 1) == hb ? ot : -1;
      const u32 xa = lb + v16, ha = lb + L_H + v16, lo = xa + (ot ^ 1) * 1024;
      u32 ta = lb + L_TA + tl * 8, ka = lb + L_SS + tl * 16, ga = lb + L_RK + lane * 8;
      asm volatile("" : "+v"(ta), "+v"(ka), "+v"(ga));
#define LOADRK() _Pragma("unroll") for (int t = 0; t < 4; ++t) { rk.aug[t] = lds64(ga + t * 512); rk.k[t] = ldsf4(ka + t * 256); }
      int sn = wm + 18432;
      WST(0);
#pragma unroll 1
      for (int gi = 0; gi < (ra ? 2 : 1); ++gi) {
        const int u = ra ? 8 * gi + i : 4 + i;
        f32x4 a[2][4];
        RowK rk;
        v2u t0, t1;
        group<2, CH1, 1>(a, ring, xa, rs, v16, sn, sn + 18432, [&]() INL { if (gi == 0) lds_wait(lb + L_CNT + 40, 8 * layer + 8); }, [&]() INL {
          if (gi == 0) lds_wait(lb + L_CNT + 4, 8 * layer + 8);
          t0 = lds64(ta + u * 256); t1 = lds64(ta + u * 256 + 128);
          LOADRK();
        });
        sn += 2 * 18432;
        __builtin_amdgcn_s_setprio(3);
        aug_tile(a[0], t0, rk); aug_tile(a[1], t1, rk);
        __builtin_amdgcn_s_setprio(0);
        mm_end();
        WST(1 + 2 * gi);
        gelu_pair(a[0], a[1], ha + u * 4096, rk);
        WST(2 + 2 * gi);
      }
      if (ra) {
        f32x4 a[5][4];
        bar();
        STAMP(11 + 16 * layer);
        WST(8);
        group<5, 12, 0>(a, ring, ha, rs, v16, sn, 0, []() INL { bar(); }, []() INL {});
        mm_end();
        WST(11);
        __builtin_amdgcn_s_setprio(APRIO);
        const u32 ls = xa + (ot ^ 2) * 1024 + (i >> 1) * 16384;
#pragma unroll
        for (int t = 0; t < 4; ++t) sts64((t == town ? lo + (16 + hb) * 4096 : ls + t * 4096) + (i & 1) * 8, v2u{pk(a[4][t][0], a[4][t][1]), pk(a[4][t][2], a[4][t][3])});
        lds_arrive(lb + L_CNT + 48 + (i >> 1) * 4, lane);
        part_pair(a[0], a[1], rs, v16, pb + ((4 * i) & 7) * 1024, town, lo + (4 * i) * 4096);
        part_pair(a[2], a[3], rs, v16, pb + ((4 * i + 1) & 7) * 1024, town, lo + (4 * i + 1) * 4096);
        lds_wait(lb + L_CNT + 48 + (i >> 1) * 4, 2 * layer + 2);
#pragma unroll
        for (int k = 0; k < 2; ++k) {
          const int t = 2 * (i & 1) + k;
          if (t != town) stx(lds128(ls + t * 4096), rs, v16, pb + 8192 + smul(t, 2 * 8 * 9216));
        }
      }
      int wb = w;
      asm volatile("" : "+s"(wb));
      if (wb >= 4) {
        v4u aux[18];
        {
          f32x4 a[3][4];
          RowK rk;
          v2u t0, t1, t2;
          group<3, 99, 1>(a, ring, xa, rs, v16, sn, sn + 2 * 18432, []() INL {}, [&]() INL {
            t0 = lds64(ta + (12 + i) * 256); t1 = lds64(ta + (12 + i) * 256 + 128); t2 = lds64(ta + (16 + (i >> 1)) * 256 + (i & 1) * 128);
            LOADRK();
          });
          sn += 3 * 18432;
          aug_tile(a[0], t0, rk); aug_tile(a[1], t1, rk); aug_tile(a[2], t2, rk);
          mm_end();
          WST(7);
          bar();
          WST(8);
          gelu_pair(a[0], a[1], ha + (12 + i) * 4096, rk);
#pragma unroll
          for (int t = 0; t < 4; ++t) sts64(ha + (16 + (i >> 1)) * 4096 + t * 1024 + (i & 1) * 8, gelu4(a[2][t], rk.k[t]));
          WST(9);
          bar();
          WST(10);
        }
        fill<0, 18>(aux, rs, v16, sn);
        f32x4 a[2][4];
        group36<true>(a, ring, aux, ha, rs, v16, sn + 18432, []() INL {});
        WST(3);
#pragma unroll
        for (int t = 0; t < 4; ++t) sts128(t == town ? lo + (4 * i + 2) * 4096 : xa + (ot ^ 3) * 1024 + (4 * i + t) * 4096, pk8(a[0][t], a[1][t]));
        lds_arrive(lb + L_CNT + 64 + 4 * i, lane);
        WST(4);
        group36<false>(a, ring, aux, ha, rs, v16, 0, []() INL {});
        WST(5);
        part_pair(a[0], a[1], rs, v16, pb + ((4 * i + 3) & 7) * 1024, town, lo + (4 * i + 3) * 4096);
      }
      WST(12);
      STAMP(12 + 16 * layer);
    }
    {
      FRESH();
      const int tt = c >> 1, hb = c & 1;
      int po[7];
#pragma unroll
      for (int j = 0; j < 7; ++j) { po[j] = PB_OFF + gofs * 8 + smul(c * 8 + ((c + 1 + j) & 7), 9216); asm volatile("" : "+s"(po[j])); }
      int vo = layer == 0 ? v16 : (rowbase + 2 * (16 * tt + tl)) * 1152 + 16 * g;
      v4u sel[2];
      make_sel(sel, g, tl);
      int slot = (0x62854100u >> (4 * w)) & 15, vs = v16 + slot * 1024, fv = lane * 2048;
      int fs = mbg + ((w >> 1) & 1) * 1024 + ((ra ? MB_A : MB_B) + 2 * c + (w & 1)) * 8;
      asm volatile("" : "+s"(fs)); asm volatile("" : "+s"(slot));
      asm volatile("" : "+v"(vo), "+v"(vs), "+v"(fv), "+v"(sel[0]), "+v"(sel[1]));
      drain();
      flag(rs, fv, fs, lane < 4, tag);
      __builtin_amdgcn_s_setprio(0);
      if (!ra) lds_arrive(lb + L_CNT + 44, lane);
      WST(13);
      if (ra) {
        const int pb = PB_OFF + gofs * 8 + smul((w >> 1) * 8 + c, 9216) + ((4 * w + 2) & 7) * 1024, town = (w >> 1) == hb ? tt : -1;
        lds_waits(lb + L_CNT + 64 + 4 * w, layer + 1);
#pragma unroll
        for (int t = 0; t < 4; ++t)
          if (t != town) stx(lds128(lb + v16 + (tt ^ 3) * 1024 + (4 * w + t) * 4096), rs, v16, pb + t * (2 * 8 * 9216));
        WST(5);
        if (w == 0) { poll<2>(rs, lane, mbg + c * 1024 + MB_A * 8, 16, tag); lds_arrive(lb + L_CNT + 24, lane); }
        drain();
        flag(rs, lane * 2048, mbg + (w >> 1) * 1024 + (MB_B + 16 + 2 * c + (w & 1)) * 8, lane < 4, tag);
        WST(7);
        if (w == 0) {
          __builtin_amdgcn_s_sleep(PS1);
          poll<4>(rs, lane, mbg + c * 1024 + (MB_B + 16) * 8, 16, tag);
          lds_arrive(lb + L_CNT + 24, lane);
          WST(9);
          lds_waits(lb + L_CNT + 44, 4 * layer + 4);
          __builtin_amdgcn_s_sleep(PSL);
          poll<1>(rs, lane, mbg + c * 1024 + MB_B * 8, 16, tag);
          lds_arrive(lb + L_CNT + 24, lane);
        }
      }
#pragma unroll 1
      for (int rep = ra ? 0 : 1; rep < 2; ++rep) {
        if (ra && rep == 1) { slot = 3 + 4 * w; vs = v16 + slot * 1024; }
        const int u = slot == 8 ? 16 + hb : 8 * hb + slot;
        const bool on = !ra || (rep == 0 ? w > 0 : w < 2);
        v4u pv[7];
#pragma unroll
        for (int j = 0; j < 7; ++j) undef(pv[j]);
        if (on) {
          if (!ra || (NOEARLY && rep == 0)) lds_wait(lb + L_CNT + 44, 4 * layer + 4);
          lds_waits(lb + L_CNT + 24, 3 * layer + (ra ? 1 + 2 * rep : w < 6 ? 1 : 2));
          if (rep == 1) { WST(14); STAMP(13 + 16 * layer); } else WST(6);
#pragma unroll
          for (int j = 0; j < 7; ++j) pv[j] = ldx(rs, vs, po[j]);
        }
        SB();
        if (on) {
          const v4u xv = lds128(lb + u * 4096 + tt * 1024 + v16), ov = lds128(lb + u * 4096 + (tt ^ 1) * 1024 + v16);
          f32x4 ya = mfma(sel[0], ov, mfma(sel[0], xv, ldsf4(lb + L_TB2 + u * 128 + g * 32)));
          f32x4 yb = mfma(sel[1], ov, mfma(sel[1], xv, ldsf4(lb + L_TB2 + u * 128 + g * 32 + 16)));
#pragma unroll
          for (int j = 0; j < 7; ++j) { ya = mfma(sel[0], pv[j], ya); yb = mfma(sel[1], pv[j], yb); }
          if (HOLD && w != 1) lds_waits(lb + L_CNT + 24, 3 * layer + 3);
          if (layer == 0) stx(pk8(ya, yb), rs, vo, X2_OFF + gofs + u * 4096 + tt * 1024);
          else st128<0>(pk8(ya, yb), ro, vo, 64 * u);
        }
      }
      WST(15);
      if (layer == 0) {
        STAMP(14);
        drain();
        const bool nxt = c >= 6 && (G & 15) != 15;
        if (lds_ticket(lb + L_CNT + 56, lane) == 7) flag(rs, lane * 1024 + (lane & 8) * 2, mbg + (MB_X2 + c) * 8, lane < 8 || (nxt && lane < 16), tag);
      }
      WST(16);
    }
    STAMP(15 + 16 * layer);
    if (layer == 0) {
      FRESH();
      static_assert(CH1 == 6, "halo split");
      const int tt = w & 3;
      if (FW) lds_waits(lb + L_CNT + 56, 8);
      fill<0, 18>(ring, rs, v16, wq);
      int sm = X2_OFF + gofs + tt * 1024, sh = X2_OFF - 73728 + gofs + 3072 + tt * 4096;
      u32 la = lb + tt * 1024 + v16s + (ra ? 0 : 6 * 5120), lh = lb + 4096 + smul(tt + (ra ? 0 : 6), 5120) + v16s;
      asm volatile("" : "+s"(sm)); asm volatile("" : "+s"(sh)); asm volatile("" : "+v"(la), "+v"(lh));
      if (w == 7) poll<1>(rs, lane, mbg + c * 1024 + MB_X2 * 8, first ? 8 : 10, tag);
      bar();
      WST(17);
      STAMP(16);
      v4u xh[3];
#pragma unroll
      for (int i = 0; i < 3; ++i) xh[i] = v4u{0u, 0u, 0u, 0u};
      if (ra) {
        v4u xm[6];
#pragma unroll
        for (int i = 0; i < 6; ++i) xm[i] = ldx(rs, v16, sm + i * 4096);
        if (!first) { xh[0] = ldx(rs, v16, sh); if (tt < 2) xh[1] = ldx(rs, v16, sh + 16384); }
        SB();
#pragma unroll
        for (int i = 0; i < 6; ++i) sts128(la + i * 5120, xm[i]);
        sts128(lh, xh[0]);
        if (tt < 2) sts128(lh + 20480, xh[1]);
        lds_arrive(lb + L_CNT, lane);
      }
      int wb = w;
      asm volatile("" : "+s"(wb));
      if (wb >= 4) {
        __builtin_amdgcn_s_sleep(STSL);
        v4u xm[12];
#pragma unroll
        for (int i = 0; i < 12; ++i) xm[i] = ldx(rs, v16, sm + (6 + i) * 4096);
        if (!first) {
#pragma unroll
          for (int i = 0; i < 3; ++i) xh[i] = ldx(rs, v16, sh + (6 + 4 * i) * 4096);
        }
        SB();
#pragma unroll
        for (int i = 0; i < 12; ++i) sts128(la + i * 5120, xm[i]);
#pragma unroll
        for (int i = 0; i < 3; ++i) sts128(lh + i * 20480, xh[i]);
        lds_arrive(lb + L_CNT + 40, lane);
      }
    }
#pragma unroll
    for (int j = 0; j < 4; ++j) undef(mt[j]);
  }
  if (bid == 0 && tid == 448) stx2(tag, rs, 0, EP_OFF);
  if (w_ == 1) { while (__builtin_amdgcn_s_memrealtime() - prb < 10000ull) __builtin_amdgcn_s_sleep(1); }
}

extern "C" void kernel_launch(void* const* d_in, const int* in_sizes, int n_in, void* d_out, int out_size, void* d_ws, size_t ws_size,
                              hipStream_t stream) {
  const u16* x = (const u16*)d_in[0];
  const u16* wqkv = (const u16*)d_in[1];
  const u16* wo = (const u16*)d_in[2];
  const u16* w1 = (const u16*)d_in[4];
  const u16* b1 = (const u16*)d_in[5];
  const u16* w2 = (const u16*)d_in[6];
  const u16* b2 = (const u16*)d_in[7];
  (void)hipFuncSetAttribute((const void*)k_main, hipFuncAttributeMaxDynamicSharedMemorySize, L_TOTAL);
  k_main<<<256, 512, L_TOTAL, stream>>>(x, wqkv, wo, w1, b1, w2, b2, (u16*)d_out, (char*)d_ws);
}
```

```cpp
#include <hip/hip_runtime.h>
#include <hip/hip_bf16.h>

typedef unsigned short u16;
typedef unsigned u32;
typedef unsigned long long u64;
typedef u32 v4u __attribute__((ext_vector_type(4)));
typedef u32 v2u __attribute__((ext_vector_type(2)));
typedef float f32x4 __attribute__((ext_vector_type(4)));
typedef float f32x2 __attribute__((ext_vector_type(2)));
typedef short s16x4 __attribute__((ext_vector_type(4)));
typedef __bf16 bf16x2 __attribute__((ext_vector_type(2)));
typedef __bf16 bf16x8 __attribute__((ext_vector_type(8)));
#define GAS __attribute__((address_space(1)))
#define LAS __attribute__((address_space(3)))
#define DI __device__ __forceinline__
#define RLX __ATOMIC_RELAXED, __HIP_MEMORY_SCOPE_AGENT

#define XG 2
#define AUX1 6
#define AUX2 6
#define AUX3 12
#define BF1 12
#define PSL 6
#define BSL 24
#define B1LATE 1
#define HOLD 0
#define FW 1
#define APRIO 3
#define APOLL 1
#define PD0 0
#define MBW 1
#define PS1 8
#define NOEARLY 0
#define CH1 6
#define SIGK 14
#define STSL 3
#define STAMPS 0

#define CODE_BYTES 55296
constexpr int EP_OFF = 0;
constexpr int MB_OFF = 4096, MB_BYTES = 3 * 256 * 1024;
constexpr int MB_O = 0, MB_X1 = 16, MB_A = 32, MB_B = 48, MB_X2 = 80, MB_WQ = 0, MB_W1 = 32, MB_W2 = 64;
constexpr int CSQ_OFF = MB_OFF + MB_BYTES;
constexpr int CS1_OFF = CSQ_OFF + 8 * 18432;
constexpr int WQ_OFF = CS1_OFF + 8 * 20736;
constexpr int WO_OFF = WQ_OFF + 64 * 36864;
constexpr int WM_OFF = WO_OFF + 80 * 18432;
constexpr int OB_OFF = WM_OFF + 64 * 165888;
constexpr int X1_OFF = OB_OFF + 64 * 73728;
constexpr int X2_OFF = X1_OFF + 64 * 73728;
constexpr int PB_OFF = X2_OFF + 32 * 73728;
constexpr int ST_OFF = PB_OFF + 2 * 32 * 64 * 9216;
constexpr int WS_USED = ST_OFF + 2 * 256 * 32 * 8 + 256 * 2048;
constexpr int WS_DUMP = WS_USED;
constexpr int L_E = 92160, L_OI = 92160, L_H = 73728;
constexpr int L_TQ = 92160, L_TC = L_TQ + 18432 + 6 * 4608;
constexpr int L_PX = 129024;
constexpr int L_SS = 147456;
constexpr int L_RK = L_SS + 1024;
constexpr int L_CNT = L_RK + 2048;
constexpr int L_TA = L_CNT + 96;
constexpr int L_TB2 = L_TA + 4608;
constexpr int L_TCQ = L_TB2 + 2304;
constexpr int L_DBG = L_TCQ + 1024;
constexpr int L_TOTAL = L_DBG + (STAMPS ? 2048 : 0);

DI float bf2f(u16 b) { return __builtin_bit_cast(float, (u32)((u32)b) << 16); }
DI float lo2f(u32 x) { return __builtin_bit_cast(float, (u32)x << 16); }
DI float hi2f(u32 x) { return __builtin_bit_cast(float, (u32)x & 0xffff0000u); }
DI u32 pk(float a, float b) { f32x2 f = {a, b}; return __builtin_bit_cast(u32, __builtin_convertvector(f, bf16x2)); }
DI f32x4 mfma(v4u a, v4u b, f32x4 c) {
  return __builtin_amdgcn_mfma_f32_16x16x32_bf16(__builtin_bit_cast(bf16x8, a), __builtin_bit_cast(bf16x8, b), c, 0, 0, 0);
}
DI void mm(f32x4& c, v4u a, v4u b) { asm volatile("v_mfma_f32_16x16x32_bf16 %0, %1, %2, %0" : "+v"(c) : "v"(a), "v"(b)); }
DI void mm0(f32x4& c, v4u a, v4u b) { asm volatile("v_mfma_f32_16x16x32_bf16 %0, %1, %2, 0" : "=&v"(c) : "v"(a), "v"(b)); }
DI void mmk16(f32x4& c, v2u a, v2u b) { asm volatile("v_mfma_f32_16x16x16_bf16 %0, %1, %2, %0" : "+v"(c) : "v"(a), "v"(b)); }
DI void mm_end() { __builtin_amdgcn_sched_barrier(0); asm volatile("s_nop 7\n\ts_nop 1" ::: "memory"); __builtin_amdgcn_sched_barrier(0); }
DI float xsum(float v) {
  u32 x = __builtin_bit_cast(u32, v);
  auto a = __builtin_amdgcn_permlane16_swap(x, x, false, false);
  v = __builtin_bit_cast(float, (u32)a[0]) + __builtin_bit_cast(float, (u32)a[1]);
  x = __builtin_bit_cast(u32, v);
  auto b = __builtin_amdgcn_permlane32_swap(x, x, false, false);
  return __builtin_bit_cast(float, (u32)b[0]) + __builtin_bit_cast(float, (u32)b[1]);
}
DI float xmax(float v) {
  u32 x = __builtin_bit_cast(u32, v);
  auto a = __builtin_amdgcn_permlane16_swap(x, x, false, false);
  v = __builtin_fmaxf(__builtin_bit_cast(float, (u32)a[0]), __builtin_bit_cast(float, (u32)a[1]));
  x = __builtin_bit_cast(u32, v);
  auto b = __builtin_amdgcn_permlane32_swap(x, x, false, false);
  return __builtin_fmaxf(__builtin_bit_cast(float, (u32)b[0]), __builtin_bit_cast(float, (u32)b[1]));
}
#define SB() __builtin_amdgcn_sched_barrier(0)
#define INL __attribute__((always_inline))
DI int smul(int a, int k) { int r; asm("s_mul_i32 %0, %1, %2" : "=s"(r) : "s"(a), "s"(k)); return r; }
DI void bar() {
  asm volatile("s_waitcnt lgkmcnt(0)" ::: "memory");
  __builtin_amdgcn_s_barrier();
  asm volatile("" ::: "memory");
}
typedef __amdgpu_buffer_rsrc_t rsrc_t;
#define WAUX 16
DI v4u ldw(rsrc_t rs, int v, int s) { return __builtin_amdgcn_raw_buffer_load_b128(rs, v, s, WAUX); }
DI v4u ldp(rsrc_t rs, int v, int s) { return __builtin_amdgcn_raw_buffer_load_b128(rs, v, s, 0); }
constexpr int OOB = 0x70000000;
DI v4u ldx(rsrc_t rs, int v, int s) { return __builtin_amdgcn_raw_buffer_load_b128(rs, v, s, 16); }
DI v2u ldx2(rsrc_t rs, int v, int s) { return __builtin_amdgcn_raw_buffer_load_b64(rs, v, s, 16); }
template <int AUX> DI void st128(v4u d, rsrc_t rs, int v, int s) { __builtin_amdgcn_raw_buffer_store_b128(d, rs, v, s, AUX); asm volatile("s_nop 1" :: "v"(d)); }
DI void stx(v4u d, rsrc_t rs, int v, int s) { st128<16>(d, rs, v, s); }
DI void stx2(v2u d, rsrc_t rs, int v, int s) { __builtin_amdgcn_raw_buffer_store_b64(d, rs, v, s, 16); }
DI v4u lds128(u32 a) { return *(const LAS v4u*)(size_t)a; }
DI v2u lds64(u32 a) { return *(const LAS v2u*)(size_t)a; }
DI f32x4 ldsf4(u32 a) { return *(const LAS f32x4*)(size_t)a; }
DI f32x2 ldsf2(u32 a) { return *(const LAS f32x2*)(size_t)a; }
DI void sts128(u32 a, v4u v) { *(LAS v4u*)(size_t)a = v; }
DI void sts64(u32 a, v2u v) { *(LAS v2u*)(size_t)a = v; }
DI v2u ldstr(u32 a) { return __builtin_bit_cast(v2u, __builtin_amdgcn_ds_read_tr16_b64_v4i16((LAS s16x4*)(size_t)a)); }

__host__ __device__ __forceinline__ int colq(int c, int w, int t, int m) {
  if (w < 4 || t == 0) {
    const int h = w & 3, sec = w < 4 ? (t == 0 ? 1 : 0) : 2;
    return sec * 576 + 72 * c + 36 * (h >> 1) + 20 * (h & 1) + m;
  }
  const int qd = m >> 2;
  if (w < 6) return (qd & 1) * 576 + 72 * c + 36 * (qd >> 1) + 16 + (m & 3);
  return 2 * 576 + 72 * c + 36 * (qd & 1) + 16 + (m & 3);
}
__host__ __device__ __forceinline__ int krot(int c) { return 0; }
__host__ __device__ __forceinline__ int colp(int u, int e, int m) { return 32 * u + 8 * (m >> 2) + 4 * e + (m & 3); }
__host__ __device__ __forceinline__ int rowo(int ks, int g, int j) {
  if (ks < 16) return 72 * (ks >> 1) + 36 * (ks & 1) + 20 * (j >> 2) + 4 * g + (j & 3);
  return 72 * (4 * (ks - 16) + g) + 36 * (j >> 2) + 16 + (j & 3);
}
DI void lds_arrive(u32 a, int lane) {
  asm volatile("s_waitcnt lgkmcnt(0)" ::: "memory");
  if (lane == 0) __hip_atomic_fetch_add((LAS u32*)(size_t)a, 1u, __ATOMIC_RELAXED, __HIP_MEMORY_SCOPE_WORKGROUP);
}
DI void lds_waits(u32 a, u32 n) {
  while ((u32)__builtin_amdgcn_readfirstlane(*(volatile LAS u32*)(size_t)a) < n) __builtin_amdgcn_s_sleep(2);
  asm volatile("" ::: "memory");
}
DI void lds_wait(u32 a, u32 n) {
  while ((u32)__builtin_amdgcn_readfirstlane(*(volatile LAS u32*)(size_t)a) < n) {}
  asm volatile("" ::: "memory");
}
#define REFILL(f_, NU) do { const int q_ = (f_) % 18, u_ = (f_) / 18; \
    if (u_ < (NU) - 1) ring[q_] = ldw(rs, v16 + (q_ & 3) * 1024, s0 + u_ * 18432 + (q_ >> 2) * 4096); \
    else if (LAST == 1) ring[q_] = ldw(rs, v16 + (q_ & 3) * 1024, slast + (q_ >> 2) * 4096); } while (0)
template <int NT, int MIDBAR, int LAST, class MID, class PRE>
DI void group(f32x4 (&acc)[NT][4], v4u (&ring)[18], u32 xa, rsrc_t rs, int v16, int s0, int slast, MID&& mid, PRE&& pre) {
  v4u xf[2][4];
  u32 xb = xa + 9 * 4096;
  asm volatile("" : "+v"(xa), "+v"(xb));
#pragma unroll
  for (int t = 0; t < 4; ++t) xf[0][t] = lds128(xa + t * 1024);
  SB();
#pragma unroll
  for (int ks = 0; ks < 18; ++ks) {
    const int kn = ks + 1;
#pragma unroll
    for (int n = 0; n < NT; ++n) {
      const int f = ks * NT + n;
#pragma unroll
      for (int t = 0; t < 4; ++t) {
        if (ks == 0) mm0(acc[n][t], ring[f % 18], xf[0][t]); else mm(acc[n][t], ring[f % 18], xf[ks & 1][t]);
        if (t == 0 && f > 0) REFILL(f - 1, NT);
        const int n0 = kn == MIDBAR ? NT - 2 : 0;
        const int q = n == n0 ? t - 1 : (n == n0 + 1 && t == 1 ? 3 : -1);
        if (q == 0 && kn == MIDBAR) { mid(); SB(); }
        if (q >= 0 && kn < 18) xf[kn & 1][q] = kn < 9 ? lds128(xa + kn * 4096 + q * 1024) : lds128(xb + (kn - 9) * 4096 + q * 1024);
        if (kn == 18 && n == 0 && t == 1) pre();
        SB();
      }
    }
  }
  REFILL(18 * NT - 1, NT);
  SB();
}
template <bool REF, class MID>
DI void group36(f32x4 (&acc)[2][4], v4u (&r0)[18], v4u (&r1)[18], u32 xa, rsrc_t rs, int v16, int sn, MID&& mid) {
  v4u xf[2][4];
  u32 xb = xa + 9 * 4096;
  asm volatile("" : "+v"(xa), "+v"(xb));
#pragma unroll
  for (int t = 0; t < 4; ++t) xf[0][t] = lds128(xa + t * 1024);
  SB();
#pragma unroll
  for (int f = 0; f < 36; ++f) {
    const int ks = f >> 1, n = f & 1, kn = ks + 1;
#pragma unroll
    for (int t = 0; t < 4; ++t) {
      if (ks == 0) mm0(acc[n][t], r0[f], xf[0][t]); else mm(acc[n][t], f < 18 ? r0[f % 18] : r1[f % 18], xf[ks & 1][t]);
      if (REF && t == 0 && f > 0) (f - 1 < 18 ? r0[(f - 1) % 18] : r1[(f - 1) % 18]) = ldw(rs, v16 + ((f - 1) & 3) * 1024, sn + ((f - 1) >> 2) * 4096);
      const int q = n == 0 ? t - 1 : (t == 1 ? 3 : -1);
      if (q >= 0 && kn < 18) xf[kn & 1][q] = kn < 9 ? lds128(xa + kn * 4096 + q * 1024) : lds128(xb + (kn - 9) * 4096 + q * 1024);
      if (f == 2 * SIGK + 1 && t == 2) mid();
      SB();
    }
  }
  mm_end();
  if (REF) r1[17] = ldw(rs, v16 + 3072, sn + 8 * 4096);
  SB();
}
DI void groupq(f32x4 (&a0)[5], f32x4 (&a1)[4], v4u (&ring)[18], u32 x0, u32 x1, u32 x4, rsrc_t rs, int v16, int s0, int slast, u32 cnt, u32 want) {
  constexpr int LAST = 1;
  v4u xf[2][5];
  u32 y0 = x0 + 46080, y1 = x1 + 46080, y4 = x4 + 46080;
  asm volatile("" : "+v"(y0), "+v"(y1), "+v"(y4));
  xf[0][0] = lds128(x0); xf[0][1] = lds128(x1); xf[0][2] = lds128(x1 + 1024); xf[0][3] = lds128(x1 + 2048); xf[0][4] = lds128(x4);
  SB();
#pragma unroll
  for (int ks = 0; ks < 18; ++ks) {
    const int sl = (2 * ks) % 18, kn = ks + 1, o = (kn % 9) * 5120;
#pragma unroll
    for (int t = 0; t < 5; ++t) {
      if (ks == 0) mm0(a0[t], ring[sl], xf[0][t]); else mm(a0[t], ring[sl], xf[ks & 1][t]);
      if (t == 0 && ks > 0) REFILL(2 * ks - 1, 2);
      if (t == 1 && kn == CH1) { lds_wait(cnt, want); SB(); }
      if (t > 0 && kn < 18) xf[kn & 1][t - 1] = lds128(t == 1 ? (kn < 9 ? x0 : y0) + o : (kn < 9 ? x1 : y1) + o + (t - 2) * 1024);
      SB();
    }
#pragma unroll
    for (int t = 0; t < 4; ++t) {
      if (ks == 0) mm0(a1[t], ring[sl + 1], xf[0][t]); else mm(a1[t], ring[sl + 1], xf[ks & 1][t]);
      if (t == 0) REFILL(2 * ks, 2);
      if (t == 1 && kn < 18) xf[kn & 1][4] = lds128((kn < 9 ? x4 : y4) + o);
      SB();
    }
  }
  mm_end();
  REFILL(35, 2);
  SB();
}
DI void groupo(f32x4 (&a)[5], const v4u (&ring)[18], const v4u (&ex)[6], u32 x0, u32 x1, u32 xe) {
  v4u xf[3][2], ef[6];
  asm volatile("" : "+v"(x0), "+v"(x1), "+v"(xe));
#pragma unroll
  for (int k = 0; k < 2; ++k) { xf[k][0] = lds128(x0 + k * 2048); xf[k][1] = lds128(x1 + k * 2048); }
  SB();
#pragma unroll
  for (int ks = 0; ks < 9; ++ks) {
    const int kn = ks + 2;
#pragma unroll
    for (int j = 0; j < 4; ++j) {
      if (ks == 0) mm0(a[j], ring[2 * ks + (j >> 1)], xf[0][j & 1]); else mm(a[j], ring[2 * ks + (j >> 1)], xf[ks % 3][j & 1]);
      if (kn < 9 && j < 2) xf[kn % 3][j] = lds128((j ? x1 : x0) + kn * 2048);
      if (kn >= 9 && j < 3) ef[3 * (kn - 9) + j] = lds128(xe + (3 * (kn - 9) + j) * 2048);
      SB();
    }
  }
#pragma unroll
  for (int k = 0; k < 6; ++k) { if (k == 0) mm0(a[4], ex[0], ef[0]); else mm(a[4], ex[k], ef[k]); SB(); }
  mm_end();
}
template <int LO, int HI> DI void fill(v4u (&ring)[18], rsrc_t rs, int v16, int s) {
  SB();
#pragma unroll
  for (int i = LO; i < HI; ++i) ring[i] = ldw(rs, v16 + (i & 3) * 1024, s + (i >> 2) * 4096);
  SB();
}
template <class T> DI void undef(T& v) { asm volatile("" : "=v"(v)); }
DI void stat_acc(v4u v, f32x4& s, f32x4& q) { s = mfma(v4u{0x3f803f80u, 0x3f803f80u, 0x3f803f80u, 0x3f803f80u}, v, s); q = mfma(v, v, q); }
DI float diag(f32x4 q, int g, int tl) {
  const float a = (tl & 1) ? q[1] : q[0], b = (tl & 1) ? q[3] : q[2];
  return xsum(g == (tl >> 2) ? ((tl & 2) ? b : a) : 0.f);
}
DI void split3(float x, u32& h, u32& m, u32& l) {
  h = pk(x, 0.f); x -= lo2f(h);
  m = pk(x, 0.f); x -= lo2f(m);
  l = pk(x, 0.f);
}
struct RowK { f32x4 k[4]; v2u aug[4]; };
template <int N> DI void gelu(float (&v)[N], f32x4 k) {
  float y[N];
#pragma unroll
  for (int r = 0; r < N; ++r) y[r] = v[r] * v[r];
  SB();
#pragma unroll
  for (int r = 0; r < N; ++r) y[r] = __builtin_fmaf(y[r], k[1], k[0]);
  SB();
#pragma unroll
  for (int r = 0; r < N; ++r) y[r] = v[r] * y[r];
  SB();
#pragma unroll
  for (int r = 0; r < N; ++r) y[r] = __builtin_amdgcn_exp2f(y[r]);
  SB();
#pragma unroll
  for (int r = 0; r < N; ++r) y[r] = __builtin_fmaf(y[r], k[2], k[2]);
  SB();
#pragma unroll
  for (int r = 0; r < N; ++r) y[r] = __builtin_amdgcn_rcpf(y[r]);
  SB();
#pragma unroll
  for (int r = 0; r < N; ++r) v[r] = v[r] * y[r];
  SB();
}
DI v2u gelu4(f32x4 a, f32x4 k) {
  float v[4] = {a[0], a[1], a[2], a[3]};
  gelu<4>(v, k);
  return v2u{pk(v[0], v[1]), pk(v[2], v[3])};
}
DI void aug_tile(f32x4 (&a)[4], v2u ta, const RowK& k) {
#pragma unroll
  for (int t = 0; t < 4; ++t) mmk16(a[t], ta, k.aug[t]);
}
DI void gelu_pair(f32x4 (&a0)[4], f32x4 (&a1)[4], u32 hp, const RowK& k) {
#pragma unroll
  for (int t = 0; t < 4; ++t) {
    float v[8] = {a0[t][0], a0[t][1], a0[t][2], a0[t][3], a1[t][0], a1[t][1], a1[t][2], a1[t][3]};
    gelu<8>(v, k.k[t]);
    sts128(hp + t * 1024, v4u{pk(v[0], v[1]), pk(v[2], v[3]), pk(v[4], v[5]), pk(v[6], v[7])});
  }
}
DI v4u pk8(f32x4 a, f32x4 b) { return v4u{pk(a[0], a[1]), pk(a[2], a[3]), pk(b[0], b[1]), pk(b[2], b[3])}; }
DI void part_pair(const f32x4 (&a0)[4], const f32x4 (&a1)[4], rsrc_t rs, int v16, int sb, int town, u32 lown) {
#pragma unroll
  for (int t = 0; t < 4; ++t) {
    const v4u d = pk8(a0[t], a1[t]);
    if (t == town) sts128(lown, d); else stx(d, rs, v16, sb + t * (2 * 8 * 9216));
  }
}
DI void make_sel(v4u (&sel)[2], int g, int tl) {
  const u32 one = g == (tl >> 2) ? ((tl & 1) ? 0x3f800000u : 0x00003f80u) : 0u, lo = (tl & 2) ? 0u : one, hi = (tl & 2) ? one : 0u;
  sel[0] = v4u{lo, hi, 0u, 0u};
  sel[1] = v4u{0u, 0u, lo, hi};
}
typedef GAS u64 gu64;
DI void drain() { asm volatile("s_waitcnt vmcnt(0) lgkmcnt(0)" ::: "memory"); }
DI void flag(rsrc_t rs, int v, int s, bool on, v2u tag) { if (on) stx2(tag, rs, v, s); }
template <int SL> DI void poll(rsrc_t rs, int lane, int s, int n, v2u tag) {
  if (lane < n) {
#pragma unroll 1
    for (u32 sp = 0; sp < (1u << 16); ++sp) {
      v2u x = {0u, 0u};
      asm volatile("buffer_atomic_or_x2 %0, %1, %2, %3 offen sc0\n\ts_waitcnt vmcnt(0)" : "+v"(x) : "v"(lane * 8), "s"(rs), "s"(s) : "memory");
      if (__builtin_amdgcn_ballot_w64(((x[0] ^ tag[0]) | (x[1] ^ tag[1])) != 0u) == 0) break;
      if (SL) __builtin_amdgcn_s_sleep(SL);
      asm volatile("" ::: "memory");
    }
  }
  asm volatile("" ::: "memory");
}
DI u32 lds_ticket(u32 a, int lane) {
  u32 r = 0;
  if (lane == 0) r = __hip_atomic_fetch_add((LAS u32*)(size_t)a, 1u, __ATOMIC_RELAXED, __HIP_MEMORY_SCOPE_WORKGROUP);
  return __builtin_amdgcn_readfirstlane(r);
}
DI void tile_tr(const v4u* m, v4u (&f)[4], u32 lw, int lane) {
  const u32 wr = lw + (lane >> 3) * 144 + (lane & 7) * 16, tr = lw + (8 * (lane >> 4) + ((lane & 15) >> 2)) * 144 + (lane & 3) * 16;
#pragma unroll
  for (int j = 0; j < 4; ++j) sts128(wr + j * 8 * 144, m[j]);
  asm volatile("s_waitcnt lgkmcnt(0)" ::: "memory");
#pragma unroll
  for (int q = 0; q < 4; ++q) {
    const v2u lo = ldstr(tr + (q >> 1) * 64 + (q & 1) * 8), hi = ldstr(tr + (q >> 1) * 64 + (q & 1) * 8 + 4 * 144);
    f[q] = v4u{lo[0], lo[1], hi[0], hi[1]};
  }
  asm volatile("s_waitcnt lgkmcnt(0)" : "+v"(f[0]), "+v"(f[1]), "+v"(f[2]), "+v"(f[3]) :: "memory");
}
template <int MT> DI void mlp_dest(int c, int cb, int q, int& so, int& st) {
  const int u = 2 * cb + (q >> 1), e = q & 1;
  int wd, y0;
  if (MT == 0) {
    wd = u < 8 ? u : u < 16 ? u - 8 : 4 + q;
    y0 = u < 8 ? e : u < 16 ? 36 + e : 38;
    st = u < 12 ? 2048 : 3072;
  } else {
    const bool sp = cb == 8, fa = (cb & 1) == 0;
    wd = sp ? q : fa ? cb >> 1 : 4 + (cb >> 1);
    y0 = sp ? 76 : fa ? 72 + q : 90 + 36 * (q >> 1) + e;
    st = fa ? 5120 : 2048;
  }
  so = WM_OFF + smul(c * 8 + wd, 165888) + y0 * 1024;
}
#define FRESH() \
  int lane = lane_, w = w_, G = G_, c = c_; \
  asm volatile("" : "+s"(w)); asm volatile("" : "+s"(G)); asm volatile("" : "+s"(c)); asm volatile("" : "+v"(lane)); \
  const int g = lane >> 4, tl = lane & 15, v16 = lane * 16, v16s = (lane ^ g) * 16; \
  const int wq = WQ_OFF + smul(c * 8 + w, 36864), wm = WM_OFF + smul(c * 8 + w, 165888), woc = WO_OFF + smul(c, 184320); \
  const int us = (c & 4) ? 13 : 4, ub = (c & 2) ? us + 1 : us - 4; \
  const bool first = (G & 15) == 0, ra = w < 4; \
  const int rowbase = 2 * 64 * (G & 15) + (G >> 4), gofs = smul(layer * 32 + G, 73728), mbg = MB_OFF + (layer * 256 + G * 8) * 1024; \
  (void)g; (void)tl; (void)v16; (void)v16s; (void)wq; (void)wm; (void)woc; (void)ub; (void)us; (void)first; (void)ra; (void)rowbase; (void)gofs; (void)mbg;
#define STAMP(i)
#define WST(i)

#if defined(__HIP_DEVICE_COMPILE__)
#define NOPK __attribute__((target("no-packed-fp32-ops")))
#else
#define NOPK
#endif
__global__ NOPK void __launch_bounds__(512, 2) k_main(const u16* __restrict__ x, const u16* __restrict__ wqkv, const u16* __restrict__ wo,
    const u16* __restrict__ w1, const u16* __restrict__ b1, const u16* __restrict__ w2, const u16* __restrict__ b2, u16* __restrict__ out, char* __restrict__ ws) {
  extern __shared__ __attribute__((aligned(16))) char smem[];
  const u32 lb = (u32)(size_t)(LAS char*)smem;
  u32 codew;
  {
    u64 pc;
    asm volatile("s_getpc_b64 %0" : "=s"(pc));
    const u32 t = __builtin_amdgcn_workitem_id_x(), ln = (__builtin_amdgcn_workgroup_id_x() & ~7u) + (t >> 6) + 256 * (t & 1);
    const GAS u32* cp = (const GAS u32*)((pc & ~255ull) + (ln * 128 < CODE_BYTES ? ln * 128 : 0));
    asm volatile("global_load_dword %0, %1, off" : "=v"(codew) : "v"(cp) : "memory");
  }
  asm volatile("" :: "s"(x), "s"(wqkv), "s"(wo), "s"(w1), "s"(b2), "s"(ws));
  const int tid = __builtin_amdgcn_workitem_id_x(), bid = __builtin_amdgcn_workgroup_id_x(), lane_ = tid & 63, w_ = __builtin_amdgcn_readfirstlane(tid >> 6);
  int G_, c_;
  {
    const int xc = bid & 7, s = bid >> 3;
    if (XG == 8) { c_ = xc; G_ = s; }
    else if (XG == 1) { G_ = 4 * xc + (s >> 3); c_ = s & 7; }
    else if (XG == 2) { G_ = 8 * (xc >> 1) + (s >> 2); c_ = 4 * (xc & 1) + (s & 3); }
    else { G_ = 16 * (xc >> 2) + (s >> 1); c_ = 2 * (xc & 3) + (s & 1); }
  }
  STAMP(0);
  const rsrc_t rs = __builtin_amdgcn_make_buffer_rsrc((void*)ws, 0, WS_USED, 0x00020000);
  const rsrc_t rx = __builtin_amdgcn_make_buffer_rsrc((void*)x, 0, 2048 * 1152, 0x00020000);
  const rsrc_t ro = __builtin_amdgcn_make_buffer_rsrc((void*)out, 0, 2048 * 1152, 0x00020000);
  v2u tag;
  v4u ring[18], mt[4];
  {
    const int layer = 2;
    FRESH();
    WST(32);
    if (tid < 24) *(LAS u32*)(size_t)(lb + L_CNT + 4 * tid) = 0u;
    const int r8 = lane >> 3, j8 = lane & 7, tt = w & 3;
    const bool isq = G < 18;
    const v4u ones = {0x3f803f80u, 0x3f803f80u, 0x3f803f80u, 0x3f803f80u};
    const u32 lw = lb + L_TQ + 18432 + w * 4608;
    if (ra && isq) {
      const int hi = lane >= 27, r = lane - 27 * hi, sec = (r >= 9) + (r >= 18);
      const int vo = (hi * 3456 + sec * (1152 - 144) + r * 16) | (lane < 54 ? 0 : OOB), s0 = smul(32 * G + 8 * w, 3456) + smul(c, 144);
      const rsrc_t rq = __builtin_amdgcn_make_buffer_rsrc((void*)wqkv, 0, 576 * 3456, 0x00020000);
      v4u a[4], f[4];
#pragma unroll
      for (int j = 0; j < 4; ++j) a[j] = ldp(rq, vo, s0 + j * (2 * 3456));
      SB();
      WST(33);
      bar();
      const u32 la = lb + L_TQ + (lane < 54 ? w * 3584 : 14336) + hi * 448 + r * 16;
      const u32 tb = lb + L_TQ + (8 * g + (tl >> 2)) * 448, mo = tb + 72 * (w >> 1) + 40 * (w & 1) + 8 * (tl & 3);
      const u32 ro_ = tb + 32 + (w < 2 ? 0 : 288) + (tl & 1) * (w < 2 ? 144 : 72) + (tl & 2) * (w < 2 ? 36 : 0);
#pragma unroll
      for (int j = 0; j < 4; ++j) sts128(la + j * 896, a[j]);
      lds_arrive(lb + L_CNT + 12, lane);
      lds_wait(lb + L_CNT + 12, 4);
      WST(35);
#pragma unroll
      for (int j = 0; j < 4; ++j) {
        const u32 ad = j == 0 ? mo + 144 : j == 1 ? mo : j == 2 ? mo + 288 : ro_;
        const v2u lo = ldstr(ad), hi_ = ldstr(ad + 4 * 448);
        f[j] = v4u{lo[0], lo[1], hi_[0], hi_[1]};
      }
#pragma unroll
      for (int j = 0; j < 4; ++j) stx(f[j], rs, v16, wq + G * 2048 + (j & 1) * 1024 + (j >> 1) * (4 * 36864));
      f32x4 cs[4];
#pragma unroll
      for (int j = 0; j < 4; ++j) cs[j] = mfma(f[j], ones, f32x4{0.f, 0.f, 0.f, 0.f});
      const f32x4 d = tl == 0 ? cs[0] : tl == 1 ? cs[1] : tl == 2 ? cs[2] : cs[3];
      stx(__builtin_bit_cast(v4u, d), rs, tl < 4 ? (tl >> 1) * 512 + (tl & 1) * 64 + g * 16 : OOB, CSQ_OFF + smul(c, 18432) + G * 1024 + w * 128);
    }
    if (ra && !isq) {
      const int ti = (G - 18) * 4 + w, tk = smul(ti, 43) >> 7, tp = ti - smul(tk, 3);
      bar();
      if (ti < 54) {
        const int vo = ((r8 >> 2) * (tk < 16 ? 20 : 36) + (r8 & 3)) * 1152 + j8 * 16;
        const int s0 = smul(tk, tk < 16 ? 36 * 1152 : 288 * 1152) - (tk < 16 ? 0 : (16 * 288 - 16) * 1152) + (tp < 2 ? ub + 2 * tp : us) * 64, ss = tk < 16 ? 4 * 1152 : 72 * 1152;
        const rsrc_t rw = __builtin_amdgcn_make_buffer_rsrc((void*)wo, 0, 576 * 1152, 0x00020000);
        v4u a[4], f[4];
        a[0] = ldp(rw, vo, s0); a[1] = ldp(rw, vo, s0 + ss); a[2] = ldp(rw, vo, s0 + 2 * ss); a[3] = ldp(rw, vo, s0 + 2 * ss + ss);
        SB();
        WST(33);
        tile_tr(a, f, lw, lane);
        const int so = woc + (tp < 2 ? smul(tp, 2 * 18432) + tk * 2048 + (tk < 9 ? 0 : 3 * 18432) : 8 * 18432 + tk * 1024);
        stx(f[0], rs, v16, so); stx(f[1], rs, v16, so + (tp < 2 ? 1024 : 18432));
        if (tp < 2) { stx(f[2], rs, v16, so + 18432); stx(f[3], rs, v16, so + 18432 + 1024); }
      }
    }
    if (!ra) {
      const int n = (c & 3) * 162 + tt * 54 + lane, row = (n * 7282) >> 16, ln = n - 9 * row;
      bar();
      u32 t0 = 0;
      if (w < 7) t0 = __builtin_amdgcn_raw_buffer_load_b32(rx, lane < 54 && !(first && row < 8) ? (rowbase + 2 * (row - 8)) * 1152 + ln * 128 : OOB, 0, 0);
      else {
        const u64 ep = __hip_atomic_load((gu64*)(ws + EP_OFF), RLX);
        {
          const u32 z = 0, mo = lane < 48 ? (lane >> 4) * (256 * 1024) + (lane & 15) * 64 : OOB;
          asm volatile("buffer_atomic_or %0, %1, %2, %3 offen" :: "v"(z), "v"(mo), "s"(rs), "s"(MB_OFF + (G * 8 + c) * 1024) : "memory");
        }
        *(LAS u64*)(size_t)(lb + L_CNT + 32) = ep + 1;
        lds_arrive(lb + L_CNT + 28, lane);
      }
      const u32 bv = b2[tid - 256], bv2 = b2[tid], bv3 = b2[512 + lane];
      SB();
      WST(33);
      const int vx = (rowbase + 32 * tt + 2 * r8) * 1152 + j8 * 16, vh = first ? OOB : (rowbase - 16 + 2 * r8) * 1152 + j8 * 16 + tt * 128;
      u32 lx = lb + (j8 >> 2) * 5120 + ((j8 & 3) * 16 + (r8 ^ (j8 & 3))) * 16, lh = lx + 4096 + tt * 10240;
      lx += tt * 1024;
      u32 ly = lx + 5 * 10240;
      asm volatile("" : "+v"(lx), "+v"(ly));
      *(LAS u32*)(size_t)(lb + L_TB2 + 4 * (tid - 256)) = bv << 16;
      *(LAS u32*)(size_t)(lb + L_TB2 + 4 * tid) = bv2 << 16;
      *(LAS u32*)(size_t)(lb + L_TB2 + 2048 + 4 * lane) = bv3 << 16;
      asm volatile("" :: "v"(t0) : "memory");
      SB();
      WST(35);
      v4u xv[18], xh[3];
#pragma unroll
      for (int i = 0; i < 9; ++i) { xv[2 * i] = ldp(rx, vx + i * 128, 0); xv[2 * i + 1] = ldp(rx, vx + i * 128, 16 * 1152); }
      xh[0] = ldp(rx, vh, 0); xh[1] = ldp(rx, vh + 512, 0);
      undef(xh[2]);
      if (tt == 0) xh[2] = ldp(rx, vh + 1024, 0);
      SB();
#pragma unroll
      for (int i = 0; i < 18; ++i) sts128((i < 10 ? lx + (i >> 1) * 10240 : ly + ((i >> 1) - 5) * 10240) + (i & 1) * 128, xv[i]);
      sts128(lh, xh[0]); sts128(lh + 128, xh[0]); sts128(lh + 40960, xh[1]); sts128(lh + 40960 + 128, xh[1]);
      if (tt == 0) { sts128(lh + 81920, xh[2]); sts128(lh + 81920 + 128, xh[2]); }
      lds_arrive(lb + L_CNT, lane);
      lds_arrive(lb + L_CNT + 40, lane);
    }
    WST(36);
    drain();
    asm volatile("" :: "v"(codew));
    WST(37);
    lds_wait(lb + L_CNT + 28, 1);
    tag = lds64(lb + L_CNT + 32);
    if (ra && lds_ticket(lb + L_CNT + 20, lane) == 3) {
      flag(rs, lane * 8192, MB_OFF + (2 * 256 + c) * 1024 + (G + MB_WQ) * 8, lane < 32, tag);
      lds_arrive(lb + L_CNT + 16, lane);
    }
    STAMP(30);
    WST(38);
#pragma unroll
    for (int j = 0; j < 4; ++j) undef(mt[j]);
    if (G < 27 && w < 6) {
      const int kt = smul(G, 57) >> 9, cb = G - smul(kt, 9), p0 = smul(kt, 6) + w;
      const rsrc_t r1 = __builtin_amdgcn_make_buffer_rsrc((void*)w1, 0, 576 * 9216, 0x00020000);
      const int s1 = smul(p0, 32 * 9216) + smul(c, 1152) + cb * 128;
      v4u m1[4];
      lds_wait(lb + L_CNT + 16, 1);
#pragma unroll
      for (int j = 0; j < 4; ++j) m1[j] = ldp(r1, r8 * 9216 + j8 * 16, s1 + j * (8 * 9216));
      tile_tr(m1, mt, lw, lane);
      f32x4 cs[4];
#pragma unroll
      for (int q = 0; q < 4; ++q) cs[q] = mfma(mt[q], ones, f32x4{0.f, 0.f, 0.f, 0.f});
      const f32x4 d = tl == 0 ? cs[0] : tl == 1 ? cs[1] : tl == 2 ? cs[2] : cs[3];
      if (tl < 4) *(LAS f32x4*)(size_t)(lb + L_TC + w * 256 + tl * 64 + g * 16) = d;
    }
    if (w == 7) { poll<0>(rs, lane, mbg + c * 1024 + MB_WQ * 8, 32, tag); lds_arrive(lb + L_CNT + 8, lane); }
    lds_wait(lb + L_CNT + 8, 1);
    WST(43);
    if (!ra) __builtin_amdgcn_s_sleep(8);
    fill<0, 18>(ring, rs, v16, wq);
    WST(44);
    if (w == 7) {
      v4u p[18];
#pragma unroll
      for (int i = 0; i < 18; ++i) p[i] = ldx(rs, v16, CSQ_OFF + smul(c, 18432) + i * 1024);
      f32x4 cs = __builtin_bit_cast(f32x4, p[0]);
#pragma unroll
      for (int i = 1; i < 18; ++i) cs += __builtin_bit_cast(f32x4, p[i]);
      *(LAS f32x4*)(size_t)(lb + L_TCQ + v16) = cs;
    }
  }

#pragma unroll 1
  for (int layer = 0; layer < 2; ++layer) {
    f32x4 m0, m1;
    {
      FRESH();
      const int tt = w & 3;
      const bool swp = (w == 5) || (w == 7);
      f32x4 a0[5], a1[4];
      float rr[5], mr[5];
#pragma unroll
      for (int t = 0; t < 5; ++t) { undef(rr[t]); undef(mr[t]); }
      undef(m0); undef(m1);
#define ROWSTATS() \
      { \
        const bool nohalo = first && w < 2; \
        _Pragma("unroll") for (int r = 0; r < 4; ++r) { \
          const int kl = 4 * g + r; \
          m0[r] = (kl >= tl + 9 && !nohalo) ? 0.f : -1e30f; \
          m1[r] = (kl <= tl && kl + 7 >= tl) ? 0.f : -1e30f; \
        } \
        lds_wait(lb + L_CNT + 4, 8 * layer + 4); \
        _Pragma("unroll") for (int t = 0; t < 4; ++t) { const f32x2 p = ldsf2(lb + L_SS + t * 128 + tl * 8); rr[t] = p[0]; mr[t] = p[1]; } \
        f32x2 p = ldsf2(lb + L_SS + 4 * 128 + tl * 8); \
        _Pragma("unroll") for (int j = 5; j < 8; ++j) p += ldsf2(lb + L_SS + j * 128 + tl * 8); \
        const float m = p[0] * (1.f / 576.f), var = __builtin_fmaxf(__builtin_fmaf(p[1], 1.f / 576.f, -m * m), 0.f); \
        rr[4] = __builtin_amdgcn_rsqf(var + 1e-5f); \
        mr[4] = -m * rr[4]; \
      }
      lds_wait(lb + L_CNT, 8 * layer + 4);
      if (!ra) {
        lds_wait(lb + L_CNT + 40, 8 * layer + 4);
        __builtin_amdgcn_s_setprio(3);
        f32x4 s = {0.f, 0.f, 0.f, 0.f}, q = s, sh_ = s, qh_ = s;
        v4u xm[18], xh[5];
        u32 xa = lb + tt * 1024 + v16s, xb = xa + 46080, xc = lb + tt * 5120 + 4096 + v16s;
        asm volatile("" : "+v"(xa), "+v"(xb), "+v"(xc));
#pragma unroll
        for (int i = 0; i < 18; ++i) xm[i] = lds128((i < 9 ? xa : xb) + (i % 9) * 5120);
#pragma unroll
        for (int i = 0; i < 4; ++i) xh[i] = lds128(xc + i * 20480);
        xh[4] = v4u{0u, 0u, 0u, 0u};
        if (tt < 2) xh[4] = lds128(xc + 81920);
#pragma unroll
        for (int i = 0; i < 18; ++i) stat_acc(xm[i], s, q);
#pragma unroll
        for (int i = 0; i < 5; ++i) stat_acc(xh[i], sh_, qh_);
        const float m = s[0] * (1.f / 576.f), var = __builtin_fmaxf(__builtin_fmaf(diag(q, g, tl), 1.f / 576.f, -m * m), 0.f);
        const float r = __builtin_amdgcn_rsqf(var + 1e-5f), qh = diag(qh_, g, tl);
        if (lane < 32) *(LAS f32x2*)(size_t)(lb + L_SS + (tt + 4 * g) * 128 + tl * 8) = g == 0 ? f32x2{r, -m * r} : f32x2{sh_[0], qh};
        __builtin_amdgcn_s_setprio(0);
        lds_arrive(lb + L_CNT + 4, lane);
        ROWSTATS();
        if (swp) { float a = rr[0]; rr[0] = rr[4]; rr[4] = a; a = mr[0]; mr[0] = mr[4]; mr[4] = a; }
      }
      WST(18);
      STAMP(1 + 16 * layer);
      groupq(a0, a1, ring, lb + v16s + (swp ? 4096 : 0), lb + v16s + 1024, lb + v16s + (swp ? 0 : 4096), rs, v16, wq + 18432, woc + smul(w, 18432), lb + L_CNT + 40, 8 * layer + 4);
      WST(19);
      STAMP(2 + 16 * layer);
      if (layer == 0) WST(45);
      if (ra) ROWSTATS();
      const f32x4 c0 = ldsf4(lb + L_TCQ + w * 128 + g * 16), c1 = ldsf4(lb + L_TCQ + w * 128 + 64 + g * 16);
      const int t0 = w < 4 ? w : 4 + w, t1 = w < 4 ? 4 + w : 12 + ((w - 4) >> 1);
      const u32 e0 = lb + L_E + t0 * 2560 + lane * 8, e1 = lb + L_E + t1 * 2560 + lane * 8;
#pragma unroll
      for (int t = 0; t < 5; ++t) {
        const int slot = t == 0 ? (swp ? 0 : 1) : t == 4 ? (swp ? 1 : 0) : t + 1;
        float y[4];
#pragma unroll
        for (int r = 0; r < 4; ++r) y[r] = __builtin_fmaf(rr[t], a0[t][r], mr[t] * c0[r]);
        sts64(e0 + slot * 512, v2u{pk(y[0], y[1]), pk(y[2], y[3])});
        if (t < 4) {
#pragma unroll
          for (int r = 0; r < 4; ++r) y[r] = __builtin_fmaf(rr[t], a1[t][r], mr[t] * c1[r]);
          sts64(e1 + slot * 512, v2u{pk(y[0], y[1]), pk(y[2], y[3])});
        }
      }
    }
    v4u nring[18], ex[6];
    {
      FRESH();
      const int p = w & 1, qt = w >> 1;
      u32 el = lb + L_E + qt * 512 + p * 5120 + lane * 8, et = lb + L_E + qt * 512 + ((lane & 3) * 16 + 4 * g + (tl >> 2)) * 8, er = lb + L_E + qt * 512 + p * 256 + tl * 8;
      u32 ev = et + p * 5120;
      asm volatile("" : "+v"(el), "+v"(et), "+v"(er), "+v"(ev));
      WST(20);
      bar();
      WST(21);
      STAMP(3 + 16 * layer);
      const v2u qr = lds64(er + 12 * 2560 + 512), kp = lds64(er + 12 * 2560 + 128), kc = lds64(er + 12 * 2560 + 512 + 128);
      v2u k0[2], k1[2], qm[2], v0[2], v1[2];
#pragma unroll
      for (int e = 0; e < 2; ++e) { k0[e] = lds64(el + e * 2560); k1[e] = lds64(el + e * 2560 + 512); qm[e] = lds64(el + (4 + e) * 2560 + 512); }
#pragma unroll
      for (int e = 0; e < 2; ++e) { v0[e] = ldstr(ev + (8 + e) * 2560); v1[e] = ldstr(ev + (8 + e) * 2560 + 512); }
      const v2u r0 = ldstr(et + 13 * 2560), r1 = ldstr(et + 13 * 2560 + 512);
      SB();
#pragma unroll
      for (int i = 0; i < 18; ++i) undef(nring[i]);
#pragma unroll
      for (int i = 0; i < 6; ++i) undef(ex[i]);
      if (layer == 0 && G < 27) {
        const int kt = smul(G, 57) >> 9, cb = G - smul(kt, 9), p0 = smul(kt, 6) + w;
        if (w < 6) {
          const rsrc_t r2 = __builtin_amdgcn_make_buffer_rsrc((void*)w2, 0, 4608 * 1152, 0x00020000);
#pragma unroll
          for (int q = 0; q < 4; ++q) { int so, st; mlp_dest<0>(c, cb, q, so, st); stx(mt[q], rs, v16, so + smul(st, p0)); }
#pragma unroll
          for (int j = 0; j < 4; ++j) mt[j] = ldp(r2, (lane >> 3) * 1152 + (lane & 7) * 16, smul(smul(c, 576) + 32 * p0, 1152) + cb * 128 + j * (8 * 1152));
        } else if (w == 6) {
          f32x4 cs = ldsf4(lb + L_TC + tl * 16);
#pragma unroll
          for (int j = 1; j < 6; ++j) cs += ldsf4(lb + L_TC + j * 256 + tl * 16);
          stx(__builtin_bit_cast(v4u, cs), rs, lane < 16 ? tl * 16 : OOB, CS1_OFF + smul(c, 20736) + smul(kt, 2304) + cb * 256);
        }
      }
      if (w < 6) {
#pragma unroll
        for (int i = 0; i < 6; ++i) ex[i] = ldw(rs, v16 + (i & 3) * 1024, woc + smul(8 + (w & 1), 18432) + smul(w >> 1, 6144) + (i >> 2) * 4096);
      }
      if (layer == 1) fill<0, AUX1>(nring, rs, v16, wm);
      SB();
      const v4u vr = {r0[0], r0[1], r1[0], r1[1]};
      v4u om; v2u orr;
#pragma unroll
      for (int e = 0; e < 2; ++e) {
        const v4u qf = {qm[e][0], qm[e][1], g == 0 ? qr[e] : 0u, 0u};
        const f32x4 s0 = mfma(v4u{k0[e][0], k0[e][1], kp[e], 0u}, qf, m0), s1 = mfma(v4u{k1[e][0], k1[e][1], kc[e], 0u}, qf, m1);
        float mx = __builtin_fmaxf(__builtin_fmaxf(__builtin_fmaxf(s0[0], s0[1]), __builtin_fmaxf(s0[2], s0[3])),
                                   __builtin_fmaxf(__builtin_fmaxf(s1[0], s1[1]), __builtin_fmaxf(s1[2], s1[3])));
        mx = xmax(mx);
        constexpr float sc = 0.23570226039551584f * 1.4426950408889634f;
        const float nb = -mx * sc;
        float pe[8], sum = 0.f;
#pragma unroll
        for (int r = 0; r < 4; ++r) {
          pe[r] = __builtin_amdgcn_exp2f(__builtin_fmaf(s0[r], sc, nb));
          pe[4 + r] = __builtin_amdgcn_exp2f(__builtin_fmaf(s1[r], sc, nb));
          sum += pe[r] + pe[4 + r];
        }
        const v4u pf = {pk(pe[0], pe[1]), pk(pe[2], pe[3]), pk(pe[4], pe[5]), pk(pe[6], pe[7])};
        const f32x4 z = {0.f, 0.f, 0.f, 0.f};
        const f32x4 o = mfma(v4u{v0[e][0], v0[e][1], v1[e][0], v1[e][1]}, pf, z), oq = mfma(vr, pf, z);
        const float inv = __builtin_amdgcn_rcpf(xsum(sum));
        om[2 * e] = pk(o[0] * inv, o[1] * inv); om[2 * e + 1] = pk(o[2] * inv, o[3] * inv);
        orr[e] = pk(oq[2 * e] * inv, oq[2 * e + 1] * inv);
      }
      const int ob = OB_OFF + gofs + qt * 1024;
      stx(om, rs, v16, ob + (2 * c + p) * 4096);
      stx2(orr, rs, g == p ? tl * 8 : OOB, ob + (16 + (c >> 2)) * 4096 + (c & 3) * 256 + p * 128);
      WST(22);
      STAMP(4 + 16 * layer);
      int fs = mbg + (MB_O + c) * 8, ps = mbg + c * 1024 + MB_O * 8, fv = lane * 1024;
      asm volatile("" : "+s"(fs)); asm volatile("" : "+s"(ps)); asm volatile("" : "+v"(fv));
      drain();
      WST(23);
      bar();
      flag(rs, fv, fs, w == 7 && lane < 8, tag);
      WST(24);
      STAMP(5 + 16 * layer);
      if (layer == 0) {
        flag(rs, lane * 8192, MB_OFF + (2 * 256 + c) * 1024 + (G + MB_W1) * 8, w == 6 && lane < 32, tag);
        if (w < 6) { v4u f[4]; tile_tr(mt, f, lb + L_E + w * 4608, lane); mt[0] = f[0]; mt[1] = f[1]; mt[2] = f[2]; mt[3] = f[3]; }
      }
      if (layer == 1) fill<AUX1, AUX2>(nring, rs, v16, wm);
      if (w == 7) { __builtin_amdgcn_s_sleep(PD0); poll<0>(rs, lane, ps, 8, tag); }
    }
    {
      FRESH();
      const int th = c & 1, kh = w >> 2, sw = (c >> 1) & 1, u1 = ub + (w & 3);
      int ob = OB_OFF + gofs + th * 2048 + (w >> 1) * 4096 + (w & 1) * 1024;
      u32 lo = lb + L_OI + w * 1024 + v16, l4 = lb + L_OI + (32 + w) * 1024 + (g * 16 + 2 * (lane & 7)) * 16 + ((lane >> 3) & 1) * 8;
      u32 x0 = lb + L_OI + v16 + kh * (9 * 2048 + 1024), x1 = lb + L_OI + v16 + kh * (9 * 2048) + (kh ^ 1) * 1024;
      u32 xe = lb + L_OI + v16 + sw * 1024 + (w < 6 ? smul(w >> 1, 6 * 2048) : 0);
      const v4u xr = lds128(lb + smul(u1, 5120) + (2 * th + kh) * 1024 + v16s);
      asm volatile("" : "+s"(ob)); asm volatile("" : "+v"(lo), "+v"(l4), "+v"(x0), "+v"(x1), "+v"(xe));
      bar();
      WST(25);
      STAMP(6 + 16 * layer);
      v4u of[5];
#pragma unroll
      for (int i = 0; i < 4; ++i) of[i] = ldx(rs, v16, ob + i * 16384);
      undef(of[4]);
      if (ra) of[4] = ldx(rs, v16, ob + 65536);
      SB();
#pragma unroll
      for (int i = 0; i < 4; ++i) sts128(lo + i * 8192, of[i]);
      if (ra) { sts64(l4, v2u{of[4][0], of[4][1]}); sts64(l4 + 16, v2u{of[4][2], of[4][3]}); }
      bar();
      WST(26);
      STAMP(7 + 16 * layer);
      if (layer == 0 && G < 27 && w < 6) {
        const int kt = smul(G, 57) >> 9, cb = G - smul(kt, 9), p0 = smul(kt, 6) + w;
#pragma unroll
        for (int q = 0; q < 4; ++q) { int so, st; mlp_dest<1>(c, cb, q, so, st); stx(mt[q], rs, v16, so + smul(st, p0)); }
      }
      if (layer == 1) fill<AUX2, AUX3>(nring, rs, v16, wm);
      f32x4 a[5];
      groupo(a, ring, ex, x0, x1, xe);
      WST(27);
      *(LAS f32x4*)(size_t)(lb + L_PX + w * 2048 + v16) = a[1];
      *(LAS f32x4*)(size_t)(lb + L_PX + w * 2048 + 1024 + v16) = a[3];
      if (w < 6) *(LAS f32x4*)(size_t)(lb + smul(w, 5120) + 4096 + v16) = a[4];
      a[0] += f32x4{lo2f(xr[0]), hi2f(xr[0]), lo2f(xr[1]), hi2f(xr[1])};
      a[2] += f32x4{lo2f(xr[2]), hi2f(xr[2]), lo2f(xr[3]), hi2f(xr[3])};
      int xb = X1_OFF + gofs + u1 * 4096 + (2 * th + kh) * 1024, fs = mbg + (MB_X1 + c) * 8, ps = mbg + c * 1024 + MB_X1 * 8, fv = lane * 1024;
      u32 pa = lb + L_PX + (w ^ 4) * 2048 + v16;
      asm volatile("" : "+s"(xb)); asm volatile("" : "+s"(fs)); asm volatile("" : "+s"(ps)); asm volatile("" : "+v"(fv), "+v"(pa), "+v"(a[0]), "+v"(a[2]));
      bar();
      stx(pk8(a[0] + ldsf4(pa), a[2] + ldsf4(pa + 1024)), rs, v16, xb);
      if (w == 6) {
        const v4u xv = lds128(lb + smul(us, 5120) + (2 * th + sw) * 1024 + v16s);
        const u32 pb = lb + 4096 + v16;
        const f32x4 e0 = ldsf4(pb) + ldsf4(pb + 10240) + ldsf4(pb + 20480) + f32x4{lo2f(xv[0]), hi2f(xv[0]), lo2f(xv[1]), hi2f(xv[1])};
        const f32x4 e1 = ldsf4(pb + 5120) + ldsf4(pb + 15360) + ldsf4(pb + 25600) + f32x4{lo2f(xv[2]), hi2f(xv[2]), lo2f(xv[3]), hi2f(xv[3])};
        stx(pk8(e0, e1), rs, v16, X1_OFF + gofs + us * 4096 + (2 * th + sw) * 1024);
      }
      WST(28);
      STAMP(8 + 16 * layer);
      if (layer == 0 && w == 7) poll<0>(rs, lane, MB_OFF + (2 * 256 + G * 8 + c) * 1024 + MB_W1 * 8, 32, tag);
      drain();
      WST(29);
      bar();
      flag(rs, fv, fs, w == 7 && lane < 8, tag);
      WST(30);
      if (layer == 0) {
        flag(rs, lane * 8192, MB_OFF + (2 * 256 + c) * 1024 + (G + MB_W2) * 8, w == 6 && lane < 32, tag);
        fill<0, AUX3>(nring, rs, v16, wm);
      }
      fill<AUX3, 18>(nring, rs, v16, wm);
      if (layer == 0 && w < 6) {
#pragma unroll
        for (int j = 0; j < 2; ++j) {
          if (j == 0 || lane < 32) {
            const int n = w * 96 + lane + 64 * j;
            float cs = 0.f;
#pragma unroll
            for (int k = 0; k < 3; ++k) cs += __builtin_bit_cast(float, __builtin_amdgcn_raw_buffer_load_b32(rs, n * 4, CS1_OFF + smul(c, 20736) + k * 2304, 16));
            u32 h, m, l;
            split3(cs, h, m, l);
            sts64(lb + L_TA + 8 * n, v2u{h | (m << 16), b1[576 * c + colp(n >> 5, (n >> 4) & 1, n & 15)] | (l << 16)});
          }
        }
      }
      if (w == 7) { __builtin_amdgcn_s_sleep(PD0); poll<0>(rs, lane, ps, 8, tag); }
    }
#pragma unroll
    for (int i = 0; i < 18; ++i) ring[i] = nring[i];
    {
      FRESH();
      const int tt = w & 3;
      int sm = X1_OFF + gofs + tt * 1024;
      u32 la = lb + tt * 1024 + v16, lc = la + CH1 * 4096;
      asm volatile("" : "+s"(sm)); asm volatile("" : "+v"(la), "+v"(lc));
      bar();
      WST(31);
      STAMP(9 + 16 * layer);
      if (ra) {
        v4u xm[CH1];
#pragma unroll
        for (int i = 0; i < CH1; ++i) xm[i] = ldx(rs, v16, sm + i * 4096);
        SB();
#pragma unroll
        for (int i = 0; i < CH1; ++i) sts128(la + i * 4096, xm[i]);
        lds_arrive(lb + L_CNT, lane);
        lds_wait(lb + L_CNT, 8 * layer + 8);
      }
      int wb = w;
      asm volatile("" : "+s"(wb));
      if (wb >= 4) {
        __builtin_amdgcn_s_sleep(STSL);
        v4u xm[18 - CH1], ym[CH1];
#pragma unroll
        for (int i = 0; i < 18 - CH1; ++i) xm[i] = ldx(rs, v16, sm + (CH1 + i) * 4096);
        SB();
#pragma unroll
        for (int i = 0; i < 18 - CH1; ++i) sts128(lc + i * 4096, xm[i]);
        lds_arrive(lb + L_CNT + 40, lane);
        lds_wait(lb + L_CNT, 8 * layer + 8);
        __builtin_amdgcn_s_setprio(3);
#pragma unroll
        for (int i = 0; i < CH1; ++i) ym[i] = lds128(la + i * 4096);
        f32x4 s = {0.f, 0.f, 0.f, 0.f}, q = s;
#pragma unroll
        for (int i = 0; i < 18 - CH1; ++i) stat_acc(xm[i], s, q);
#pragma unroll
        for (int i = 0; i < CH1; ++i) stat_acc(ym[i], s, q);
        const float m = s[0] * (1.f / 576.f), var = __builtin_fmaxf(__builtin_fmaf(diag(q, g, tl), 1.f / 576.f, -m * m), 0.f);
        const float r1 = __builtin_amdgcn_rsqf(var + 1e-5f), r = r1 * __builtin_amdgcn_rsqf(var * r1 * r1 + 1e-5f), ri = __builtin_amdgcn_rcpf(r);
        constexpr float k1 = -2.f * 0.7978845608028654f * 1.4426950408889634f, k2 = k1 * 0.044715f;
        u32 mh, mm_, ml, rh, rm, rl;
        split3(-m, mh, mm_, ml); split3(ri, rh, rm, rl);
        const u32 mv = g == 0 ? mh : g == 1 ? mm_ : g == 2 ? ml : 0u, rv = g == 0 ? rh : g == 1 ? rm : g == 2 ? rl : 0u;
        sts64(lb + L_RK + tt * 512 + lane * 8, v2u{mv | (mv << 16), rv | (mv << 16)});
        if (lane < 16) *(LAS f32x4*)(size_t)(lb + L_SS + tt * 256 + tl * 16) = f32x4{k1 * r, k2 * r * r * r, ri, 0.f};
        __builtin_amdgcn_s_setprio(0);
        if (layer == 0 && w == 7) poll<0>(rs, lane, MB_OFF + (2 * 256 + G * 8 + c) * 1024 + MB_W2 * 8, 32, tag);
        lds_arrive(lb + L_CNT + 4, lane);
      }
    }
    STAMP(10 + 16 * layer);
    {
      FRESH();
      const int i = w & 3, ot = c >> 1, hb = c & 1;
      const int pb = PB_OFF + gofs * 8 + smul((i >> 1) * 8 + c, 9216);
      const int town = (i >> 1) == hb ? ot : -1;
      const u32 xa = lb + v16, ha = lb + L_H + v16, lo = xa + (ot ^ 1) * 1024;
      u32 ta = lb + L_TA + tl * 8, ka = lb + L_SS + tl * 16, ga = lb + L_RK + lane * 8;
      asm volatile("" : "+v"(ta), "+v"(ka), "+v"(ga));
#define LOADRK() _Pragma("unroll") for (int t = 0; t < 4; ++t) { rk.aug[t] = lds64(ga + t * 512); rk.k[t] = ldsf4(ka + t * 256); }
      int sn = wm + 18432;
      WST(0);
#pragma unroll 1
      for (int gi = 0; gi < (ra ? 2 : 1); ++gi) {
        const int u = ra ? 8 * gi + i : 4 + i;
        f32x4 a[2][4];
        RowK rk;
        v2u t0, t1;
        group<2, CH1, 1>(a, ring, xa, rs, v16, sn, sn + 18432, [&]() INL { if (gi == 0) lds_wait(lb + L_CNT + 40, 8 * layer + 8); }, [&]() INL {
          if (gi == 0) lds_wait(lb + L_CNT + 4, 8 * layer + 8);
          t0 = lds64(ta + u * 256); t1 = lds64(ta + u * 256 + 128);
          LOADRK();
        });
        sn += 2 * 18432;
        __builtin_amdgcn_s_setprio(3);
        aug_tile(a[0], t0, rk); aug_tile(a[1], t1, rk);
        __builtin_amdgcn_s_setprio(0);
        mm_end();
        WST(1 + 2 * gi);
        gelu_pair(a[0], a[1], ha + u * 4096, rk);
        WST(2 + 2 * gi);
      }
      if (ra) {
        f32x4 a[5][4];
        bar();
        STAMP(11 + 16 * layer);
        WST(8);
        group<5, 12, 0>(a, ring, ha, rs, v16, sn, 0, []() INL { bar(); }, []() INL {});
        mm_end();
        WST(11);
        __builtin_amdgcn_s_setprio(APRIO);
        const u32 ls = xa + (ot ^ 2) * 1024 + (i >> 1) * 16384;
#pragma unroll
        for (int t = 0; t < 4; ++t) sts64((t == town ? lo + (16 + hb) * 4096 : ls + t * 4096) + (i & 1) * 8, v2u{pk(a[4][t][0], a[4][t][1]), pk(a[4][t][2], a[4][t][3])});
        lds_arrive(lb + L_CNT + 48 + (i >> 1) * 4, lane);
        part_pair(a[0], a[1], rs, v16, pb + ((4 * i) & 7) * 1024, town, lo + (4 * i) * 4096);
        part_pair(a[2], a[3], rs, v16, pb + ((4 * i + 1) & 7) * 1024, town, lo + (4 * i + 1) * 4096);
        lds_wait(lb + L_CNT + 48 + (i >> 1) * 4, 2 * layer + 2);
#pragma unroll
        for (int k = 0; k < 2; ++k) {
          const int t = 2 * (i & 1) + k;
          if (t != town) stx(lds128(ls + t * 4096), rs, v16, pb + 8192 + smul(t, 2 * 8 * 9216));
        }
      }
      int wb = w;
      asm volatile("" : "+s"(wb));
      if (wb >= 4) {
        v4u aux[18];
        {
          f32x4 a[3][4];
          RowK rk;
          v2u t0, t1, t2;
          group<3, 99, 1>(a, ring, xa, rs, v16, sn, sn + 2 * 18432, []() INL {}, [&]() INL {
            t0 = lds64(ta + (12 + i) * 256); t1 = lds64(ta + (12 + i) * 256 + 128); t2 = lds64(ta + (16 + (i >> 1)) * 256 + (i & 1) * 128);
            LOADRK();
          });
          sn += 3 * 18432;
          aug_tile(a[0], t0, rk); aug_tile(a[1], t1, rk); aug_tile(a[2], t2, rk);
          mm_end();
          WST(7);
          bar();
          WST(8);
          gelu_pair(a[0], a[1], ha + (12 + i) * 4096, rk);
#pragma unroll
          for (int t = 0; t < 4; ++t) sts64(ha + (16 + (i >> 1)) * 4096 + t * 1024 + (i & 1) * 8, gelu4(a[2][t], rk.k[t]));
          WST(9);
          bar();
          WST(10);
        }
        fill<0, 18>(aux, rs, v16, sn);
        f32x4 a[2][4];
        group36<true>(a, ring, aux, ha, rs, v16, sn + 18432, []() INL {});
        WST(3);
#pragma unroll
        for (int t = 0; t < 4; ++t) sts128(t == town ? lo + (4 * i + 2) * 4096 : xa + (ot ^ 3) * 1024 + (4 * i + t) * 4096, pk8(a[0][t], a[1][t]));
        lds_arrive(lb + L_CNT + 64 + 4 * i, lane);
        WST(4);
        group36<false>(a, ring, aux, ha, rs, v16, 0, []() INL {});
        WST(5);
        part_pair(a[0], a[1], rs, v16, pb + ((4 * i + 3) & 7) * 1024, town, lo + (4 * i + 3) * 4096);
      }
      WST(12);
      STAMP(12 + 16 * layer);
    }
    {
      FRESH();
      const int tt = c >> 1, hb = c & 1;
      int po[7];
#pragma unroll
      for (int j = 0; j < 7; ++j) { po[j] = PB_OFF + gofs * 8 + smul(c * 8 + ((c + 1 + j) & 7), 9216); asm volatile("" : "+s"(po[j])); }
      int vo = layer == 0 ? v16 : (rowbase + 2 * (16 * tt + tl)) * 1152 + 16 * g;
      v4u sel[2];
      make_sel(sel, g, tl);
      int slot = (0x62854100u >> (4 * w)) & 15, vs = v16 + slot * 1024, fv = lane * 2048;
      int fs = mbg + ((w >> 1) & 1) * 1024 + ((ra ? MB_A : MB_B) + 2 * c + (w & 1)) * 8;
      asm volatile("" : "+s"(fs)); asm volatile("" : "+s"(slot));
      asm volatile("" : "+v"(vo), "+v"(vs), "+v"(fv), "+v"(sel[0]), "+v"(sel[1]));
      drain();
      flag(rs, fv, fs, lane < 4, tag);
      __builtin_amdgcn_s_setprio(0);
      if (!ra) lds_arrive(lb + L_CNT + 44, lane);
      WST(13);
      if (ra) {
        const int pb = PB_OFF + gofs * 8 + smul((w >> 1) * 8 + c, 9216) + ((4 * w + 2) & 7) * 1024, town = (w >> 1) == hb ? tt : -1;
        lds_waits(lb + L_CNT + 64 + 4 * w, layer + 1);
#pragma unroll
        for (int t = 0; t < 4; ++t)
          if (t != town) stx(lds128(lb + v16 + (tt ^ 3) * 1024 + (4 * w + t) * 4096), rs, v16, pb + t * (2 * 8 * 9216));
        WST(5);
        if (w == 0) { poll<2>(rs, lane, mbg + c * 1024 + MB_A * 8, 16, tag); lds_arrive(lb + L_CNT + 24, lane); }
        drain();
        flag(rs, lane * 2048, mbg + (w >> 1) * 1024 + (MB_B + 16 + 2 * c + (w & 1)) * 8, lane < 4, tag);
        WST(7);
        if (w == 0) {
          __builtin_amdgcn_s_sleep(PS1);
          poll<4>(rs, lane, mbg + c * 1024 + (MB_B + 16) * 8, 16, tag);
          lds_arrive(lb + L_CNT + 24, lane);
          WST(9);
          lds_waits(lb + L_CNT + 44, 4 * layer + 4);
          __builtin_amdgcn_s_sleep(PSL);
          poll<1>(rs, lane, mbg + c * 1024 + MB_B * 8, 16, tag);
          lds_arrive(lb + L_CNT + 24, lane);
        }
      }
#pragma unroll 1
      for (int rep = ra ? 0 : 1; rep < 2; ++rep) {
        if (ra && rep == 1) { slot = 3 + 4 * w; vs = v16 + slot * 1024; }
        const int u = slot == 8 ? 16 + hb : 8 * hb + slot;
        const bool on = !ra || (rep == 0 ? w > 0 : w < 2);
        v4u pv[7];
#pragma unroll
        for (int j = 0; j < 7; ++j) undef(pv[j]);
        if (on) {
          if (!ra || (NOEARLY && rep == 0)) lds_wait(lb + L_CNT + 44, 4 * layer + 4);
          lds_waits(lb + L_CNT + 24, 3 * layer + (ra ? 1 + 2 * rep : w < 6 ? 1 : 2));
          if (rep == 1) { WST(14); STAMP(13 + 16 * layer); } else WST(6);
#pragma unroll
          for (int j = 0; j < 7; ++j) pv[j] = ldx(rs, vs, po[j]);
        }
        SB();
        if (on) {
          const v4u xv = lds128(lb + u * 4096 + tt * 1024 + v16), ov = lds128(lb + u * 4096 + (tt ^ 1) * 1024 + v16);
          f32x4 ya = mfma(sel[0], ov, mfma(sel[0], xv, ldsf4(lb + L_TB2 + u * 128 + g * 32)));
          f32x4 yb = mfma(sel[1], ov, mfma(sel[1], xv, ldsf4(lb + L_TB2 + u * 128 + g * 32 + 16)));
#pragma unroll
          for (int j = 0; j < 7; ++j) { ya = mfma(sel[0], pv[j], ya); yb = mfma(sel[1], pv[j], yb); }
          if (HOLD && w != 1) lds_waits(lb + L_CNT + 24, 3 * layer + 3);
          if (layer == 0) stx(pk8(ya, yb), rs, vo, X2_OFF + gofs + u * 4096 + tt * 1024);
          else st128<16>(pk8(ya, yb), ro, vo, 64 * u);
        }
      }
      WST(15);
      if (layer == 0) {
        STAMP(14);
        drain();
        const bool nxt = c >= 6 && (G & 15) != 15;
        if (lds_ticket(lb + L_CNT + 56, lane) == 7) flag(rs, lane * 1024 + (lane & 8) * 2, mbg + (MB_X2 + c) * 8, lane < 8 || (nxt && lane < 16), tag);
      }
      WST(16);
    }
    STAMP(15 + 16 * layer);
    if (layer == 0) {
      FRESH();
      static_assert(CH1 == 6, "halo split");
      const int tt = w & 3;
      if (FW) lds_waits(lb + L_CNT + 56, 8);
      fill<0, 18>(ring, rs, v16, wq);
      int sm = X2_OFF + gofs + tt * 1024, sh = X2_OFF - 73728 + gofs + 3072 + tt * 4096;
      u32 la = lb + tt * 1024 + v16s + (ra ? 0 : 6 * 5120), lh = lb + 4096 + smul(tt + (ra ? 0 : 6), 5120) + v16s;
      asm volatile("" : "+s"(sm)); asm volatile("" : "+s"(sh)); asm volatile("" : "+v"(la), "+v"(lh));
      if (w == 7) poll<1>(rs, lane, mbg + c * 1024 + MB_X2 * 8, first ? 8 : 10, tag);
      bar();
      WST(17);
      STAMP(16);
      v4u xh[3];
#pragma unroll
      for (int i = 0; i < 3; ++i) xh[i] = v4u{0u, 0u, 0u, 0u};
      if (ra) {
        v4u xm[6];
#pragma unroll
        for (int i = 0; i < 6; ++i) xm[i] = ldx(rs, v16, sm + i * 4096);
        if (!first) { xh[0] = ldx(rs, v16, sh); if (tt < 2) xh[1] = ldx(rs, v16, sh + 16384); }
        SB();
#pragma unroll
        for (int i = 0; i < 6; ++i) sts128(la + i * 5120, xm[i]);
        sts128(lh, xh[0]);
        if (tt < 2) sts128(lh + 20480, xh[1]);
        lds_arrive(lb + L_CNT, lane);
      }
      int wb = w;
      asm volatile("" : "+s"(wb));
      if (wb >= 4) {
        __builtin_amdgcn_s_sleep(STSL);
        v4u xm[12];
#pragma unroll
        for (int i = 0; i < 12; ++i) xm[i] = ldx(rs, v16, sm + (6 + i) * 4096);
        if (!first) {
#pragma unroll
          for (int i = 0; i < 3; ++i) xh[i] = ldx(rs, v16, sh + (6 + 4 * i) * 4096);
        }
        SB();
#pragma unroll
        for (int i = 0; i < 12; ++i) sts128(la + i * 5120, xm[i]);
#pragma unroll
        for (int i = 0; i < 3; ++i) sts128(lh + i * 20480, xh[i]);
        lds_arrive(lb + L_CNT + 40, lane);
      }
    }
#pragma unroll
    for (int j = 0; j < 4; ++j) undef(mt[j]);
  }
  if (bid == 0 && tid == 448) stx2(tag, rs, 0, EP_OFF);
}

extern "C" void kernel_launch(void* const* d_in, const int* in_sizes, int n_in, void* d_out, int out_size, void* d_ws, size_t ws_size,
                              hipStream_t stream) {
  const u16* x = (const u16*)d_in[0];
  const u16* wqkv = (const u16*)d_in[1];
  const u16* wo = (const u16*)d_in[2];
  const u16* w1 = (const u16*)d_in[4];
  const u16* b1 = (const u16*)d_in[5];
  const u16* w2 = (const u16*)d_in[6];
  const u16* b2 = (const u16*)d_in[7];
  (void)hipFuncSetAttribute((const void*)k_main, hipFuncAttributeMaxDynamicSharedMemorySize, L_TOTAL);
  k_main<<<256, 512, L_TOTAL, stream>>>(x, wqkv, wo, w1, b1, w2, b2, (u16*)d_out, (char*)d_ws);
}
```

```cpp
#include <hip/hip_runtime.h>
#include <hip/hip_bf16.h>

typedef unsigned short u16;
typedef unsigned u32;
typedef unsigned long long u64;
typedef u32 v4u __attribute__((ext_vector_type(4)));
typedef u32 v2u __attribute__((ext_vector_type(2)));
typedef float f32x4 __attribute__((ext_vector_type(4)));
typedef float f32x2 __attribute__((ext_vector_type(2)));
typedef short s16x4 __attribute__((ext_vector_type(4)));
typedef __bf16 bf16x2 __attribute__((ext_vector_type(2)));
typedef __bf16 bf16x8 __attribute__((ext_vector_type(8)));
#define GAS __attribute__((address_space(1)))
#define LAS __attribute__((address_space(3)))
#define DI __device__ __forceinline__
#define RLX __ATOMIC_RELAXED, __HIP_MEMORY_SCOPE_AGENT

#define XG 2
#define AUX1 6
#define AUX2 6
#define AUX3 12
#define BF1 12
#define PSL 6
#define BSL 24
#define B1LATE 1
#define HOLD 0
#define FW 1
#define APRIO 3
#define APOLL 1
#define PD0 0
#define MBW 1
#define PS1 8
#define NOEARLY 0
#define CH1 6
#define SIGK 14
#define STSL 3
#define STAMPS 0

#define CODE_BYTES 55296
constexpr int EP_OFF = 0;
constexpr int MB_OFF = 4096, MB_BYTES = 3 * 256 * 1024;
constexpr int MB_O = 0, MB_X1 = 16, MB_A = 32, MB_B = 48, MB_X2 = 80, MB_WQ = 0, MB_W1 = 32, MB_W2 = 64;
constexpr int CSQ_OFF = MB_OFF + MB_BYTES;
constexpr int CS1_OFF = CSQ_OFF + 8 * 18432;
constexpr int WQ_OFF = CS1_OFF + 8 * 20736;
constexpr int WO_OFF = WQ_OFF + 64 * 36864;
constexpr int WM_OFF = WO_OFF + 80 * 18432;
constexpr int OB_OFF = WM_OFF + 64 * 165888;
constexpr int X1_OFF = OB_OFF + 64 * 73728;
constexpr int X2_OFF = X1_OFF + 64 * 73728;
constexpr int PB_OFF = X2_OFF + 32 * 73728;
constexpr int ST_OFF = PB_OFF + 2 * 32 * 64 * 9216;
constexpr int WS_USED = ST_OFF + 2 * 256 * 32 * 8 + 256 * 2048;
constexpr int WS_DUMP = WS_USED;
constexpr int L_E = 92160, L_OI = 92160, L_H = 73728;
constexpr int L_TQ = 92160, L_TC = L_TQ + 18432 + 6 * 4608;
constexpr int L_PX = 129024;
constexpr int L_SS = 147456;
constexpr int L_RK = L_SS + 1024;
constexpr int L_CNT = L_RK + 2048;
constexpr int L_TA = L_CNT + 96;
constexpr int L_TB2 = L_TA + 4608;
constexpr int L_TCQ = L_TB2 + 2304;
constexpr int L_DBG = L_TCQ + 1024;
constexpr int L_TOTAL = L_DBG + (STAMPS ? 2048 : 0);

DI float bf2f(u16 b) { return __builtin_bit_cast(float, (u32)((u32)b) << 16); }
DI float lo2f(u32 x) { return __builtin_bit_cast(float, (u32)x << 16); }
DI float hi2f(u32 x) { return __builtin_bit_cast(float, (u32)x & 0xffff0000u); }
DI u32 pk(float a, float b) { f32x2 f = {a, b}; return __builtin_bit_cast(u32, __builtin_convertvector(f, bf16x2)); }
DI f32x4 mfma(v4u a, v4u b, f32x4 c) {
  return __builtin_amdgcn_mfma_f32_16x16x32_bf16(__builtin_bit_cast(bf16x8, a), __builtin_bit_cast(bf16x8, b), c, 0, 0, 0);
}
DI void mm(f32x4& c, v4u a, v4u b) { asm volatile("v_mfma_f32_16x16x32_bf16 %0, %1, %2, %0" : "+v"(c) : "v"(a), "v"(b)); }
DI void mm0(f32x4& c, v4u a, v4u b) { asm volatile("v_mfma_f32_16x16x32_bf16 %0, %1, %2, 0" : "=&v"(c) : "v"(a), "v"(b)); }
DI void mmk16(f32x4& c, v2u a, v2u b) { asm volatile("v_mfma_f32_16x16x16_bf16 %0, %1, %2, %0" : "+v"(c) : "v"(a), "v"(b)); }
DI void mm_end() { __builtin_amdgcn_sched_barrier(0); asm volatile("s_nop 7\n\ts_nop 1" ::: "memory"); __builtin_amdgcn_sched_barrier(0); }
DI float xsum(float v) {
  u32 x = __builtin_bit_cast(u32, v);
  auto a = __builtin_amdgcn_permlane16_swap(x, x, false, false);
  v = __builtin_bit_cast(float, (u32)a[0]) + __builtin_bit_cast(float, (u32)a[1]);
  x = __builtin_bit_cast(u32, v);
  auto b = __builtin_amdgcn_permlane32_swap(x, x, false, false);
  return __builtin_bit_cast(float, (u32)b[0]) + __builtin_bit_cast(float, (u32)b[1]);
}
DI float xmax(float v) {
  u32 x = __builtin_bit_cast(u32, v);
  auto a = __builtin_amdgcn_permlane16_swap(x, x, false, false);
  v = __builtin_fmaxf(__builtin_bit_cast(float, (u32)a[0]), __builtin_bit_cast(float, (u32)a[1]));
  x = __builtin_bit_cast(u32, v);
  auto b = __builtin_amdgcn_permlane32_swap(x, x, false, false);
  return __builtin_fmaxf(__builtin_bit_cast(float, (u32)b[0]), __builtin_bit_cast(float, (u32)b[1]));
}
#define SB() __builtin_amdgcn_sched_barrier(0)
#define INL __attribute__((always_inline))
DI int smul(int a, int k) { int r; asm("s_mul_i32 %0, %1, %2" : "=s"(r) : "s"(a), "s"(k)); return r; }
DI void bar() {
  asm volatile("s_waitcnt lgkmcnt(0)" ::: "memory");
  __builtin_amdgcn_s_barrier();
  asm volatile("" ::: "memory");
}
typedef __amdgpu_buffer_rsrc_t rsrc_t;
#define WAUX 16
DI v4u ldw(rsrc_t rs, int v, int s) { return __builtin_amdgcn_raw_buffer_load_b128(rs, v, s, WAUX); }
DI v4u ldp(rsrc_t rs, int v, int s) { return __builtin_amdgcn_raw_buffer_load_b128(rs, v, s, 0); }
constexpr int OOB = 0x70000000;
DI v4u ldx(rsrc_t rs, int v, int s) { return __builtin_amdgcn_raw_buffer_load_b128(rs, v, s, 16); }
DI v2u ldx2(rsrc_t rs, int v, int s) { return __builtin_amdgcn_raw_buffer_load_b64(rs, v, s, 16); }
template <int AUX> DI void st128(v4u d, rsrc_t rs, int v, int s) { __builtin_amdgcn_raw_buffer_store_b128(d, rs, v, s, AUX); asm volatile("s_nop 1" :: "v"(d)); }
DI void stx(v4u d, rsrc_t rs, int v, int s) { st128<16>(d, rs, v, s); }
DI void stx2(v2u d, rsrc_t rs, int v, int s) { __builtin_amdgcn_raw_buffer_store_b64(d, rs, v, s, 16); }
DI v4u lds128(u32 a) { return *(const LAS v4u*)(size_t)a; }
DI v2u lds64(u32 a) { return *(const LAS v2u*)(size_t)a; }
DI f32x4 ldsf4(u32 a) { return *(const LAS f32x4*)(size_t)a; }
DI f32x2 ldsf2(u32 a) { return *(const LAS f32x2*)(size_t)a; }
DI void sts128(u32 a, v4u v) { *(LAS v4u*)(size_t)a = v; }
DI void sts64(u32 a, v2u v) { *(LAS v2u*)(size_t)a = v; }
DI v2u ldstr(u32 a) { return __builtin_bit_cast(v2u, __builtin_amdgcn_ds_read_tr16_b64_v4i16((LAS s16x4*)(size_t)a)); }

__host__ __device__ __forceinline__ int colq(int c, int w, int t, int m) {
  if (w < 4 || t == 0) {
    const int h = w & 3, sec = w < 4 ? (t == 0 ? 1 : 0) : 2;
    return sec * 576 + 72 * c + 36 * (h >> 1) + 20 * (h & 1) + m;
  }
  const int qd = m >> 2;
  if (w < 6) return (qd & 1) * 576 + 72 * c + 36 * (qd >> 1) + 16 + (m & 3);
  return 2 * 576 + 72 * c + 36 * (qd & 1) + 16 + (m & 3);
}
__host__ __device__ __forceinline__ int krot(int c) { return 0; }
__host__ __device__ __forceinline__ int colp(int u, int e, int m) { return 32 * u + 8 * (m >> 2) + 4 * e + (m & 3); }
__host__ __device__ __forceinline__ int rowo(int ks, int g, int j) {
  if (ks < 16) return 72 * (ks >> 1) + 36 * (ks & 1) + 20 * (j >> 2) + 4 * g + (j & 3);
  return 72 * (4 * (ks - 16) + g) + 36 * (j >> 2) + 16 + (j & 3);
}
DI void lds_arrive(u32 a, int lane) {
  asm volatile("s_waitcnt lgkmcnt(0)" ::: "memory");
  if (lane == 0) __hip_atomic_fetch_add((LAS u32*)(size_t)a, 1u, __ATOMIC_RELAXED, __HIP_MEMORY_SCOPE_WORKGROUP);
}
DI void lds_waits(u32 a, u32 n) {
  while ((u32)__builtin_amdgcn_readfirstlane(*(volatile LAS u32*)(size_t)a) < n) __builtin_amdgcn_s_sleep(2);
  asm volatile("" ::: "memory");
}
DI void lds_wait(u32 a, u32 n) {
  while ((u32)__builtin_amdgcn_readfirstlane(*(volatile LAS u32*)(size_t)a) < n) {}
  asm volatile("" ::: "memory");
}
#define REFILL(f_, NU) do { const int q_ = (f_) % 18, u_ = (f_) / 18; \
    if (u_ < (NU) - 1) ring[q_] = ldw(rs, v16 + (q_ & 3) * 1024, s0 + u_ * 18432 + (q_ >> 2) * 4096); \
    else if (LAST == 1) ring[q_] = ldw(rs, v16 + (q_ & 3) * 1024, slast + (q_ >> 2) * 4096); } while (0)
template <int NT, int MIDBAR, int LAST, class MID, class PRE>
DI void group(f32x4 (&acc)[NT][4], v4u (&ring)[18], u32 xa, rsrc_t rs, int v16, int s0, int slast, MID&& mid, PRE&& pre) {
  v4u xf[2][4];
  u32 xb = xa + 9 * 4096;
  asm volatile("" : "+v"(xa), "+v"(xb));
#pragma unroll
  for (int t = 0; t < 4; ++t) xf[0][t] = lds128(xa + t * 1024);
  SB();
#pragma unroll
  for (int ks = 0; ks < 18; ++ks) {
    const int kn = ks + 1;
#pragma unroll
    for (int n = 0; n < NT; ++n) {
      const int f = ks * NT + n;
#pragma unroll
      for (int t = 0; t < 4; ++t) {
        if (ks == 0) mm0(acc[n][t], ring[f % 18], xf[0][t]); else mm(acc[n][t], ring[f % 18], xf[ks & 1][t]);
        if (t == 0 && f > 0) REFILL(f - 1, NT);
        const int n0 = kn == MIDBAR ? NT - 2 : 0;
        const int q = n == n0 ? t - 1 : (n == n0 + 1 && t == 1 ? 3 : -1);
        if (q == 0 && kn == MIDBAR) { mid(); SB(); }
        if (q >= 0 && kn < 18) xf[kn & 1][q] = kn < 9 ? lds128(xa + kn * 4096 + q * 1024) : lds128(xb + (kn - 9) * 4096 + q * 1024);
        if (kn == 18 && n == 0 && t == 1) pre();
        SB();
      }
    }
  }
  REFILL(18 * NT - 1, NT);
  SB();
}
template <bool REF, class MID>
DI void group36(f32x4 (&acc)[2][4], v4u (&r0)[18], v4u (&r1)[18], u32 xa, rsrc_t rs, int v16, int sn, MID&& mid) {
  v4u xf[2][4];
  u32 xb = xa + 9 * 4096;
  asm volatile("" : "+v"(xa), "+v"(xb));
#pragma unroll
  for (int t = 0; t < 4; ++t) xf[0][t] = lds128(xa + t * 1024);
  SB();
#pragma unroll
  for (int f = 0; f < 36; ++f) {
    const int ks = f >> 1, n = f & 1, kn = ks + 1;
#pragma unroll
    for (int t = 0; t < 4; ++t) {
      if (ks == 0) mm0(acc[n][t], r0[f], xf[0][t]); else mm(acc[n][t], f < 18 ? r0[f % 18] : r1[f % 18], xf[ks & 1][t]);
      if (REF && t == 0 && f > 0) (f - 1 < 18 ? r0[(f - 1) % 18] : r1[(f - 1) % 18]) = ldw(rs, v16 + ((f - 1) & 3) * 1024, sn + ((f - 1) >> 2) * 4096);
      const int q = n == 0 ? t - 1 : (t == 1 ? 3 : -1);
      if (q >= 0 && kn < 18) xf[kn & 1][q] = kn < 9 ? lds128(xa + kn * 4096 + q * 1024) : lds128(xb + (kn - 9) * 4096 + q * 1024);
      if (f == 2 * SIGK + 1 && t == 2) mid();
      SB();
    }
  }
  mm_end();
  if (REF) r1[17] = ldw(rs, v16 + 3072, sn + 8 * 4096);
  SB();
}
DI void groupq(f32x4 (&a0)[5], f32x4 (&a1)[4], v4u (&ring)[18], u32 x0, u32 x1, u32 x4, rsrc_t rs, int v16, int s0, int slast, u32 cnt, u32 want) {
  constexpr int LAST = 1;
  v4u xf[2][5];
  u32 y0 = x0 + 46080, y1 = x1 + 46080, y4 = x4 + 46080;
  asm volatile("" : "+v"(y0), "+v"(y1), "+v"(y4));
  xf[0][0] = lds128(x0); xf[0][1] = lds128(x1); xf[0][2] = lds128(x1 + 1024); xf[0][3] = lds128(x1 + 2048); xf[0][4] = lds128(x4);
  SB();
#pragma unroll
  for (int ks = 0; ks < 18; ++ks) {
    const int sl = (2 * ks) % 18, kn = ks + 1, o = (kn % 9) * 5120;
#pragma unroll
    for (int t = 0; t < 5; ++t) {
      if (ks == 0) mm0(a0[t], ring[sl], xf[0][t]); else mm(a0[t], ring[sl], xf[ks & 1][t]);
      if (t == 0 && ks > 0) REFILL(2 * ks - 1, 2);
      if (t == 1 && kn == CH1) { lds_wait(cnt, want); SB(); }
      if (t > 0 && kn < 18) xf[kn & 1][t - 1] = lds128(t == 1 ? (kn < 9 ? x0 : y0) + o : (kn < 9 ? x1 : y1) + o + (t - 2) * 1024);
      SB();
    }
#pragma unroll
    for (int t = 0; t < 4; ++t) {
      if (ks == 0) mm0(a1[t], ring[sl + 1], xf[0][t]); else mm(a1[t], ring[sl + 1], xf[ks & 1][t]);
      if (t == 0) REFILL(2 * ks, 2);
      if (t == 1 && kn < 18) xf[kn & 1][4] = lds128((kn < 9 ? x4 : y4) + o);
      SB();
    }
  }
  mm_end();
  REFILL(35, 2);
  SB();
}
DI void groupo(f32x4 (&a)[5], const v4u (&ring)[18], const v4u (&ex)[6], u32 x0, u32 x1, u32 xe) {
  v4u xf[3][2], ef[6];
  asm volatile("" : "+v"(x0), "+v"(x1), "+v"(xe));
#pragma unroll
  for (int k = 0; k < 2; ++k) { xf[k][0] = lds128(x0 + k * 2048); xf[k][1] = lds128(x1 + k * 2048); }
  SB();
#pragma unroll
  for (int ks = 0; ks < 9; ++ks) {
    const int kn = ks + 2;
#pragma unroll
    for (int j = 0; j < 4; ++j) {
      if (ks == 0) mm0(a[j], ring[2 * ks + (j >> 1)], xf[0][j & 1]); else mm(a[j], ring[2 * ks + (j >> 1)], xf[ks % 3][j & 1]);
      if (kn < 9 && j < 2) xf[kn % 3][j] = lds128((j ? x1 : x0) + kn * 2048);
      if (kn >= 9 && j < 3) ef[3 * (kn - 9) + j] = lds128(xe + (3 * (kn - 9) + j) * 2048);
      SB();
    }
  }
#pragma unroll
  for (int k = 0; k < 6; ++k) { if (k == 0) mm0(a[4], ex[0], ef[0]); else mm(a[4], ex[k], ef[k]); SB(); }
  mm_end();
}
template <int LO, int HI> DI void fill(v4u (&ring)[18], rsrc_t rs, int v16, int s) {
  SB();
#pragma unroll
  for (int i = LO; i < HI; ++i) ring[i] = ldw(rs, v16 + (i & 3) * 1024, s + (i >> 2) * 4096);
  SB();
}
template <class T> DI void undef(T& v) { asm volatile("" : "=v"(v)); }
DI void stat_acc(v4u v, f32x4& s, f32x4& q) { s = mfma(v4u{0x3f803f80u, 0x3f803f80u, 0x3f803f80u, 0x3f803f80u}, v, s); q = mfma(v, v, q); }
DI float diag(f32x4 q, int g, int tl) {
  const float a = (tl & 1) ? q[1] : q[0], b = (tl & 1) ? q[3] : q[2];
  return xsum(g == (tl >> 2) ? ((tl & 2) ? b : a) : 0.f);
}
DI void split3(float x, u32& h, u32& m, u32& l) {
  h = pk(x, 0.f); x -= lo2f(h);
  m = pk(x, 0.f); x -= lo2f(m);
  l = pk(x, 0.f);
}
struct RowK { f32x4 k[4]; v2u aug[4]; };
template <int N> DI void gelu(float (&v)[N], f32x4 k) {
  float y[N];
#pragma unroll
  for (int r = 0; r < N; ++r) y[r] = v[r] * v[r];
  SB();
#pragma unroll
  for (int r = 0; r < N; ++r) y[r] = __builtin_fmaf(y[r], k[1], k[0]);
  SB();
#pragma unroll
  for (int r = 0; r < N; ++r) y[r] = v[r] * y[r];
  SB();
#pragma unroll
  for (int r = 0; r < N; ++r) y[r] = __builtin_amdgcn_exp2f(y[r]);
  SB();
#pragma unroll
  for (int r = 0; r < N; ++r) y[r] = __builtin_fmaf(y[r], k[2], k[2]);
  SB();
#pragma unroll
  for (int r = 0; r < N; ++r) y[r] = __builtin_amdgcn_rcpf(y[r]);
  SB();
#pragma unroll
  for (int r = 0; r < N; ++r) v[r] = v[r] * y[r];
  SB();
}
DI v2u gelu4(f32x4 a, f32x4 k) {
  float v[4] = {a[0], a[1], a[2], a[3]};
  gelu<4>(v, k);
  return v2u{pk(v[0], v[1]), pk(v[2], v[3])};
}
DI void aug_tile(f32x4 (&a)[4], v2u ta, const RowK& k) {
#pragma unroll
  for (int t = 0; t < 4; ++t) mmk16(a[t], ta, k.aug[t]);
}
DI void gelu_pair(f32x4 (&a0)[4], f32x4 (&a1)[4], u32 hp, const RowK& k) {
#pragma unroll
  for (int t = 0; t < 4; ++t) {
    float v[8] = {a0[t][0], a0[t][1], a0[t][2], a0[t][3], a1[t][0], a1[t][1], a1[t][2], a1[t][3]};
    gelu<8>(v, k.k[t]);
    sts128(hp + t * 1024, v4u{pk(v[0], v[1]), pk(v[2], v[3]), pk(v[4], v[5]), pk(v[6], v[7])});
  }
}
DI v4u pk8(f32x4 a, f32x4 b) { return v4u{pk(a[0], a[1]), pk(a[2], a[3]), pk(b[0], b[1]), pk(b[2], b[3])}; }
DI void part_pair(const f32x4 (&a0)[4], const f32x4 (&a1)[4], rsrc_t rs, int v16, int sb, int town, u32 lown) {
#pragma unroll
  for (int t = 0; t < 4; ++t) {
    const v4u d = pk8(a0[t], a1[t]);
    if (t == town) sts128(lown, d); else stx(d, rs, v16, sb + t * (2 * 8 * 9216));
  }
}
DI void make_sel(v4u (&sel)[2], int g, int tl) {
  const u32 one = g == (tl >> 2) ? ((tl & 1) ? 0x3f800000u : 0x00003f80u) : 0u, lo = (tl & 2) ? 0u : one, hi = (tl & 2) ? one : 0u;
  sel[0] = v4u{lo, hi, 0u, 0u};
  sel[1] = v4u{0u, 0u, lo, hi};
}
typedef GAS u64 gu64;
DI void drain() { asm volatile("s_waitcnt vmcnt(0) lgkmcnt(0)" ::: "memory"); }
DI void flag(rsrc_t rs, int v, int s, bool on, v2u tag) { if (on) stx2(tag, rs, v, s); }
template <int SL> DI void poll(rsrc_t rs, int lane, int s, int n, v2u tag) {
  if (lane < n) {
#pragma unroll 1
    for (u32 sp = 0; sp < (1u << 16); ++sp) {
      v2u x = {0u, 0u};
      asm volatile("buffer_atomic_or_x2 %0, %1, %2, %3 offen sc0\n\ts_waitcnt vmcnt(0)" : "+v"(x) : "v"(lane * 8), "s"(rs), "s"(s) : "memory");
      if (__builtin_amdgcn_ballot_w64(((x[0] ^ tag[0]) | (x[1] ^ tag[1])) != 0u) == 0) break;
      if (SL) __builtin_amdgcn_s_sleep(SL);
      asm volatile("" ::: "memory");
    }
  }
  asm volatile("" ::: "memory");
}
DI u32 lds_ticket(u32 a, int lane) {
  u32 r = 0;
  if (lane == 0) r = __hip_atomic_fetch_add((LAS u32*)(size_t)a, 1u, __ATOMIC_RELAXED, __HIP_MEMORY_SCOPE_WORKGROUP);
  return __builtin_amdgcn_readfirstlane(r);
}
DI void tile_tr(const v4u* m, v4u (&f)[4], u32 lw, int lane) {
  const u32 wr = lw + (lane >> 3) * 144 + (lane & 7) * 16, tr = lw + (8 * (lane >> 4) + ((lane & 15) >> 2)) * 144 + (lane & 3) * 16;
#pragma unroll
  for (int j = 0; j < 4; ++j) sts128(wr + j * 8 * 144, m[j]);
  asm volatile("s_waitcnt lgkmcnt(0)" ::: "memory");
#pragma unroll
  for (int q = 0; q < 4; ++q) {
    const v2u lo = ldstr(tr + (q >> 1) * 64 + (q & 1) * 8), hi = ldstr(tr + (q >> 1) * 64 + (q & 1) * 8 + 4 * 144);
    f[q] = v4u{lo[0], lo[1], hi[0], hi[1]};
  }
  asm volatile("s_waitcnt lgkmcnt(0)" : "+v"(f[0]), "+v"(f[1]), "+v"(f[2]), "+v"(f[3]) :: "memory");
}
template <int MT> DI void mlp_dest(int c, int cb, int q, int& so, int& st) {
  const int u = 2 * cb + (q >> 1), e = q & 1;
  int wd, y0;
  if (MT == 0) {
    wd = u < 8 ? u : u < 16 ? u - 8 : 4 + q;
    y0 = u < 8 ? e : u < 16 ? 36 + e : 38;
    st = u < 12 ? 2048 : 3072;
  } else {
    const bool sp = cb == 8, fa = (cb & 1) == 0;
    wd = sp ? q : fa ? cb >> 1 : 4 + (cb >> 1);
    y0 = sp ? 76 : fa ? 72 + q : 90 + 36 * (q >> 1) + e;
    st = fa ? 5120 : 2048;
  }
  so = WM_OFF + smul(c * 8 + wd, 165888) + y0 * 1024;
}
#define FRESH() \
  int lane = lane_, w = w_, G = G_, c = c_; \
  asm volatile("" : "+s"(w)); asm volatile("" : "+s"(G)); asm volatile("" : "+s"(c)); asm volatile("" : "+v"(lane)); \
  const int g = lane >> 4, tl = lane & 15, v16 = lane * 16, v16s = (lane ^ g) * 16; \
  const int wq = WQ_OFF + smul(c * 8 + w, 36864), wm = WM_OFF + smul(c * 8 + w, 165888), woc = WO_OFF + smul(c, 184320); \
  const int us = (c & 4) ? 13 : 4, ub = (c & 2) ? us + 1 : us - 4; \
  const bool first = (G & 15) == 0, ra = w < 4; \
  const int rowbase = 2 * 64 * (G & 15) + (G >> 4), gofs = smul(layer * 32 + G, 73728), mbg = MB_OFF + (layer * 256 + G * 8) * 1024; \
  (void)g; (void)tl; (void)v16; (void)v16s; (void)wq; (void)wm; (void)woc; (void)ub; (void)us; (void)first; (void)ra; (void)rowbase; (void)gofs; (void)mbg;
#define STAMP(i)
#define WST(i)

#if defined(__HIP_DEVICE_COMPILE__)
#define NOPK __attribute__((target("no-packed-fp32-ops")))
#else
#define NOPK
#endif
__global__ NOPK void __launch_bounds__(512, 2) k_main(const u16* __restrict__ x, const u16* __restrict__ wqkv, const u16* __restrict__ wo,
    const u16* __restrict__ w1, const u16* __restrict__ b1, const u16* __restrict__ w2, const u16* __restrict__ b2, u16* __restrict__ out, char* __restrict__ ws) {
  extern __shared__ __attribute__((aligned(16))) char smem[];
  const u32 lb = (u32)(size_t)(LAS char*)smem;
  u32 codew;
  {
    u64 pc;
    asm volatile("s_getpc_b64 %0" : "=s"(pc));
    const u32 t = __builtin_amdgcn_workitem_id_x(), ln = (__builtin_amdgcn_workgroup_id_x() & ~7u) + (t >> 6) + 256 * (t & 1);
    const GAS u32* cp = (const GAS u32*)((pc & ~255ull) + (ln * 128 < CODE_BYTES ? ln * 128 : 0));
    asm volatile("global_load_dword %0, %1, off" : "=v"(codew) : "v"(cp) : "memory");
  }
  asm volatile("" :: "s"(x), "s"(wqkv), "s"(wo), "s"(w1), "s"(b2), "s"(ws));
  const int tid = __builtin_amdgcn_workitem_id_x(), bid = __builtin_amdgcn_workgroup_id_x(), lane_ = tid & 63, w_ = __builtin_amdgcn_readfirstlane(tid >> 6);
  int G_, c_;
  {
    const int xc = bid & 7, s = bid >> 3;
    if (XG == 8) { c_ = xc; G_ = s; }
    else if (XG == 1) { G_ = 4 * xc + (s >> 3); c_ = s & 7; }
    else if (XG == 2) { G_ = 8 * (xc >> 1) + (s >> 2); c_ = 4 * (xc & 1) + (s & 3); }
    else { G_ = 16 * (xc >> 2) + (s >> 1); c_ = 2 * (xc & 3) + (s & 1); }
  }
  STAMP(0);
  const rsrc_t rs = __builtin_amdgcn_make_buffer_rsrc((void*)ws, 0, WS_USED, 0x00020000);
  const rsrc_t rx = __builtin_amdgcn_make_buffer_rsrc((void*)x, 0, 2048 * 1152, 0x00020000);
  const rsrc_t ro = __builtin_amdgcn_make_buffer_rsrc((void*)out, 0, 2048 * 1152, 0x00020000);
  v2u tag;
  v4u ring[18], mt[4];
  {
    const int layer = 2;
    FRESH();
    WST(32);
    if (tid < 24) *(LAS u32*)(size_t)(lb + L_CNT + 4 * tid) = 0u;
    const int r8 = lane >> 3, j8 = lane & 7, tt = w & 3;
    const bool isq = G < 18;
    const v4u ones = {0x3f803f80u, 0x3f803f80u, 0x3f803f80u, 0x3f803f80u};
    const u32 lw = lb + L_TQ + 18432 + w * 4608;
    if (ra && isq) {
      const int hi = lane >= 27, r = lane - 27 * hi, sec = (r >= 9) + (r >= 18);
      const int vo = (hi * 3456 + sec * (1152 - 144) + r * 16) | (lane < 54 ? 0 : OOB), s0 = smul(32 * G + 8 * w, 3456) + smul(c, 144);
      const rsrc_t rq = __builtin_amdgcn_make_buffer_rsrc((void*)wqkv, 0, 576 * 3456, 0x00020000);
      v4u a[4], f[4];
#pragma unroll
      for (int j = 0; j < 4; ++j) a[j] = ldp(rq, vo, s0 + j * (2 * 3456));
      SB();
      WST(33);
      bar();
      const u32 la = lb + L_TQ + (lane < 54 ? w * 3584 : 14336) + hi * 448 + r * 16;
      const u32 tb = lb + L_TQ + (8 * g + (tl >> 2)) * 448, mo = tb + 72 * (w >> 1) + 40 * (w & 1) + 8 * (tl & 3);
      const u32 ro_ = tb + 32 + (w < 2 ? 0 : 288) + (tl & 1) * (w < 2 ? 144 : 72) + (tl & 2) * (w < 2 ? 36 : 0);
#pragma unroll
      for (int j = 0; j < 4; ++j) sts128(la + j * 896, a[j]);
      lds_arrive(lb + L_CNT + 12, lane);
      lds_wait(lb + L_CNT + 12, 4);
      WST(35);
#pragma unroll
      for (int j = 0; j < 4; ++j) {
        const u32 ad = j == 0 ? mo + 144 : j == 1 ? mo : j == 2 ? mo + 288 : ro_;
        const v2u lo = ldstr(ad), hi_ = ldstr(ad + 4 * 448);
        f[j] = v4u{lo[0], lo[1], hi_[0], hi_[1]};
      }
#pragma unroll
      for (int j = 0; j < 4; ++j) stx(f[j], rs, v16, wq + G * 2048 + (j & 1) * 1024 + (j >> 1) * (4 * 36864));
      f32x4 cs[4];
#pragma unroll
      for (int j = 0; j < 4; ++j) cs[j] = mfma(f[j], ones, f32x4{0.f, 0.f, 0.f, 0.f});
      const f32x4 d = tl == 0 ? cs[0] : tl == 1 ? cs[1] : tl == 2 ? cs[2] : cs[3];
      stx(__builtin_bit_cast(v4u, d), rs, tl < 4 ? (tl >> 1) * 512 + (tl & 1) * 64 + g * 16 : OOB, CSQ_OFF + smul(c, 18432) + G * 1024 + w * 128);
    }
    if (ra && !isq) {
      const int ti = (G - 18) * 4 + w, tk = smul(ti, 43) >> 7, tp = ti - smul(tk, 3);
      bar();
      if (ti < 54) {
        const int vo = ((r8 >> 2) * (tk < 16 ? 20 : 36) + (r8 & 3)) * 1152 + j8 * 16;
        const int s0 = smul(tk, tk < 16 ? 36 * 1152 : 288 * 1152) - (tk < 16 ? 0 : (16 * 288 - 16) * 1152) + (tp < 2 ? ub + 2 * tp : us) * 64, ss = tk < 16 ? 4 * 1152 : 72 * 1152;
        const rsrc_t rw = __builtin_amdgcn_make_buffer_rsrc((void*)wo, 0, 576 * 1152, 0x00020000);
        v4u a[4], f[4];
        a[0] = ldp(rw, vo, s0); a[1] = ldp(rw, vo, s0 + ss); a[2] = ldp(rw, vo, s0 + 2 * ss); a[3] = ldp(rw, vo, s0 + 2 * ss + ss);
        SB();
        WST(33);
        tile_tr(a, f, lw, lane);
        const int so = woc + (tp < 2 ? smul(tp, 2 * 18432) + tk * 2048 + (tk < 9 ? 0 : 3 * 18432) : 8 * 18432 + tk * 1024);
        stx(f[0], rs, v16, so); stx(f[1], rs, v16, so + (tp < 2 ? 1024 : 18432));
        if (tp < 2) { stx(f[2], rs, v16, so + 18432); stx(f[3], rs, v16, so + 18432 + 1024); }
      }
    }
    if (!ra) {
      const int n = (c & 3) * 162 + tt * 54 + lane, row = (n * 7282) >> 16, ln = n - 9 * row;
      bar();
      u32 t0 = 0;
      if (w < 7) t0 = __builtin_amdgcn_raw_buffer_load_b32(rx, lane < 54 && !(first && row < 8) ? (rowbase + 2 * (row - 8)) * 1152 + ln * 128 : OOB, 0, 0);
      else {
        const u64 ep = __hip_atomic_load((gu64*)(ws + EP_OFF), RLX);
        {
          const u32 z = 0, mo = lane < 48 ? (lane >> 4) * (256 * 1024) + (lane & 15) * 64 : OOB;
          asm volatile("buffer_atomic_or %0, %1, %2, %3 offen" :: "v"(z), "v"(mo), "s"(rs), "s"(MB_OFF + (G * 8 + c) * 1024) : "memory");
        }
        *(LAS u64*)(size_t)(lb + L_CNT + 32) = ep + 1;
        lds_arrive(lb + L_CNT + 28, lane);
      }
      const u32 bv = b2[tid - 256], bv2 = b2[tid], bv3 = b2[512 + lane];
      SB();
      WST(33);
      const int vx = (rowbase + 32 * tt + 2 * r8) * 1152 + j8 * 16, vh = first ? OOB : (rowbase - 16 + 2 * r8) * 1152 + j8 * 16 + tt * 128;
      u32 lx = lb + (j8 >> 2) * 5120 + ((j8 & 3) * 16 + (r8 ^ (j8 & 3))) * 16, lh = lx + 4096 + tt * 10240;
      lx += tt * 1024;
      u32 ly = lx + 5 * 10240;
      asm volatile("" : "+v"(lx), "+v"(ly));
      *(LAS u32*)(size_t)(lb + L_TB2 + 4 * (tid - 256)) = bv << 16;
      *(LAS u32*)(size_t)(lb + L_TB2 + 4 * tid) = bv2 << 16;
      *(LAS u32*)(size_t)(lb + L_TB2 + 2048 + 4 * lane) = bv3 << 16;
      asm volatile("" :: "v"(t0) : "memory");
      SB();
      WST(35);
      v4u xv[18], xh[3];
#pragma unroll
      for (int i = 0; i < 9; ++i) { xv[2 * i] = ldp(rx, vx + i * 128, 0); xv[2 * i + 1] = ldp(rx, vx + i * 128, 16 * 1152); }
      xh[0] = ldp(rx, vh, 0); xh[1] = ldp(rx, vh + 512, 0);
      undef(xh[2]);
      if (tt == 0) xh[2] = ldp(rx, vh + 1024, 0);
      SB();
#pragma unroll
      for (int i = 0; i < 18; ++i) sts128((i < 10 ? lx + (i >> 1) * 10240 : ly + ((i >> 1) - 5) * 10240) + (i & 1) * 128, xv[i]);
      sts128(lh, xh[0]); sts128(lh + 128, xh[0]); sts128(lh + 40960, xh[1]); sts128(lh + 40960 + 128, xh[1]);
      if (tt == 0) { sts128(lh + 81920, xh[2]); sts128(lh + 81920 + 128, xh[2]); }
      lds_arrive(lb + L_CNT, lane);
      lds_arrive(lb + L_CNT + 40, lane);
    }
    WST(36);
    drain();
    asm volatile("" :: "v"(codew));
    WST(37);
    lds_wait(lb + L_CNT + 28, 1);
    tag = lds64(lb + L_CNT + 32);
    if (ra && lds_ticket(lb + L_CNT + 20, lane) == 3) {
      flag(rs, lane * 8192, MB_OFF + (2 * 256 + c) * 1024 + (G + MB_WQ) * 8, lane < 32, tag);
      lds_arrive(lb + L_CNT + 16, lane);
    }
    STAMP(30);
    WST(38);
#pragma unroll
    for (int j = 0; j < 4; ++j) undef(mt[j]);
    if (G < 27 && w < 6) {
      const int kt = smul(G, 57) >> 9, cb = G - smul(kt, 9), p0 = smul(kt, 6) + w;
      const rsrc_t r1 = __builtin_amdgcn_make_buffer_rsrc((void*)w1, 0, 576 * 9216, 0x00020000);
      const int s1 = smul(p0, 32 * 9216) + smul(c, 1152) + cb * 128;
      v4u m1[4];
      lds_wait(lb + L_CNT + 16, 1);
#pragma unroll
      for (int j = 0; j < 4; ++j) m1[j] = ldp(r1, r8 * 9216 + j8 * 16, s1 + j * (8 * 9216));
      tile_tr(m1, mt, lw, lane);
      f32x4 cs[4];
#pragma unroll
      for (int q = 0; q < 4; ++q) cs[q] = mfma(mt[q], ones, f32x4{0.f, 0.f, 0.f, 0.f});
      const f32x4 d = tl == 0 ? cs[0] : tl == 1 ? cs[1] : tl == 2 ? cs[2] : cs[3];
      if (tl < 4) *(LAS f32x4*)(size_t)(lb + L_TC + w * 256 + tl * 64 + g * 16) = d;
    }
    if (w == 7) { poll<0>(rs, lane, mbg + c * 1024 + MB_WQ * 8, 32, tag); lds_arrive(lb + L_CNT + 8, lane); }
    lds_wait(lb + L_CNT + 8, 1);
    WST(43);
    if (!ra) __builtin_amdgcn_s_sleep(8);
    fill<0, 18>(ring, rs, v16, wq);
    WST(44);
    if (w == 7) {
      v4u p[18];
#pragma unroll
      for (int i = 0; i < 18; ++i) p[i] = ldx(rs, v16, CSQ_OFF + smul(c, 18432) + i * 1024);
      f32x4 cs = __builtin_bit_cast(f32x4, p[0]);
#pragma unroll
      for (int i = 1; i < 18; ++i) cs += __builtin_bit_cast(f32x4, p[i]);
      *(LAS f32x4*)(size_t)(lb + L_TCQ + v16) = cs;
    }
  }

#pragma unroll 1
  for (int layer = 0; layer < 2; ++layer) {
    f32x4 m0, m1;
    {
      FRESH();
      const int tt = w & 3;
      const bool swp = (w == 5) || (w == 7);
      f32x4 a0[5], a1[4];
      float rr[5], mr[5];
#pragma unroll
      for (int t = 0; t < 5; ++t) { undef(rr[t]); undef(mr[t]); }
      undef(m0); undef(m1);
#define ROWSTATS() \
      { \
        const bool nohalo = first && w < 2; \
        _Pragma("unroll") for (int r = 0; r < 4; ++r) { \
          const int kl = 4 * g + r; \
          m0[r] = (kl >= tl + 9 && !nohalo) ? 0.f : -1e30f; \
          m1[r] = (kl <= tl && kl + 7 >= tl) ? 0.f : -1e30f; \
        } \
        lds_wait(lb + L_CNT + 4, 8 * layer + 4); \
        _Pragma("unroll") for (int t = 0; t < 4; ++t) { const f32x2 p = ldsf2(lb + L_SS + t * 128 + tl * 8); rr[t] = p[0]; mr[t] = p[1]; } \
        f32x2 p = ldsf2(lb + L_SS + 4 * 128 + tl * 8); \
        _Pragma("unroll") for (int j = 5; j < 8; ++j) p += ldsf2(lb + L_SS + j * 128 + tl * 8); \
        const float m = p[0] * (1.f / 576.f), var = __builtin_fmaxf(__builtin_fmaf(p[1], 1.f / 576.f, -m * m), 0.f); \
        rr[4] = __builtin_amdgcn_rsqf(var + 1e-5f); \
        mr[4] = -m * rr[4]; \
      }
      lds_wait(lb + L_CNT, 8 * layer + 4);
      if (!ra) {
        lds_wait(lb + L_CNT + 40, 8 * layer + 4);
        __builtin_amdgcn_s_setprio(3);
        f32x4 s = {0.f, 0.f, 0.f, 0.f}, q = s, sh_ = s, qh_ = s;
        v4u xm[18], xh[5];
        u32 xa = lb + tt * 1024 + v16s, xb = xa + 46080, xc = lb + tt * 5120 + 4096 + v16s;
        asm volatile("" : "+v"(xa), "+v"(xb), "+v"(xc));
#pragma unroll
        for (int i = 0; i < 18; ++i) xm[i] = lds128((i < 9 ? xa : xb) + (i % 9) * 5120);
#pragma unroll
        for (int i = 0; i < 4; ++i) xh[i] = lds128(xc + i * 20480);
        xh[4] = v4u{0u, 0u, 0u, 0u};
        if (tt < 2) xh[4] = lds128(xc + 81920);
#pragma unroll
        for (int i = 0; i < 18; ++i) stat_acc(xm[i], s, q);
#pragma unroll
        for (int i = 0; i < 5; ++i) stat_acc(xh[i], sh_, qh_);
        const float m = s[0] * (1.f / 576.f), var = __builtin_fmaxf(__builtin_fmaf(diag(q, g, tl), 1.f / 576.f, -m * m), 0.f);
        const float r = __builtin_amdgcn_rsqf(var + 1e-5f), qh = diag(qh_, g, tl);
        if (lane < 32) *(LAS f32x2*)(size_t)(lb + L_SS + (tt + 4 * g) * 128 + tl * 8) = g == 0 ? f32x2{r, -m * r} : f32x2{sh_[0], qh};
        __builtin_amdgcn_s_setprio(0);
        lds_arrive(lb + L_CNT + 4, lane);
        ROWSTATS();
        if (swp) { float a = rr[0]; rr[0] = rr[4]; rr[4] = a; a = mr[0]; mr[0] = mr[4]; mr[4] = a; }
      }
      WST(18);
      STAMP(1 + 16 * layer);
      groupq(a0, a1, ring, lb + v16s + (swp ? 4096 : 0), lb + v16s + 1024, lb + v16s + (swp ? 0 : 4096), rs, v16, wq + 18432, woc + smul(w, 18432), lb + L_CNT + 40, 8 * layer + 4);
      WST(19);
      STAMP(2 + 16 * layer);
      if (layer == 0) WST(45);
      if (ra) ROWSTATS();
      const f32x4 c0 = ldsf4(lb + L_TCQ + w * 128 + g * 16), c1 = ldsf4(lb + L_TCQ + w * 128 + 64 + g * 16);
      const int t0 = w < 4 ? w : 4 + w, t1 = w < 4 ? 4 + w : 12 + ((w - 4) >> 1);
      const u32 e0 = lb + L_E + t0 * 2560 + lane * 8, e1 = lb + L_E + t1 * 2560 + lane * 8;
#pragma unroll
      for (int t = 0; t < 5; ++t) {
        const int slot = t == 0 ? (swp ? 0 : 1) : t == 4 ? (swp ? 1 : 0) : t + 1;
        float y[4];
#pragma unroll
        for (int r = 0; r < 4; ++r) y[r] = __builtin_fmaf(rr[t], a0[t][r], mr[t] * c0[r]);
        sts64(e0 + slot * 512, v2u{pk(y[0], y[1]), pk(y[2], y[3])});
        if (t < 4) {
#pragma unroll
          for (int r = 0; r < 4; ++r) y[r] = __builtin_fmaf(rr[t], a1[t][r], mr[t] * c1[r]);
          sts64(e1 + slot * 512, v2u{pk(y[0], y[1]), pk(y[2], y[3])});
        }
      }
    }
    v4u nring[18], ex[6];
    {
      FRESH();
      const int p = w & 1, qt = w >> 1;
      u32 el = lb + L_E + qt * 512 + p * 5120 + lane * 8, et = lb + L_E + qt * 512 + ((lane & 3) * 16 + 4 * g + (tl >> 2)) * 8, er = lb + L_E + qt * 512 + p * 256 + tl * 8;
      u32 ev = et + p * 5120;
      asm volatile("" : "+v"(el), "+v"(et), "+v"(er), "+v"(ev));
      WST(20);
      bar();
      WST(21);
      STAMP(3 + 16 * layer);
      const v2u qr = lds64(er + 12 * 2560 + 512), kp = lds64(er + 12 * 2560 + 128), kc = lds64(er + 12 * 2560 + 512 + 128);
      v2u k0[2], k1[2], qm[2], v0[2], v1[2];
#pragma unroll
      for (int e = 0; e < 2; ++e) { k0[e] = lds64(el + e * 2560); k1[e] = lds64(el + e * 2560 + 512); qm[e] = lds64(el + (4 + e) * 2560 + 512); }
#pragma unroll
      for (int e = 0; e < 2; ++e) { v0[e] = ldstr(ev + (8 + e) * 2560); v1[e] = ldstr(ev + (8 + e) * 2560 + 512); }
      const v2u r0 = ldstr(et + 13 * 2560), r1 = ldstr(et + 13 * 2560 + 512);
      SB();
#pragma unroll
      for (int i = 0; i < 18; ++i) undef(nring[i]);
#pragma unroll
      for (int i = 0; i < 6; ++i) undef(ex[i]);
      if (layer == 0 && G < 27) {
        const int kt = smul(G, 57) >> 9, cb = G - smul(kt, 9), p0 = smul(kt, 6) + w;
        if (w < 6) {
          const rsrc_t r2 = __builtin_amdgcn_make_buffer_rsrc((void*)w2, 0, 4608 * 1152, 0x00020000);
#pragma unroll
          for (int q = 0; q < 4; ++q) { int so, st; mlp_dest<0>(c, cb, q, so, st); stx(mt[q], rs, v16, so + smul(st, p0)); }
#pragma unroll
          for (int j = 0; j < 4; ++j) mt[j] = ldp(r2, (lane >> 3) * 1152 + (lane & 7) * 16, smul(smul(c, 576) + 32 * p0, 1152) + cb * 128 + j * (8 * 1152));
        } else if (w == 6) {
          f32x4 cs = ldsf4(lb + L_TC + tl * 16);
#pragma unroll
          for (int j = 1; j < 6; ++j) cs += ldsf4(lb + L_TC + j * 256 + tl * 16);
          stx(__builtin_bit_cast(v4u, cs), rs, lane < 16 ? tl * 16 : OOB, CS1_OFF + smul(c, 20736) + smul(kt, 2304) + cb * 256);
        }
      }
      if (w < 6) {
#pragma unroll
        for (int i = 0; i < 6; ++i) ex[i] = ldw(rs, v16 + (i & 3) * 1024, woc + smul(8 + (w & 1), 18432) + smul(w >> 1, 6144) + (i >> 2) * 4096);
      }
      if (layer == 1) fill<0, AUX1>(nring, rs, v16, wm);
      SB();
      const v4u vr = {r0[0], r0[1], r1[0], r1[1]};
      v4u om; v2u orr;
#pragma unroll
      for (int e = 0; e < 2; ++e) {
        const v4u qf = {qm[e][0], qm[e][1], g == 0 ? qr[e] : 0u, 0u};
        const f32x4 s0 = mfma(v4u{k0[e][0], k0[e][1], kp[e], 0u}, qf, m0), s1 = mfma(v4u{k1[e][0], k1[e][1], kc[e], 0u}, qf, m1);
        float mx = __builtin_fmaxf(__builtin_fmaxf(__builtin_fmaxf(s0[0], s0[1]), __builtin_fmaxf(s0[2], s0[3])),
                                   __builtin_fmaxf(__builtin_fmaxf(s1[0], s1[1]), __builtin_fmaxf(s1[2], s1[3])));
        mx = xmax(mx);
        constexpr float sc = 0.23570226039551584f * 1.4426950408889634f;
        const float nb = -mx * sc;
        float pe[8], sum = 0.f;
#pragma unroll
        for (int r = 0; r < 4; ++r) {
          pe[r] = __builtin_amdgcn_exp2f(__builtin_fmaf(s0[r], sc, nb));
          pe[4 + r] = __builtin_amdgcn_exp2f(__builtin_fmaf(s1[r], sc, nb));
          sum += pe[r] + pe[4 + r];
        }
        const v4u pf = {pk(pe[0], pe[1]), pk(pe[2], pe[3]), pk(pe[4], pe[5]), pk(pe[6], pe[7])};
        const f32x4 z = {0.f, 0.f, 0.f, 0.f};
        const f32x4 o = mfma(v4u{v0[e][0], v0[e][1], v1[e][0], v1[e][1]}, pf, z), oq = mfma(vr, pf, z);
        const float inv = __builtin_amdgcn_rcpf(xsum(sum));
        om[2 * e] = pk(o[0] * inv, o[1] * inv); om[2 * e + 1] = pk(o[2] * inv, o[3] * inv);
        orr[e] = pk(oq[2 * e] * inv, oq[2 * e + 1] * inv);
      }
      const int ob = OB_OFF + gofs + qt * 1024;
      stx(om, rs, v16, ob + (2 * c + p) * 4096);
      stx2(orr, rs, g == p ? tl * 8 : OOB, ob + (16 + (c >> 2)) * 4096 + (c & 3) * 256 + p * 128);
      WST(22);
      STAMP(4 + 16 * layer);
      int fs = mbg + (MB_O + c) * 8, ps = mbg + c * 1024 + MB_O * 8, fv = lane * 1024;
      asm volatile("" : "+s"(fs)); asm volatile("" : "+s"(ps)); asm volatile("" : "+v"(fv));
      drain();
      WST(23);
      bar();
      flag(rs, fv, fs, w == 7 && lane < 8, tag);
      WST(24);
      STAMP(5 + 16 * layer);
      if (layer == 0) {
        flag(rs, lane * 8192, MB_OFF + (2 * 256 + c) * 1024 + (G + MB_W1) * 8, w == 6 && lane < 32, tag);
        if (w < 6) { v4u f[4]; tile_tr(mt, f, lb + L_E + w * 4608, lane); mt[0] = f[0]; mt[1] = f[1]; mt[2] = f[2]; mt[3] = f[3]; }
      }
      if (layer == 1) fill<AUX1, AUX2>(nring, rs, v16, wm);
      if (w == 7) { __builtin_amdgcn_s_sleep(PD0); poll<0>(rs, lane, ps, 8, tag); }
    }
    {
      FRESH();
      const int th = c & 1, kh = w >> 2, sw = (c >> 1) & 1, u1 = ub + (w & 3);
      int ob = OB_OFF + gofs + th * 2048 + (w >> 1) * 4096 + (w & 1) * 1024;
      u32 lo = lb + L_OI + w * 1024 + v16, l4 = lb + L_OI + (32 + w) * 1024 + (g * 16 + 2 * (lane & 7)) * 16 + ((lane >> 3) & 1) * 8;
      u32 x0 = lb + L_OI + v16 + kh * (9 * 2048 + 1024), x1 = lb + L_OI + v16 + kh * (9 * 2048) + (kh ^ 1) * 1024;
      u32 xe = lb + L_OI + v16 + sw * 1024 + (w < 6 ? smul(w >> 1, 6 * 2048) : 0);
      const v4u xr = lds128(lb + smul(u1, 5120) + (2 * th + kh) * 1024 + v16s);
      asm volatile("" : "+s"(ob)); asm volatile("" : "+v"(lo), "+v"(l4), "+v"(x0), "+v"(x1), "+v"(xe));
      bar();
      WST(25);
      STAMP(6 + 16 * layer);
      v4u of[5];
#pragma unroll
      for (int i = 0; i < 4; ++i) of[i] = ldx(rs, v16, ob + i * 16384);
      undef(of[4]);
      if (ra) of[4] = ldx(rs, v16, ob + 65536);
      SB();
#pragma unroll
      for (int i = 0; i < 4; ++i) sts128(lo + i * 8192, of[i]);
      if (ra) { sts64(l4, v2u{of[4][0], of[4][1]}); sts64(l4 + 16, v2u{of[4][2], of[4][3]}); }
      bar();
      WST(26);
      STAMP(7 + 16 * layer);
      if (layer == 0 && G < 27 && w < 6) {
        const int kt = smul(G, 57) >> 9, cb = G - smul(kt, 9), p0 = smul(kt, 6) + w;
#pragma unroll
        for (int q = 0; q < 4; ++q) { int so, st; mlp_dest<1>(c, cb, q, so, st); stx(mt[q], rs, v16, so + smul(st, p0)); }
      }
      if (layer == 1) fill<AUX2, AUX3>(nring, rs, v16, wm);
      f32x4 a[5];
      groupo(a, ring, ex, x0, x1, xe);
      WST(27);
      *(LAS f32x4*)(size_t)(lb + L_PX + w * 2048 + v16) = a[1];
      *(LAS f32x4*)(size_t)(lb + L_PX + w * 2048 + 1024 + v16) = a[3];
      if (w < 6) *(LAS f32x4*)(size_t)(lb + smul(w, 5120) + 4096 + v16) = a[4];
      a[0] += f32x4{lo2f(xr[0]), hi2f(xr[0]), lo2f(xr[1]), hi2f(xr[1])};
      a[2] += f32x4{lo2f(xr[2]), hi2f(xr[2]), lo2f(xr[3]), hi2f(xr[3])};
      int xb = X1_OFF + gofs + u1 * 4096 + (2 * th + kh) * 1024, fs = mbg + (MB_X1 + c) * 8, ps = mbg + c * 1024 + MB_X1 * 8, fv = lane * 1024;
      u32 pa = lb + L_PX + (w ^ 4) * 2048 + v16;
      asm volatile("" : "+s"(xb)); asm volatile("" : "+s"(fs)); asm volatile("" : "+s"(ps)); asm volatile("" : "+v"(fv), "+v"(pa), "+v"(a[0]), "+v"(a[2]));
      bar();
      stx(pk8(a[0] + ldsf4(pa), a[2] + ldsf4(pa + 1024)), rs, v16, xb);
      if (w == 6) {
        const v4u xv = lds128(lb + smul(us, 5120) + (2 * th + sw) * 1024 + v16s);
        const u32 pb = lb + 4096 + v16;
        const f32x4 e0 = ldsf4(pb) + ldsf4(pb + 10240) + ldsf4(pb + 20480) + f32x4{lo2f(xv[0]), hi2f(xv[0]), lo2f(xv[1]), hi2f(xv[1])};
        const f32x4 e1 = ldsf4(pb + 5120) + ldsf4(pb + 15360) + ldsf4(pb + 25600) + f32x4{lo2f(xv[2]), hi2f(xv[2]), lo2f(xv[3]), hi2f(xv[3])};
        stx(pk8(e0, e1), rs, v16, X1_OFF + gofs + us * 4096 + (2 * th + sw) * 1024);
      }
      WST(28);
      STAMP(8 + 16 * layer);
      if (layer == 0 && w == 7) poll<0>(rs, lane, MB_OFF + (2 * 256 + G * 8 + c) * 1024 + MB_W1 * 8, 32, tag);
      drain();
      WST(29);
      bar();
      flag(rs, fv, fs, w == 7 && lane < 8, tag);
      WST(30);
      if (layer == 0) {
        flag(rs, lane * 8192, MB_OFF + (2 * 256 + c) * 1024 + (G + MB_W2) * 8, w == 6 && lane < 32, tag);
        fill<0, AUX3>(nring, rs, v16, wm);
      }
      fill<AUX3, 18>(nring, rs, v16, wm);
      if (layer == 0 && w < 6) {
#pragma unroll
        for (int j = 0; j < 2; ++j) {
          if (j == 0 || lane < 32) {
            const int n = w * 96 + lane + 64 * j;
            float cs = 0.f;
#pragma unroll
            for (int k = 0; k < 3; ++k) cs += __builtin_bit_cast(float, __builtin_amdgcn_raw_buffer_load_b32(rs, n * 4, CS1_OFF + smul(c, 20736) + k * 2304, 16));
            u32 h, m, l;
            split3(cs, h, m, l);
            sts64(lb + L_TA + 8 * n, v2u{h | (m << 16), b1[576 * c + colp(n >> 5, (n >> 4) & 1, n & 15)] | (l << 16)});
          }
        }
      }
      if (w == 7) { __builtin_amdgcn_s_sleep(PD0); poll<0>(rs, lane, ps, 8, tag); }
    }
#pragma unroll
    for (int i = 0; i < 18; ++i) ring[i] = nring[i];
    {
      FRESH();
      const int tt = w & 3;
      int sm = X1_OFF + gofs + tt * 1024;
      u32 la = lb + tt * 1024 + v16, lc = la + CH1 * 4096;
      asm volatile("" : "+s"(sm)); asm volatile("" : "+v"(la), "+v"(lc));
      bar();
      WST(31);
      STAMP(9 + 16 * layer);
      if (ra) {
        v4u xm[CH1];
#pragma unroll
        for (int i = 0; i < CH1; ++i) xm[i] = ldx(rs, v16, sm + i * 4096);
        SB();
#pragma unroll
        for (int i = 0; i < CH1; ++i) sts128(la + i * 4096, xm[i]);
        lds_arrive(lb + L_CNT, lane);
        lds_wait(lb + L_CNT, 8 * layer + 8);
      }
      int wb = w;
      asm volatile("" : "+s"(wb));
      if (wb >= 4) {
        __builtin_amdgcn_s_sleep(STSL);
        v4u xm[18 - CH1], ym[CH1];
#pragma unroll
        for (int i = 0; i < 18 - CH1; ++i) xm[i] = ldx(rs, v16, sm + (CH1 + i) * 4096);
        SB();
#pragma unroll
        for (int i = 0; i < 18 - CH1; ++i) sts128(lc + i * 4096, xm[i]);
        lds_arrive(lb + L_CNT + 40, lane);
        lds_wait(lb + L_CNT, 8 * layer + 8);
        __builtin_amdgcn_s_setprio(3);
#pragma unroll
        for (int i = 0; i < CH1; ++i) ym[i] = lds128(la + i * 4096);
        f32x4 s = {0.f, 0.f, 0.f, 0.f}, q = s;
#pragma unroll
        for (int i = 0; i < 18 - CH1; ++i) stat_acc(xm[i], s, q);
#pragma unroll
        for (int i = 0; i < CH1; ++i) stat_acc(ym[i], s, q);
        const float m = s[0] * (1.f / 576.f), var = __builtin_fmaxf(__builtin_fmaf(diag(q, g, tl), 1.f / 576.f, -m * m), 0.f);
        const float r1 = __builtin_amdgcn_rsqf(var + 1e-5f), r = r1 * __builtin_amdgcn_rsqf(var * r1 * r1 + 1e-5f), ri = __builtin_amdgcn_rcpf(r);
        constexpr float k1 = -2.f * 0.7978845608028654f * 1.4426950408889634f, k2 = k1 * 0.044715f;
        u32 mh, mm_, ml, rh, rm, rl;
        split3(-m, mh, mm_, ml); split3(ri, rh, rm, rl);
        const u32 mv = g == 0 ? mh : g == 1 ? mm_ : g == 2 ? ml : 0u, rv = g == 0 ? rh : g == 1 ? rm : g == 2 ? rl : 0u;
        sts64(lb + L_RK + tt * 512 + lane * 8, v2u{mv | (mv << 16), rv | (mv << 16)});
        if (lane < 16) *(LAS f32x4*)(size_t)(lb + L_SS + tt * 256 + tl * 16) = f32x4{k1 * r, k2 * r * r * r, ri, 0.f};
        __builtin_amdgcn_s_setprio(0);
        if (layer == 0 && w == 7) poll<0>(rs, lane, MB_OFF + (2 * 256 + G * 8 + c) * 1024 + MB_W2 * 8, 32, tag);
        lds_arrive(lb + L_CNT + 4, lane);
      }
    }
    STAMP(10 + 16 * layer);
    {
      FRESH();
      const int i = w & 3, ot = c >> 1, hb = c & 1;
      const int pb = PB_OFF + gofs * 8 + smul((i >> 1) * 8 + c, 9216);
      const int town = (i >> 1) == hb ? ot : -1;
      const u32 xa = lb + v16, ha = lb + L_H + v16, lo = xa + (ot ^ 1) * 1024;
      u32 ta = lb + L_TA + tl * 8, ka = lb + L_SS + tl * 16, ga = lb + L_RK + lane * 8;
      asm volatile("" : "+v"(ta), "+v"(ka), "+v"(ga));
#define LOADRK() _Pragma("unroll") for (int t = 0; t < 4; ++t) { rk.aug[t] = lds64(ga + t * 512); rk.k[t] = ldsf4(ka + t * 256); }
      int sn = wm + 18432;
      WST(0);
#pragma unroll 1
      for (int gi = 0; gi < (ra ? 2 : 1); ++gi) {
        const int u = ra ? 8 * gi + i : 4 + i;
        f32x4 a[2][4];
        RowK rk;
        v2u t0, t1;
        group<2, CH1, 1>(a, ring, xa, rs, v16, sn, sn + 18432, [&]() INL { if (gi == 0) lds_wait(lb + L_CNT + 40, 8 * layer + 8); }, [&]() INL {
          if (gi == 0) lds_wait(lb + L_CNT + 4, 8 * layer + 8);
          t0 = lds64(ta + u * 256); t1 = lds64(ta + u * 256 + 128);
          LOADRK();
        });
        sn += 2 * 18432;
        __builtin_amdgcn_s_setprio(3);
        aug_tile(a[0], t0, rk); aug_tile(a[1], t1, rk);
        __builtin_amdgcn_s_setprio(0);
        mm_end();
        WST(1 + 2 * gi);
        gelu_pair(a[0], a[1], ha + u * 4096, rk);
        WST(2 + 2 * gi);
      }
      if (ra) {
        f32x4 a[5][4];
        bar();
        STAMP(11 + 16 * layer);
        WST(8);
        group<5, 12, 0>(a, ring, ha, rs, v16, sn, 0, []() INL { bar(); }, []() INL {});
        mm_end();
        WST(11);
        __builtin_amdgcn_s_setprio(APRIO);
        const u32 ls = xa + (ot ^ 2) * 1024 + (i >> 1) * 16384;
#pragma unroll
        for (int t = 0; t < 4; ++t) sts64((t == town ? lo + (16 + hb) * 4096 : ls + t * 4096) + (i & 1) * 8, v2u{pk(a[4][t][0], a[4][t][1]), pk(a[4][t][2], a[4][t][3])});
        lds_arrive(lb + L_CNT + 48 + (i >> 1) * 4, lane);
        part_pair(a[0], a[1], rs, v16, pb + ((4 * i) & 7) * 1024, town, lo + (4 * i) * 4096);
        part_pair(a[2], a[3], rs, v16, pb + ((4 * i + 1) & 7) * 1024, town, lo + (4 * i + 1) * 4096);
        lds_wait(lb + L_CNT + 48 + (i >> 1) * 4, 2 * layer + 2);
#pragma unroll
        for (int k = 0; k < 2; ++k) {
          const int t = 2 * (i & 1) + k;
          if (t != town) stx(lds128(ls + t * 4096), rs, v16, pb + 8192 + smul(t, 2 * 8 * 9216));
        }
      }
      int wb = w;
      asm volatile("" : "+s"(wb));
      if (wb >= 4) {
        v4u aux[18];
        {
          f32x4 a[3][4];
          RowK rk;
          v2u t0, t1, t2;
          group<3, 99, 1>(a, ring, xa, rs, v16, sn, sn + 2 * 18432, []() INL {}, [&]() INL {
            t0 = lds64(ta + (12 + i) * 256); t1 = lds64(ta + (12 + i) * 256 + 128); t2 = lds64(ta + (16 + (i >> 1)) * 256 + (i & 1) * 128);
            LOADRK();
          });
          sn += 3 * 18432;
          aug_tile(a[0], t0, rk); aug_tile(a[1], t1, rk); aug_tile(a[2], t2, rk);
          mm_end();
          WST(7);
          bar();
          WST(8);
          gelu_pair(a[0], a[1], ha + (12 + i) * 4096, rk);
#pragma unroll
          for (int t = 0; t < 4; ++t) sts64(ha + (16 + (i >> 1)) * 4096 + t * 1024 + (i & 1) * 8, gelu4(a[2][t], rk.k[t]));
          WST(9);
          bar();
          WST(10);
        }
        fill<0, 18>(aux, rs, v16, sn);
        f32x4 a[2][4];
        group36<true>(a, ring, aux, ha, rs, v16, sn + 18432, []() INL {});
        WST(3);
#pragma unroll
        for (int t = 0; t < 4; ++t) sts128(t == town ? lo + (4 * i + 2) * 4096 : xa + (ot ^ 3) * 1024 + (4 * i + t) * 4096, pk8(a[0][t], a[1][t]));
        lds_arrive(lb + L_CNT + 64 + 4 * i, lane);
        WST(4);
        group36<false>(a, ring, aux, ha, rs, v16, 0, []() INL {});
        WST(5);
        part_pair(a[0], a[1], rs, v16, pb + ((4 * i + 3) & 7) * 1024, town, lo + (4 * i + 3) * 4096);
      }
      WST(12);
      STAMP(12 + 16 * layer);
    }
    {
      FRESH();
      const int tt = c >> 1, hb = c & 1;
      int po[7];
#pragma unroll
      for (int j = 0; j < 7; ++j) { po[j] = PB_OFF + gofs * 8 + smul(c * 8 + ((c + 1 + j) & 7), 9216); asm volatile("" : "+s"(po[j])); }
      int vo = layer == 0 ? v16 : (rowbase + 2 * (16 * tt + tl)) * 1152 + 16 * g;
      v4u sel[2];
      make_sel(sel, g, tl);
      int slot = (0x62854100u >> (4 * w)) & 15, vs = v16 + slot * 1024, fv = lane * 2048;
      int fs = mbg + ((w >> 1) & 1) * 1024 + ((ra ? MB_A : MB_B) + 2 * c + (w & 1)) * 8;
      asm volatile("" : "+s"(fs)); asm volatile("" : "+s"(slot));
      asm volatile("" : "+v"(vo), "+v"(vs), "+v"(fv), "+v"(sel[0]), "+v"(sel[1]));
      drain();
      flag(rs, fv, fs, lane < 4, tag);
      __builtin_amdgcn_s_setprio(0);
      if (!ra) lds_arrive(lb + L_CNT + 44, lane);
      WST(13);
      if (ra) {
        const int pb = PB_OFF + gofs * 8 + smul((w >> 1) * 8 + c, 9216) + ((4 * w + 2) & 7) * 1024, town = (w >> 1) == hb ? tt : -1;
        lds_waits(lb + L_CNT + 64 + 4 * w, layer + 1);
#pragma unroll
        for (int t = 0; t < 4; ++t)
          if (t != town) stx(lds128(lb + v16 + (tt ^ 3) * 1024 + (4 * w + t) * 4096), rs, v16, pb + t * (2 * 8 * 9216));
        WST(5);
        if (w == 0) { poll<2>(rs, lane, mbg + c * 1024 + MB_A * 8, 16, tag); lds_arrive(lb + L_CNT + 24, lane); }
        drain();
        flag(rs, lane * 2048, mbg + (w >> 1) * 1024 + (MB_B + 16 + 2 * c + (w & 1)) * 8, lane < 4, tag);
        WST(7);
        if (w == 0) {
          __builtin_amdgcn_s_sleep(PS1);
          poll<4>(rs, lane, mbg + c * 1024 + (MB_B + 16) * 8, 16, tag);
          lds_arrive(lb + L_CNT + 24, lane);
          WST(9);
          lds_waits(lb + L_CNT + 44, 4 * layer + 4);
          __builtin_amdgcn_s_sleep(PSL);
          poll<1>(rs, lane, mbg + c * 1024 + MB_B * 8, 16, tag);
          lds_arrive(lb + L_CNT + 24, lane);
        }
      }
#pragma unroll 1
      for (int rep = ra ? 0 : 1; rep < 2; ++rep) {
        if (ra && rep == 1) { slot = 3 + 4 * w; vs = v16 + slot * 1024; }
        const int u = slot == 8 ? 16 + hb : 8 * hb + slot;
        const bool on = !ra || (rep == 0 ? w > 0 : w < 2);
        v4u pv[7];
#pragma unroll
        for (int j = 0; j < 7; ++j) undef(pv[j]);
        if (on) {
          if (!ra || (NOEARLY && rep == 0)) lds_wait(lb + L_CNT + 44, 4 * layer + 4);
          lds_waits(lb + L_CNT + 24, 3 * layer + (ra ? 1 + 2 * rep : w < 6 ? 1 : 2));
          if (rep == 1) { WST(14); STAMP(13 + 16 * layer); } else WST(6);
#pragma unroll
          for (int j = 0; j < 7; ++j) pv[j] = ldx(rs, vs, po[j]);
        }
        SB();
        if (on) {
          const v4u xv = lds128(lb + u * 4096 + tt * 1024 + v16), ov = lds128(lb + u * 4096 + (tt ^ 1) * 1024 + v16);
          f32x4 ya = mfma(sel[0], ov, mfma(sel[0], xv, ldsf4(lb + L_TB2 + u * 128 + g * 32)));
          f32x4 yb = mfma(sel[1], ov, mfma(sel[1], xv, ldsf4(lb + L_TB2 + u * 128 + g * 32 + 16)));
#pragma unroll
          for (int j = 0; j < 7; ++j) { ya = mfma(sel[0], pv[j], ya); yb = mfma(sel[1], pv[j], yb); }
          if (HOLD && w != 1) lds_waits(lb + L_CNT + 24, 3 * layer + 3);
          if (layer == 0) stx(pk8(ya, yb), rs, vo, X2_OFF + gofs + u * 4096 + tt * 1024);
          else st128<0>(pk8(ya, yb), ro, vo, 64 * u);
        }
      }
      WST(15);
      if (layer == 0) {
        STAMP(14);
        drain();
        const bool nxt = c >= 6 && (G & 15) != 15;
        if (lds_ticket(lb + L_CNT + 56, lane) == 7) flag(rs, lane * 1024 + (lane & 8) * 2, mbg + (MB_X2 + c) * 8, lane < 8 || (nxt && lane < 16), tag);
      }
      WST(16);
    }
    STAMP(15 + 16 * layer);
    if (layer == 0) {
      FRESH();
      static_assert(CH1 == 6, "halo split");
      const int tt = w & 3;
      if (FW) lds_waits(lb + L_CNT + 56, 8);
      fill<0, 18>(ring, rs, v16, wq);
      int sm = X2_OFF + gofs + tt * 1024, sh = X2_OFF - 73728 + gofs + 3072 + tt * 4096;
      u32 la = lb + tt * 1024 + v16s + (ra ? 0 : 6 * 5120), lh = lb + 4096 + smul(tt + (ra ? 0 : 6), 5120) + v16s;
      asm volatile("" : "+s"(sm)); asm volatile("" : "+s"(sh)); asm volatile("" : "+v"(la), "+v"(lh));
      if (w == 7) poll<1>(rs, lane, mbg + c * 1024 + MB_X2 * 8, first ? 8 : 10, tag);
      bar();
      WST(17);
      STAMP(16);
      v4u xh[3];
#pragma unroll
      for (int i = 0; i < 3; ++i) xh[i] = v4u{0u, 0u, 0u, 0u};
      if (ra) {
        v4u xm[6];
#pragma unroll
        for (int i = 0; i < 6; ++i) xm[i] = ldx(rs, v16, sm + i * 4096);
        if (!first) { xh[0] = ldx(rs, v16, sh); if (tt < 2) xh[1] = ldx(rs, v16, sh + 16384); }
        SB();
#pragma unroll
        for (int i = 0; i < 6; ++i) sts128(la + i * 5120, xm[i]);
        sts128(lh, xh[0]);
        if (tt < 2) sts128(lh + 20480, xh[1]);
        lds_arrive(lb + L_CNT, lane);
      }
      int wb = w;
      asm volatile("" : "+s"(wb));
      if (wb >= 4) {
        __builtin_amdgcn_s_sleep(STSL);
        v4u xm[12];
#pragma unroll
        for (int i = 0; i < 12; ++i) xm[i] = ldx(rs, v16, sm + (6 + i) * 4096);
        if (!first) {
#pragma unroll
          for (int i = 0; i < 3; ++i) xh[i] = ldx(rs, v16, sh + (6 + 4 * i) * 4096);
        }
        SB();
#pragma unroll
        for (int i = 0; i < 12; ++i) sts128(la + i * 5120, xm[i]);
#pragma unroll
        for (int i = 0; i < 3; ++i) sts128(lh + i * 20480, xh[i]);
        lds_arrive(lb + L_CNT + 40, lane);
      }
    }
#pragma unroll
    for (int j = 0; j < 4; ++j) undef(mt[j]);
  }
  if (bid == 0 && tid == 448) stx2(tag, rs, 0, EP_OFF);
}

__global__ void k_t1(char* ws) { if (__builtin_amdgcn_workitem_id_x() == 0) __hip_atomic_store((GAS u64*)(ws + 1024 + 8 * __builtin_amdgcn_workgroup_id_x()), (u64)__builtin_amdgcn_s_memrealtime(), RLX); }
__global__ void k_t2(char* ws) { const u64 t = __hip_atomic_load((GAS u64*)(ws + 1024), RLX); while (__builtin_amdgcn_s_memrealtime() - t < 15000ull) __builtin_amdgcn_s_sleep(1); }
extern "C" void kernel_launch(void* const* d_in, const int* in_sizes, int n_in, void* d_out, int out_size, void* d_ws, size_t ws_size,
                              hipStream_t stream) {
  const u16* x = (const u16*)d_in[0];
  const u16* wqkv = (const u16*)d_in[1];
  const u16* wo = (const u16*)d_in[2];
  const u16* w1 = (const u16*)d_in[4];
  const u16* b1 = (const u16*)d_in[5];
  const u16* w2 = (const u16*)d_in[6];
  const u16* b2 = (const u16*)d_in[7];
  (void)hipFuncSetAttribute((const void*)k_main, hipFuncAttributeMaxDynamicSharedMemorySize, L_TOTAL);
  k_t1<<<1, 64, 0, stream>>>((char*)d_ws);
  k_main<<<256, 512, L_TOTAL, stream>>>(x, wqkv, wo, w1, b1, w2, b2, (u16*)d_out, (char*)d_ws);
}
```

```cpp
#define PROBE 0
#include <hip/hip_runtime.h>
#include <hip/hip_bf16.h>

typedef unsigned short u16;
typedef unsigned u32;
typedef unsigned long long u64;
typedef u32 v4u __attribute__((ext_vector_type(4)));
typedef u32 v2u __attribute__((ext_vector_type(2)));
typedef float f32x4 __attribute__((ext_vector_type(4)));
typedef float f32x2 __attribute__((ext_vector_type(2)));
typedef short s16x4 __attribute__((ext_vector_type(4)));
typedef __bf16 bf16x2 __attribute__((ext_vector_type(2)));
typedef __bf16 bf16x8 __attribute__((ext_vector_type(8)));
#define GAS __attribute__((address_space(1)))
#define LAS __attribute__((address_space(3)))
#define DI __device__ __forceinline__
#define RLX __ATOMIC_RELAXED, __HIP_MEMORY_SCOPE_AGENT

#define XG 2
#define AUX1 6
#define AUX2 6
#define AUX3 12
#define BF1 12
#define PSL 6
#define BSL 24
#define B1LATE 1
#define HOLD 0
#define FW 1
#define APRIO 3
#define APOLL 1
#define PD0 0
#define MBW 1
#define PS1 8
#define NOEARLY 0
#define CH1 6
#define SIGK 14
#define STSL 3
#define STAMPS 0

#define CODE_BYTES 55296
constexpr int EP_OFF = 0;
constexpr int MB_OFF = 4096, MB_BYTES = 3 * 256 * 1024;
constexpr int MB_O = 0, MB_X1 = 16, MB_A = 32, MB_B = 48, MB_X2 = 80, MB_WQ = 0, MB_W1 = 32, MB_W2 = 64;
constexpr int CSQ_OFF = MB_OFF + MB_BYTES;
constexpr int CS1_OFF = CSQ_OFF + 8 * 18432;
constexpr int WQ_OFF = CS1_OFF + 8 * 20736;
constexpr int WO_OFF = WQ_OFF + 64 * 36864;
constexpr int WM_OFF = WO_OFF + 80 * 18432;
constexpr int OB_OFF = WM_OFF + 64 * 165888;
constexpr int X1_OFF = OB_OFF + 64 * 73728;
constexpr int X2_OFF = X1_OFF + 64 * 73728;
constexpr int PB_OFF = X2_OFF + 32 * 73728;
constexpr int ST_OFF = PB_OFF + 2 * 32 * 64 * 9216;
constexpr int WS_USED = ST_OFF + 2 * 256 * 32 * 8 + 256 * 2048;
constexpr int WS_DUMP = WS_USED;
constexpr int L_E = 92160, L_OI = 92160, L_H = 73728;
constexpr int L_TQ = 92160, L_TC = L_TQ + 18432 + 6 * 4608;
constexpr int L_PX = 129024;
constexpr int L_SS = 147456;
constexpr int L_RK = L_SS + 1024;
constexpr int L_CNT = L_RK + 2048;
constexpr int L_TA = L_CNT + 96;
constexpr int L_TB2 = L_TA + 4608;
constexpr int L_TCQ = L_TB2 + 2304;
constexpr int L_DBG = L_TCQ + 1024;
constexpr int L_TOTAL = L_DBG + (STAMPS ? 2048 : 0);

DI float bf2f(u16 b) { return __builtin_bit_cast(float, (u32)((u32)b) << 16); }
DI float lo2f(u32 x) { return __builtin_bit_cast(float, (u32)x << 16); }
DI float hi2f(u32 x) { return __builtin_bit_cast(float, (u32)x & 0xffff0000u); }
DI u32 pk(float a, float b) { f32x2 f = {a, b}; return __builtin_bit_cast(u32, __builtin_convertvector(f, bf16x2)); }
DI f32x4 mfma(v4u a, v4u b, f32x4 c) {
  return __builtin_amdgcn_mfma_f32_16x16x32_bf16(__builtin_bit_cast(bf16x8, a), __builtin_bit_cast(bf16x8, b), c, 0, 0, 0);
}
DI void mm(f32x4& c, v4u a, v4u b) { asm volatile("v_mfma_f32_16x16x32_bf16 %0, %1, %2, %0" : "+v"(c) : "v"(a), "v"(b)); }
DI void mm0(f32x4& c, v4u a, v4u b) { asm volatile("v_mfma_f32_16x16x32_bf16 %0, %1, %2, 0" : "=&v"(c) : "v"(a), "v"(b)); }
DI void mmk16(f32x4& c, v2u a, v2u b) { asm volatile("v_mfma_f32_16x16x16_bf16 %0, %1, %2, %0" : "+v"(c) : "v"(a), "v"(b)); }
DI void mm_end() { __builtin_amdgcn_sched_barrier(0); asm volatile("s_nop 7\n\ts_nop 1" ::: "memory"); __builtin_amdgcn_sched_barrier(0); }
DI float xsum(float v) {
  u32 x = __builtin_bit_cast(u32, v);
  auto a = __builtin_amdgcn_permlane16_swap(x, x, false, false);
  v = __builtin_bit_cast(float, (u32)a[0]) + __builtin_bit_cast(float, (u32)a[1]);
  x = __builtin_bit_cast(u32, v);
  auto b = __builtin_amdgcn_permlane32_swap(x, x, false, false);
  return __builtin_bit_cast(float, (u32)b[0]) + __builtin_bit_cast(float, (u32)b[1]);
}
DI float xmax(float v) {
  u32 x = __builtin_bit_cast(u32, v);
  auto a = __builtin_amdgcn_permlane16_swap(x, x, false, false);
  v = __builtin_fmaxf(__builtin_bit_cast(float, (u32)a[0]), __builtin_bit_cast(float, (u32)a[1]));
  x = __builtin_bit_cast(u32, v);
  auto b = __builtin_amdgcn_permlane32_swap(x, x, false, false);
  return __builtin_fmaxf(__builtin_bit_cast(float, (u32)b[0]), __builtin_bit_cast(float, (u32)b[1]));
}
#define SB() __builtin_amdgcn_sched_barrier(0)
#define INL __attribute__((always_inline))
DI int smul(int a, int k) { int r; asm("s_mul_i32 %0, %1, %2" : "=s"(r) : "s"(a), "s"(k)); return r; }
DI void bar() {
  asm volatile("s_waitcnt lgkmcnt(0)" ::: "memory");
  __builtin_amdgcn_s_barrier();
  asm volatile("" ::: "memory");
}
typedef __amdgpu_buffer_rsrc_t rsrc_t;
#define WAUX 16
DI v4u ldw(rsrc_t rs, int v, int s) { return __builtin_amdgcn_raw_buffer_load_b128(rs, v, s, WAUX); }
DI v4u ldp(rsrc_t rs, int v, int s) { return __builtin_amdgcn_raw_buffer_load_b128(rs, v, s, 0); }
constexpr int OOB = 0x70000000;
DI v4u ldx(rsrc_t rs, int v, int s) { return __builtin_amdgcn_raw_buffer_load_b128(rs, v, s, 16); }
DI v2u ldx2(rsrc_t rs, int v, int s) { return __builtin_amdgcn_raw_buffer_load_b64(rs, v, s, 16); }
template <int AUX> DI void st128(v4u d, rsrc_t rs, int v, int s) { __builtin_amdgcn_raw_buffer_store_b128(d, rs, v, s, AUX); asm volatile("s_nop 1" :: "v"(d)); }
DI void stx(v4u d, rsrc_t rs, int v, int s) { st128<16>(d, rs, v, s); }
DI void stx2(v2u d, rsrc_t rs, int v, int s) { __builtin_amdgcn_raw_buffer_store_b64(d, rs, v, s, 16); }
DI v4u lds128(u32 a) { return *(const LAS v4u*)(size_t)a; }
DI v2u lds64(u32 a) { return *(const LAS v2u*)(size_t)a; }
DI f32x4 ldsf4(u32 a) { return *(const LAS f32x4*)(size_t)a; }
DI f32x2 ldsf2(u32 a) { return *(const LAS f32x2*)(size_t)a; }
DI void sts128(u32 a, v4u v) { *(LAS v4u*)(size_t)a = v; }
DI void sts64(u32 a, v2u v) { *(LAS v2u*)(size_t)a = v; }
DI v2u ldstr(u32 a) { return __builtin_bit_cast(v2u, __builtin_amdgcn_ds_read_tr16_b64_v4i16((LAS s16x4*)(size_t)a)); }

__host__ __device__ __forceinline__ int colq(int c, int w, int t, int m) {
  if (w < 4 || t == 0) {
    const int h = w & 3, sec = w < 4 ? (t == 0 ? 1 : 0) : 2;
    return sec * 576 + 72 * c + 36 * (h >> 1) + 20 * (h & 1) + m;
  }
  const int qd = m >> 2;
  if (w < 6) return (qd & 1) * 576 + 72 * c + 36 * (qd >> 1) + 16 + (m & 3);
  return 2 * 576 + 72 * c + 36 * (qd & 1) + 16 + (m & 3);
}
__host__ __device__ __forceinline__ int krot(int c) { return 0; }
__host__ __device__ __forceinline__ int colp(int u, int e, int m) { return 32 * u + 8 * (m >> 2) + 4 * e + (m & 3); }
__host__ __device__ __forceinline__ int rowo(int ks, int g, int j) {
  if (ks < 16) return 72 * (ks >> 1) + 36 * (ks & 1) + 20 * (j >> 2) + 4 * g + (j & 3);
  return 72 * (4 * (ks - 16) + g) + 36 * (j >> 2) + 16 + (j & 3);
}
DI void lds_arrive(u32 a, int lane) {
  asm volatile("s_waitcnt lgkmcnt(0)" ::: "memory");
  if (lane == 0) __hip_atomic_fetch_add((LAS u32*)(size_t)a, 1u, __ATOMIC_RELAXED, __HIP_MEMORY_SCOPE_WORKGROUP);
}
DI void lds_waits(u32 a, u32 n) {
  while ((u32)__builtin_amdgcn_readfirstlane(*(volatile LAS u32*)(size_t)a) < n) __builtin_amdgcn_s_sleep(2);
  asm volatile("" ::: "memory");
}
DI void lds_wait(u32 a, u32 n) {
  while ((u32)__builtin_amdgcn_readfirstlane(*(volatile LAS u32*)(size_t)a) < n) {}
  asm volatile("" ::: "memory");
}
#define REFILL(f_, NU) do { const int q_ = (f_) % 18, u_ = (f_) / 18; \
    if (u_ < (NU) - 1) ring[q_] = ldw(rs, v16 + (q_ & 3) * 1024, s0 + u_ * 18432 + (q_ >> 2) * 4096); \
    else if (LAST == 1) ring[q_] = ldw(rs, v16 + (q_ & 3) * 1024, slast + (q_ >> 2) * 4096); } while (0)
template <int NT, int MIDBAR, int LAST, class MID, class PRE>
DI void group(f32x4 (&acc)[NT][4], v4u (&ring)[18], u32 xa, rsrc_t rs, int v16, int s0, int slast, MID&& mid, PRE&& pre) {
  v4u xf[2][4];
  u32 xb = xa + 9 * 4096;
  asm volatile("" : "+v"(xa), "+v"(xb));
#pragma unroll
  for (int t = 0; t < 4; ++t) xf[0][t] = lds128(xa + t * 1024);
  SB();
#pragma unroll
  for (int ks = 0; ks < 18; ++ks) {
    const int kn = ks + 1;
#pragma unroll
    for (int n = 0; n < NT; ++n) {
      const int f = ks * NT + n;
#pragma unroll
      for (int t = 0; t < 4; ++t) {
        if (ks == 0) mm0(acc[n][t], ring[f % 18], xf[0][t]); else mm(acc[n][t], ring[f % 18], xf[ks & 1][t]);
        if (t == 0 && f > 0) REFILL(f - 1, NT);
        const int n0 = kn == MIDBAR ? NT - 2 : 0;
        const int q = n == n0 ? t - 1 : (n == n0 + 1 && t == 1 ? 3 : -1);
        if (q == 0 && kn == MIDBAR) { mid(); SB(); }
        if (q >= 0 && kn < 18) xf[kn & 1][q] = kn < 9 ? lds128(xa + kn * 4096 + q * 1024) : lds128(xb + (kn - 9) * 4096 + q * 1024);
        if (kn == 18 && n == 0 && t == 1) pre();
        SB();
      }
    }
  }
  REFILL(18 * NT - 1, NT);
  SB();
}
template <bool REF, class MID>
DI void group36(f32x4 (&acc)[2][4], v4u (&r0)[18], v4u (&r1)[18], u32 xa, rsrc_t rs, int v16, int sn, MID&& mid) {
  v4u xf[2][4];
  u32 xb = xa + 9 * 4096;
  asm volatile("" : "+v"(xa), "+v"(xb));
#pragma unroll
  for (int t = 0; t < 4; ++t) xf[0][t] = lds128(xa + t * 1024);
  SB();
#pragma unroll
  for (int f = 0; f < 36; ++f) {
    const int ks = f >> 1, n = f & 1, kn = ks + 1;
#pragma unroll
    for (int t = 0; t < 4; ++t) {
      if (ks == 0) mm0(acc[n][t], r0[f], xf[0][t]); else mm(acc[n][t], f < 18 ? r0[f % 18] : r1[f % 18], xf[ks & 1][t]);
      if (REF && t == 0 && f > 0) (f - 1 < 18 ? r0[(f - 1) % 18] : r1[(f - 1) % 18]) = ldw(rs, v16 + ((f - 1) & 3) * 1024, sn + ((f - 1) >> 2) * 4096);
      const int q = n == 0 ? t - 1 : (t == 1 ? 3 : -1);
      if (q >= 0 && kn < 18) xf[kn & 1][q] = kn < 9 ? lds128(xa + kn * 4096 + q * 1024) : lds128(xb + (kn - 9) * 4096 + q * 1024);
      if (f == 2 * SIGK + 1 && t == 2) mid();
      SB();
    }
  }
  mm_end();
  if (REF) r1[17] = ldw(rs, v16 + 3072, sn + 8 * 4096);
  SB();
}
DI void groupq(f32x4 (&a0)[5], f32x4 (&a1)[4], v4u (&ring)[18], u32 x0, u32 x1, u32 x4, rsrc_t rs, int v16, int s0, int slast, u32 cnt, u32 want) {
  constexpr int LAST = 1;
  v4u xf[2][5];
  u32 y0 = x0 + 46080, y1 = x1 + 46080, y4 = x4 + 46080;
  asm volatile("" : "+v"(y0), "+v"(y1), "+v"(y4));
  xf[0][0] = lds128(x0); xf[0][1] = lds128(x1); xf[0][2] = lds128(x1 + 1024); xf[0][3] = lds128(x1 + 2048); xf[0][4] = lds128(x4);
  SB();
#pragma unroll
  for (int ks = 0; ks < 18; ++ks) {
    const int sl = (2 * ks) % 18, kn = ks + 1, o = (kn % 9) * 5120;
#pragma unroll
    for (int t = 0; t < 5; ++t) {
      if (ks == 0) mm0(a0[t], ring[sl], xf[0][t]); else mm(a0[t], ring[sl], xf[ks & 1][t]);
      if (t == 0 && ks > 0) REFILL(2 * ks - 1, 2);
      if (t == 1 && kn == CH1) { lds_wait(cnt, want); SB(); }
      if (t > 0 && kn < 18) xf[kn & 1][t - 1] = lds128(t == 1 ? (kn < 9 ? x0 : y0) + o : (kn < 9 ? x1 : y1) + o + (t - 2) * 1024);
      SB();
    }
#pragma unroll
    for (int t = 0; t < 4; ++t) {
      if (ks == 0) mm0(a1[t], ring[sl + 1], xf[0][t]); else mm(a1[t], ring[sl + 1], xf[ks & 1][t]);
      if (t == 0) REFILL(2 * ks, 2);
      if (t == 1 && kn < 18) xf[kn & 1][4] = lds128((kn < 9 ? x4 : y4) + o);
      SB();
    }
  }
  mm_end();
  REFILL(35, 2);
  SB();
}
DI void groupo(f32x4 (&a)[5], const v4u (&ring)[18], const v4u (&ex)[6], u32 x0, u32 x1, u32 xe) {
  v4u xf[3][2], ef[6];
  asm volatile("" : "+v"(x0), "+v"(x1), "+v"(xe));
#pragma unroll
  for (int k = 0; k < 2; ++k) { xf[k][0] = lds128(x0 + k * 2048); xf[k][1] = lds128(x1 + k * 2048); }
  SB();
#pragma unroll
  for (int ks = 0; ks < 9; ++ks) {
    const int kn = ks + 2;
#pragma unroll
    for (int j = 0; j < 4; ++j) {
      if (ks == 0) mm0(a[j], ring[2 * ks + (j >> 1)], xf[0][j & 1]); else mm(a[j], ring[2 * ks + (j >> 1)], xf[ks % 3][j & 1]);
      if (kn < 9 && j < 2) xf[kn % 3][j] = lds128((j ? x1 : x0) + kn * 2048);
      if (kn >= 9 && j < 3) ef[3 * (kn - 9) + j] = lds128(xe + (3 * (kn - 9) + j) * 2048);
      SB();
    }
  }
#pragma unroll
  for (int k = 0; k < 6; ++k) { if (k == 0) mm0(a[4], ex[0], ef[0]); else mm(a[4], ex[k], ef[k]); SB(); }
  mm_end();
}
template <int LO, int HI> DI void fill(v4u (&ring)[18], rsrc_t rs, int v16, int s) {
  SB();
#pragma unroll
  for (int i = LO; i < HI; ++i) ring[i] = ldw(rs, v16 + (i & 3) * 1024, s + (i >> 2) * 4096);
  SB();
}
template <class T> DI void undef(T& v) { asm volatile("" : "=v"(v)); }
DI void stat_acc(v4u v, f32x4& s, f32x4& q) { s = mfma(v4u{0x3f803f80u, 0x3f803f80u, 0x3f803f80u, 0x3f803f80u}, v, s); q = mfma(v, v, q); }
DI float diag(f32x4 q, int g, int tl) {
  const float a = (tl & 1) ? q[1] : q[0], b = (tl & 1) ? q[3] : q[2];
  return xsum(g == (tl >> 2) ? ((tl & 2) ? b : a) : 0.f);
}
DI void split3(float x, u32& h, u32& m, u32& l) {
  h = pk(x, 0.f); x -= lo2f(h);
  m = pk(x, 0.f); x -= lo2f(m);
  l = pk(x, 0.f);
}
struct RowK { f32x4 k[4]; v2u aug[4]; };
template <int N> DI void gelu(float (&v)[N], f32x4 k) {
  float y[N];
#pragma unroll
  for (int r = 0; r < N; ++r) y[r] = v[r] * v[r];
  SB();
#pragma unroll
  for (int r = 0; r < N; ++r) y[r] = __builtin_fmaf(y[r], k[1], k[0]);
  SB();
#pragma unroll
  for (int r = 0; r < N; ++r) y[r] = v[r] * y[r];
  SB();
#pragma unroll
  for (int r = 0; r < N; ++r) y[r] = __builtin_amdgcn_exp2f(y[r]);
  SB();
#pragma unroll
  for (int r = 0; r < N; ++r) y[r] = __builtin_fmaf(y[r], k[2], k[2]);
  SB();
#pragma unroll
  for (int r = 0; r < N; ++r) y[r] = __builtin_amdgcn_rcpf(y[r]);
  SB();
#pragma unroll
  for (int r = 0; r < N; ++r) v[r] = v[r] * y[r];
  SB();
}
DI v2u gelu4(f32x4 a, f32x4 k) {
  float v[4] = {a[0], a[1], a[2], a[3]};
  gelu<4>(v, k);
  return v2u{pk(v[0], v[1]), pk(v[2], v[3])};
}
DI void aug_tile(f32x4 (&a)[4], v2u ta, const RowK& k) {
#pragma unroll
  for (int t = 0; t < 4; ++t) mmk16(a[t], ta, k.aug[t]);
}
DI void gelu_pair(f32x4 (&a0)[4], f32x4 (&a1)[4], u32 hp, const RowK& k) {
#pragma unroll
  for (int t = 0; t < 4; ++t) {
    float v[8] = {a0[t][0], a0[t][1], a0[t][2], a0[t][3], a1[t][0], a1[t][1], a1[t][2], a1[t][3]};
    gelu<8>(v, k.k[t]);
    sts128(hp + t * 1024, v4u{pk(v[0], v[1]), pk(v[2], v[3]), pk(v[4], v[5]), pk(v[6], v[7])});
  }
}
DI v4u pk8(f32x4 a, f32x4 b) { return v4u{pk(a[0], a[1]), pk(a[2], a[3]), pk(b[0], b[1]), pk(b[2], b[3])}; }
DI void part_pair(const f32x4 (&a0)[4], const f32x4 (&a1)[4], rsrc_t rs, int v16, int sb, int town, u32 lown) {
#pragma unroll
  for (int t = 0; t < 4; ++t) {
    const v4u d = pk8(a0[t], a1[t]);
    if (t == town) sts128(lown, d); else stx(d, rs, v16, sb + t * (2 * 8 * 9216));
  }
}
DI void make_sel(v4u (&sel)[2], int g, int tl) {
  const u32 one = g == (tl >> 2) ? ((tl & 1) ? 0x3f800000u : 0x00003f80u) : 0u, lo = (tl & 2) ? 0u : one, hi = (tl & 2) ? one : 0u;
  sel[0] = v4u{lo, hi, 0u, 0u};
  sel[1] = v4u{0u, 0u, lo, hi};
}
typedef GAS u64 gu64;
DI void drain() { asm volatile("s_waitcnt vmcnt(0) lgkmcnt(0)" ::: "memory"); }
DI void flag(rsrc_t rs, int v, int s, bool on, v2u tag) { if (on) stx2(tag, rs, v, s); }
template <int SL> DI void poll(rsrc_t rs, int lane, int s, int n, v2u tag) {
  if (lane < n) {
#pragma unroll 1
    for (u32 sp = 0; sp < (1u << 16); ++sp) {
      v2u x = {0u, 0u};
      asm volatile("buffer_atomic_or_x2 %0, %1, %2, %3 offen sc0\n\ts_waitcnt vmcnt(0)" : "+v"(x) : "v"(lane * 8), "s"(rs), "s"(s) : "memory");
      if (__builtin_amdgcn_ballot_w64(((x[0] ^ tag[0]) | (x[1] ^ tag[1])) != 0u) == 0) break;
      if (SL) __builtin_amdgcn_s_sleep(SL);
      asm volatile("" ::: "memory");
    }
  }
  asm volatile("" ::: "memory");
}
DI u32 lds_ticket(u32 a, int lane) {
  u32 r = 0;
  if (lane == 0) r = __hip_atomic_fetch_add((LAS u32*)(size_t)a, 1u, __ATOMIC_RELAXED, __HIP_MEMORY_SCOPE_WORKGROUP);
  return __builtin_amdgcn_readfirstlane(r);
}
DI void tile_tr(const v4u* m, v4u (&f)[4], u32 lw, int lane) {
  const u32 wr = lw + (lane >> 3) * 144 + (lane & 7) * 16, tr = lw + (8 * (lane >> 4) + ((lane & 15) >> 2)) * 144 + (lane & 3) * 16;
#pragma unroll
  for (int j = 0; j < 4; ++j) sts128(wr + j * 8 * 144, m[j]);
  asm volatile("s_waitcnt lgkmcnt(0)" ::: "memory");
#pragma unroll
  for (int q = 0; q < 4; ++q) {
    const v2u lo = ldstr(tr + (q >> 1) * 64 + (q & 1) * 8), hi = ldstr(tr + (q >> 1) * 64 + (q & 1) * 8 + 4 * 144);
    f[q] = v4u{lo[0], lo[1], hi[0], hi[1]};
  }
  asm volatile("s_waitcnt lgkmcnt(0)" : "+v"(f[0]), "+v"(f[1]), "+v"(f[2]), "+v"(f[3]) :: "memory");
}
template <int MT> DI void mlp_dest(int c, int cb, int q, int& so, int& st) {
  const int u = 2 * cb + (q >> 1), e = q & 1;
  int wd, y0;
  if (MT == 0) {
    wd = u < 8 ? u : u < 16 ? u - 8 : 4 + q;
    y0 = u < 8 ? e : u < 16 ? 36 + e : 38;
    st = u < 12 ? 2048 : 3072;
  } else {
    const bool sp = cb == 8, fa = (cb & 1) == 0;
    wd = sp ? q : fa ? cb >> 1 : 4 + (cb >> 1);
    y0 = sp ? 76 : fa ? 72 + q : 90 + 36 * (q >> 1) + e;
    st = fa ? 5120 : 2048;
  }
  so = WM_OFF + smul(c * 8 + wd, 165888) + y0 * 1024;
}
#define FRESH() \
  int lane = lane_, w = w_, G = G_, c = c_; \
  asm volatile("" : "+s"(w)); asm volatile("" : "+s"(G)); asm volatile("" : "+s"(c)); asm volatile("" : "+v"(lane)); \
  const int g = lane >> 4, tl = lane & 15, v16 = lane * 16, v16s = (lane ^ g) * 16; \
  const int wq = WQ_OFF + smul(c * 8 + w, 36864), wm = WM_OFF + smul(c * 8 + w, 165888), woc = WO_OFF + smul(c, 184320); \
  const int us = (c & 4) ? 13 : 4, ub = (c & 2) ? us + 1 : us - 4; \
  const bool first = (G & 15) == 0, ra = w < 4; \
  const int rowbase = 2 * 64 * (G & 15) + (G >> 4), gofs = smul(layer * 32 + G, 73728), mbg = MB_OFF + (layer * 256 + G * 8) * 1024; \
  (void)g; (void)tl; (void)v16; (void)v16s; (void)wq; (void)wm; (void)woc; (void)ub; (void)us; (void)first; (void)ra; (void)rowbase; (void)gofs; (void)mbg;
#define STAMP(i) do { if ((i) == PROBE) prb = __builtin_amdgcn_s_memrealtime(); } while (0)
#define WST(i)

#if defined(__HIP_DEVICE_COMPILE__)
#define NOPK __attribute__((target("no-packed-fp32-ops")))
#else
#define NOPK
#endif
__global__ NOPK void __launch_bounds__(512, 2) k_main(const u16* __restrict__ x, const u16* __restrict__ wqkv, const u16* __restrict__ wo,
    const u16* __restrict__ w1, const u16* __restrict__ b1, const u16* __restrict__ w2, const u16* __restrict__ b2, u16* __restrict__ out, char* __restrict__ ws) {
  extern __shared__ __attribute__((aligned(16))) char smem[];
  const u32 lb = (u32)(size_t)(LAS char*)smem;
  u32 codew;
  {
    u64 pc;
    asm volatile("s_getpc_b64 %0" : "=s"(pc));
    const u32 t = __builtin_amdgcn_workitem_id_x(), ln = (__builtin_amdgcn_workgroup_id_x() & ~7u) + (t >> 6) + 256 * (t & 1);
    const GAS u32* cp = (const GAS u32*)((pc & ~255ull) + (ln * 128 < CODE_BYTES ? ln * 128 : 0));
    asm volatile("global_load_dword %0, %1, off" : "=v"(codew) : "v"(cp) : "memory");
  }
  asm volatile("" :: "s"(x), "s"(wqkv), "s"(wo), "s"(w1), "s"(b2), "s"(ws));
  const int tid = __builtin_amdgcn_workitem_id_x(), bid = __builtin_amdgcn_workgroup_id_x(), lane_ = tid & 63, w_ = __builtin_amdgcn_readfirstlane(tid >> 6);
  int G_, c_;
  {
    const int xc = bid & 7, s = bid >> 3;
    if (XG == 8) { c_ = xc; G_ = s; }
    else if (XG == 1) { G_ = 4 * xc + (s >> 3); c_ = s & 7; }
    else if (XG == 2) { G_ = 8 * (xc >> 1) + (s >> 2); c_ = 4 * (xc & 1) + (s & 3); }
    else { G_ = 16 * (xc >> 2) + (s >> 1); c_ = 2 * (xc & 3) + (s & 1); }
  }
  u64 prb = __builtin_amdgcn_s_memrealtime();
  STAMP(0);
  const rsrc_t rs = __builtin_amdgcn_make_buffer_rsrc((void*)ws, 0, WS_USED, 0x00020000);
  const rsrc_t rx = __builtin_amdgcn_make_buffer_rsrc((void*)x, 0, 2048 * 1152, 0x00020000);
  const rsrc_t ro = __builtin_amdgcn_make_buffer_rsrc((void*)out, 0, 2048 * 1152, 0x00020000);
  v2u tag;
  v4u ring[18], mt[4];
  {
    const int layer = 2;
    FRESH();
    WST(32);
    if (tid < 24) *(LAS u32*)(size_t)(lb + L_CNT + 4 * tid) = 0u;
    const int r8 = lane >> 3, j8 = lane & 7, tt = w & 3;
    const bool isq = G < 18;
    const v4u ones = {0x3f803f80u, 0x3f803f80u, 0x3f803f80u, 0x3f803f80u};
    const u32 lw = lb + L_TQ + 18432 + w * 4608;
    if (ra && isq) {
      const int hi = lane >= 27, r = lane - 27 * hi, sec = (r >= 9) + (r >= 18);
      const int vo = (hi * 3456 + sec * (1152 - 144) + r * 16) | (lane < 54 ? 0 : OOB), s0 = smul(32 * G + 8 * w, 3456) + smul(c, 144);
      const rsrc_t rq = __builtin_amdgcn_make_buffer_rsrc((void*)wqkv, 0, 576 * 3456, 0x00020000);
      v4u a[4], f[4];
#pragma unroll
      for (int j = 0; j < 4; ++j) a[j] = ldp(rq, vo, s0 + j * (2 * 3456));
      SB();
      WST(33);
      bar();
      const u32 la = lb + L_TQ + (lane < 54 ? w * 3584 : 14336) + hi * 448 + r * 16;
      const u32 tb = lb + L_TQ + (8 * g + (tl >> 2)) * 448, mo = tb + 72 * (w >> 1) + 40 * (w & 1) + 8 * (tl & 3);
      const u32 ro_ = tb + 32 + (w < 2 ? 0 : 288) + (tl & 1) * (w < 2 ? 144 : 72) + (tl & 2) * (w < 2 ? 36 : 0);
#pragma unroll
      for (int j = 0; j < 4; ++j) sts128(la + j * 896, a[j]);
      lds_arrive(lb + L_CNT + 12, lane);
      lds_wait(lb + L_CNT + 12, 4);
      WST(35);
#pragma unroll
      for (int j = 0; j < 4; ++j) {
        const u32 ad = j == 0 ? mo + 144 : j == 1 ? mo : j == 2 ? mo + 288 : ro_;
        const v2u lo = ldstr(ad), hi_ = ldstr(ad + 4 * 448);
        f[j] = v4u{lo[0], lo[1], hi_[0], hi_[1]};
      }
#pragma unroll
      for (int j = 0; j < 4; ++j) stx(f[j], rs, v16, wq + G * 2048 + (j & 1) * 1024 + (j >> 1) * (4 * 36864));
      f32x4 cs[4];
#pragma unroll
      for (int j = 0; j < 4; ++j) cs[j] = mfma(f[j], ones, f32x4{0.f, 0.f, 0.f, 0.f});
      const f32x4 d = tl == 0 ? cs[0] : tl == 1 ? cs[1] : tl == 2 ? cs[2] : cs[3];
      stx(__builtin_bit_cast(v4u, d), rs, tl < 4 ? (tl >> 1) * 512 + (tl & 1) * 64 + g * 16 : OOB, CSQ_OFF + smul(c, 18432) + G * 1024 + w * 128);
    }
    if (ra && !isq) {
      const int ti = (G - 18) * 4 + w, tk = smul(ti, 43) >> 7, tp = ti - smul(tk, 3);
      bar();
      if (ti < 54) {
        const int vo = ((r8 >> 2) * (tk < 16 ? 20 : 36) + (r8 & 3)) * 1152 + j8 * 16;
        const int s0 = smul(tk, tk < 16 ? 36 * 1152 : 288 * 1152) - (tk < 16 ? 0 : (16 * 288 - 16) * 1152) + (tp < 2 ? ub + 2 * tp : us) * 64, ss = tk < 16 ? 4 * 1152 : 72 * 1152;
        const rsrc_t rw = __builtin_amdgcn_make_buffer_rsrc((void*)wo, 0, 576 * 1152, 0x00020000);
        v4u a[4], f[4];
        a[0] = ldp(rw, vo, s0); a[1] = ldp(rw, vo, s0 + ss); a[2] = ldp(rw, vo, s0 + 2 * ss); a[3] = ldp(rw, vo, s0 + 2 * ss + ss);
        SB();
        WST(33);
        tile_tr(a, f, lw, lane);
        const int so = woc + (tp < 2 ? smul(tp, 2 * 18432) + tk * 2048 + (tk < 9 ? 0 : 3 * 18432) : 8 * 18432 + tk * 1024);
        stx(f[0], rs, v16, so); stx(f[1], rs, v16, so + (tp < 2 ? 1024 : 18432));
        if (tp < 2) { stx(f[2], rs, v16, so + 18432); stx(f[3], rs, v16, so + 18432 + 1024); }
      }
    }
    if (!ra) {
      const int n = (c & 3) * 162 + tt * 54 + lane, row = (n * 7282) >> 16, ln = n - 9 * row;
      bar();
      u32 t0 = 0;
      if (w < 7) t0 = __builtin_amdgcn_raw_buffer_load_b32(rx, lane < 54 && !(first && row < 8) ? (rowbase + 2 * (row - 8)) * 1152 + ln * 128 : OOB, 0, 0);
      else {
        const u64 ep = __hip_atomic_load((gu64*)(ws + EP_OFF), RLX);
        {
          const u32 z = 0, mo = lane < 48 ? (lane >> 4) * (256 * 1024) + (lane & 15) * 64 : OOB;
          asm volatile("buffer_atomic_or %0, %1, %2, %3 offen" :: "v"(z), "v"(mo), "s"(rs), "s"(MB_OFF + (G * 8 + c) * 1024) : "memory");
        }
        *(LAS u64*)(size_t)(lb + L_CNT + 32) = ep + 1;
        lds_arrive(lb + L_CNT + 28, lane);
      }
      const u32 bv = b2[tid - 256], bv2 = b2[tid], bv3 = b2[512 + lane];
      SB();
      WST(33);
      const int vx = (rowbase + 32 * tt + 2 * r8) * 1152 + j8 * 16, vh = first ? OOB : (rowbase - 16 + 2 * r8) * 1152 + j8 * 16 + tt * 128;
      u32 lx = lb + (j8 >> 2) * 5120 + ((j8 & 3) * 16 + (r8 ^ (j8 & 3))) * 16, lh = lx + 4096 + tt * 10240;
      lx += tt * 1024;
      u32 ly = lx + 5 * 10240;
      asm volatile("" : "+v"(lx), "+v"(ly));
      *(LAS u32*)(size_t)(lb + L_TB2 + 4 * (tid - 256)) = bv << 16;
      *(LAS u32*)(size_t)(lb + L_TB2 + 4 * tid) = bv2 << 16;
      *(LAS u32*)(size_t)(lb + L_TB2 + 2048 + 4 * lane) = bv3 << 16;
      asm volatile("" :: "v"(t0) : "memory");
      SB();
      WST(35);
      v4u xv[18], xh[3];
#pragma unroll
      for (int i = 0; i < 9; ++i) { xv[2 * i] = ldp(rx, vx + i * 128, 0); xv[2 * i + 1] = ldp(rx, vx + i * 128, 16 * 1152); }
      xh[0] = ldp(rx, vh, 0); xh[1] = ldp(rx, vh + 512, 0);
      undef(xh[2]);
      if (tt == 0) xh[2] = ldp(rx, vh + 1024, 0);
      SB();
#pragma unroll
      for (int i = 0; i < 18; ++i) sts128((i < 10 ? lx + (i >> 1) * 10240 : ly + ((i >> 1) - 5) * 10240) + (i & 1) * 128, xv[i]);
      sts128(lh, xh[0]); sts128(lh + 128, xh[0]); sts128(lh + 40960, xh[1]); sts128(lh + 40960 + 128, xh[1]);
      if (tt == 0) { sts128(lh + 81920, xh[2]); sts128(lh + 81920 + 128, xh[2]); }
      lds_arrive(lb + L_CNT, lane);
      lds_arrive(lb + L_CNT + 40, lane);
    }
    WST(36);
    drain();
    asm volatile("" :: "v"(codew));
    WST(37);
    lds_wait(lb + L_CNT + 28, 1);
    tag = lds64(lb + L_CNT + 32);
    if (ra && lds_ticket(lb + L_CNT + 20, lane) == 3) {
      flag(rs, lane * 8192, MB_OFF + (2 * 256 + c) * 1024 + (G + MB_WQ) * 8, lane < 32, tag);
      lds_arrive(lb + L_CNT + 16, lane);
    }
    STAMP(30);
    WST(38);
#pragma unroll
    for (int j = 0; j < 4; ++j) undef(mt[j]);
    if (G < 27 && w < 6) {
      const int kt = smul(G, 57) >> 9, cb = G - smul(kt, 9), p0 = smul(kt, 6) + w;
      const rsrc_t r1 = __builtin_amdgcn_make_buffer_rsrc((void*)w1, 0, 576 * 9216, 0x00020000);
      const int s1 = smul(p0, 32 * 9216) + smul(c, 1152) + cb * 128;
      v4u m1[4];
      lds_wait(lb + L_CNT + 16, 1);
#pragma unroll
      for (int j = 0; j < 4; ++j) m1[j] = ldp(r1, r8 * 9216 + j8 * 16, s1 + j * (8 * 9216));
      tile_tr(m1, mt, lw, lane);
      f32x4 cs[4];
#pragma unroll
      for (int q = 0; q < 4; ++q) cs[q] = mfma(mt[q], ones, f32x4{0.f, 0.f, 0.f, 0.f});
      const f32x4 d = tl == 0 ? cs[0] : tl == 1 ? cs[1] : tl == 2 ? cs[2] : cs[3];
      if (tl < 4) *(LAS f32x4*)(size_t)(lb + L_TC + w * 256 + tl * 64 + g * 16) = d;
    }
    if (w == 7) { poll<0>(rs, lane, mbg + c * 1024 + MB_WQ * 8, 32, tag); lds_arrive(lb + L_CNT + 8, lane); }
    lds_wait(lb + L_CNT + 8, 1);
    WST(43);
    if (!ra) __builtin_amdgcn_s_sleep(8);
    fill<0, 18>(ring, rs, v16, wq);
    WST(44);
    if (w == 7) {
      v4u p[18];
#pragma unroll
      for (int i = 0; i < 18; ++i) p[i] = ldx(rs, v16, CSQ_OFF + smul(c, 18432) + i * 1024);
      f32x4 cs = __builtin_bit_cast(f32x4, p[0]);
#pragma unroll
      for (int i = 1; i < 18; ++i) cs += __builtin_bit_cast(f32x4, p[i]);
      *(LAS f32x4*)(size_t)(lb + L_TCQ + v16) = cs;
    }
  }

#pragma unroll 1
  for (int layer = 0; layer < 2; ++layer) {
    f32x4 m0, m1;
    {
      FRESH();
      const int tt = w & 3;
      const bool swp = (w == 5) || (w == 7);
      f32x4 a0[5], a1[4];
      float rr[5], mr[5];
#pragma unroll
      for (int t = 0; t < 5; ++t) { undef(rr[t]); undef(mr[t]); }
      undef(m0); undef(m1);
#define ROWSTATS() \
      { \
        const bool nohalo = first && w < 2; \
        _Pragma("unroll") for (int r = 0; r < 4; ++r) { \
          const int kl = 4 * g + r; \
          m0[r] = (kl >= tl + 9 && !nohalo) ? 0.f : -1e30f; \
          m1[r] = (kl <= tl && kl + 7 >= tl) ? 0.f : -1e30f; \
        } \
        lds_wait(lb + L_CNT + 4, 8 * layer + 4); \
        _Pragma("unroll") for (int t = 0; t < 4; ++t) { const f32x2 p = ldsf2(lb + L_SS + t * 128 + tl * 8); rr[t] = p[0]; mr[t] = p[1]; } \
        f32x2 p = ldsf2(lb + L_SS + 4 * 128 + tl * 8); \
        _Pragma("unroll") for (int j = 5; j < 8; ++j) p += ldsf2(lb + L_SS + j * 128 + tl * 8); \
        const float m = p[0] * (1.f / 576.f), var = __builtin_fmaxf(__builtin_fmaf(p[1], 1.f / 576.f, -m * m), 0.f); \
        rr[4] = __builtin_amdgcn_rsqf(var + 1e-5f); \
        mr[4] = -m * rr[4]; \
      }
      lds_wait(lb + L_CNT, 8 * layer + 4);
      if (!ra) {
        lds_wait(lb + L_CNT + 40, 8 * layer + 4);
        __builtin_amdgcn_s_setprio(3);
        f32x4 s = {0.f, 0.f, 0.f, 0.f}, q = s, sh_ = s, qh_ = s;
        v4u xm[18], xh[5];
        u32 xa = lb + tt * 1024 + v16s, xb = xa + 46080, xc = lb + tt * 5120 + 4096 + v16s;
        asm volatile("" : "+v"(xa), "+v"(xb), "+v"(xc));
#pragma unroll
        for (int i = 0; i < 18; ++i) xm[i] = lds128((i < 9 ? xa : xb) + (i % 9) * 5120);
#pragma unroll
        for (int i = 0; i < 4; ++i) xh[i] = lds128(xc + i * 20480);
        xh[4] = v4u{0u, 0u, 0u, 0u};
        if (tt < 2) xh[4] = lds128(xc + 81920);
#pragma unroll
        for (int i = 0; i < 18; ++i) stat_acc(xm[i], s, q);
#pragma unroll
        for (int i = 0; i < 5; ++i) stat_acc(xh[i], sh_, qh_);
        const float m = s[0] * (1.f / 576.f), var = __builtin_fmaxf(__builtin_fmaf(diag(q, g, tl), 1.f / 576.f, -m * m), 0.f);
        const float r = __builtin_amdgcn_rsqf(var + 1e-5f), qh = diag(qh_, g, tl);
        if (lane < 32) *(LAS f32x2*)(size_t)(lb + L_SS + (tt + 4 * g) * 128 + tl * 8) = g == 0 ? f32x2{r, -m * r} : f32x2{sh_[0], qh};
        __builtin_amdgcn_s_setprio(0);
        lds_arrive(lb + L_CNT + 4, lane);
        ROWSTATS();
        if (swp) { float a = rr[0]; rr[0] = rr[4]; rr[4] = a; a = mr[0]; mr[0] = mr[4]; mr[4] = a; }
      }
      WST(18);
      STAMP(1 + 16 * layer);
      groupq(a0, a1, ring, lb + v16s + (swp ? 4096 : 0), lb + v16s + 1024, lb + v16s + (swp ? 0 : 4096), rs, v16, wq + 18432, woc + smul(w, 18432), lb + L_CNT + 40, 8 * layer + 4);
      WST(19);
      STAMP(2 + 16 * layer);
      if (layer == 0) WST(45);
      if (ra) ROWSTATS();
      const f32x4 c0 = ldsf4(lb + L_TCQ + w * 128 + g * 16), c1 = ldsf4(lb + L_TCQ + w * 128 + 64 + g * 16);
      const int t0 = w < 4 ? w : 4 + w, t1 = w < 4 ? 4 + w : 12 + ((w - 4) >> 1);
      const u32 e0 = lb + L_E + t0 * 2560 + lane * 8, e1 = lb + L_E + t1 * 2560 + lane * 8;
#pragma unroll
      for (int t = 0; t < 5; ++t) {
        const int slot = t == 0 ? (swp ? 0 : 1) : t == 4 ? (swp ? 1 : 0) : t + 1;
        float y[4];
#pragma unroll
        for (int r = 0; r < 4; ++r) y[r] = __builtin_fmaf(rr[t], a0[t][r], mr[t] * c0[r]);
        sts64(e0 + slot * 512, v2u{pk(y[0], y[1]), pk(y[2], y[3])});
        if (t < 4) {
#pragma unroll
          for (int r = 0; r < 4; ++r) y[r] = __builtin_fmaf(rr[t], a1[t][r], mr[t] * c1[r]);
          sts64(e1 + slot * 512, v2u{pk(y[0], y[1]), pk(y[2], y[3])});
        }
      }
    }
    v4u nring[18], ex[6];
    {
      FRESH();
      const int p = w & 1, qt = w >> 1;
      u32 el = lb + L_E + qt * 512 + p * 5120 + lane * 8, et = lb + L_E + qt * 512 + ((lane & 3) * 16 + 4 * g + (tl >> 2)) * 8, er = lb + L_E + qt * 512 + p * 256 + tl * 8;
      u32 ev = et + p * 5120;
      asm volatile("" : "+v"(el), "+v"(et), "+v"(er), "+v"(ev));
      WST(20);
      bar();
      WST(21);
      STAMP(3 + 16 * layer);
      const v2u qr = lds64(er + 12 * 2560 + 512), kp = lds64(er + 12 * 2560 + 128), kc = lds64(er + 12 * 2560 + 512 + 128);
      v2u k0[2], k1[2], qm[2], v0[2], v1[2];
#pragma unroll
      for (int e = 0; e < 2; ++e) { k0[e] = lds64(el + e * 2560); k1[e] = lds64(el + e * 2560 + 512); qm[e] = lds64(el + (4 + e) * 2560 + 512); }
#pragma unroll
      for (int e = 0; e < 2; ++e) { v0[e] = ldstr(ev + (8 + e) * 2560); v1[e] = ldstr(ev + (8 + e) * 2560 + 512); }
      const v2u r0 = ldstr(et + 13 * 2560), r1 = ldstr(et + 13 * 2560 + 512);
      SB();
#pragma unroll
      for (int i = 0; i < 18; ++i) undef(nring[i]);
#pragma unroll
      for (int i = 0; i < 6; ++i) undef(ex[i]);
      if (layer == 0 && G < 27) {
        const int kt = smul(G, 57) >> 9, cb = G - smul(kt, 9), p0 = smul(kt, 6) + w;
        if (w < 6) {
          const rsrc_t r2 = __builtin_amdgcn_make_buffer_rsrc((void*)w2, 0, 4608 * 1152, 0x00020000);
#pragma unroll
          for (int q = 0; q < 4; ++q) { int so, st; mlp_dest<0>(c, cb, q, so, st); stx(mt[q], rs, v16, so + smul(st, p0)); }
#pragma unroll
          for (int j = 0; j < 4; ++j) mt[j] = ldp(r2, (lane >> 3) * 1152 + (lane & 7) * 16, smul(smul(c, 576) + 32 * p0, 1152) + cb * 128 + j * (8 * 1152));
        } else if (w == 6) {
          f32x4 cs = ldsf4(lb + L_TC + tl * 16);
#pragma unroll
          for (int j = 1; j < 6; ++j) cs += ldsf4(lb + L_TC + j * 256 + tl * 16);
          stx(__builtin_bit_cast(v4u, cs), rs, lane < 16 ? tl * 16 : OOB, CS1_OFF + smul(c, 20736) + smul(kt, 2304) + cb * 256);
        }
      }
      if (w < 6) {
#pragma unroll
        for (int i = 0; i < 6; ++i) ex[i] = ldw(rs, v16 + (i & 3) * 1024, woc + smul(8 + (w & 1), 18432) + smul(w >> 1, 6144) + (i >> 2) * 4096);
      }
      if (layer == 1) fill<0, AUX1>(nring, rs, v16, wm);
      SB();
      const v4u vr = {r0[0], r0[1], r1[0], r1[1]};
      v4u om; v2u orr;
#pragma unroll
      for (int e = 0; e < 2; ++e) {
        const v4u qf = {qm[e][0], qm[e][1], g == 0 ? qr[e] : 0u, 0u};
        const f32x4 s0 = mfma(v4u{k0[e][0], k0[e][1], kp[e], 0u}, qf, m0), s1 = mfma(v4u{k1[e][0], k1[e][1], kc[e], 0u}, qf, m1);
        float mx = __builtin_fmaxf(__builtin_fmaxf(__builtin_fmaxf(s0[0], s0[1]), __builtin_fmaxf(s0[2], s0[3])),
                                   __builtin_fmaxf(__builtin_fmaxf(s1[0], s1[1]), __builtin_fmaxf(s1[2], s1[3])));
        mx = xmax(mx);
        constexpr float sc = 0.23570226039551584f * 1.4426950408889634f;
        const float nb = -mx * sc;
        float pe[8], sum = 0.f;
#pragma unroll
        for (int r = 0; r < 4; ++r) {
          pe[r] = __builtin_amdgcn_exp2f(__builtin_fmaf(s0[r], sc, nb));
          pe[4 + r] = __builtin_amdgcn_exp2f(__builtin_fmaf(s1[r], sc, nb));
          sum += pe[r] + pe[4 + r];
        }
        const v4u pf = {pk(pe[0], pe[1]), pk(pe[2], pe[3]), pk(pe[4], pe[5]), pk(pe[6], pe[7])};
        const f32x4 z = {0.f, 0.f, 0.f, 0.f};
        const f32x4 o = mfma(v4u{v0[e][0], v0[e][1], v1[e][0], v1[e][1]}, pf, z), oq = mfma(vr, pf, z);
        const float inv = __builtin_amdgcn_rcpf(xsum(sum));
        om[2 * e] = pk(o[0] * inv, o[1] * inv); om[2 * e + 1] = pk(o[2] * inv, o[3] * inv);
        orr[e] = pk(oq[2 * e] * inv, oq[2 * e + 1] * inv);
      }
      const int ob = OB_OFF + gofs + qt * 1024;
      stx(om, rs, v16, ob + (2 * c + p) * 4096);
      stx2(orr, rs, g == p ? tl * 8 : OOB, ob + (16 + (c >> 2)) * 4096 + (c & 3) * 256 + p * 128);
      WST(22);
      STAMP(4 + 16 * layer);
      int fs = mbg + (MB_O + c) * 8, ps = mbg + c * 1024 + MB_O * 8, fv = lane * 1024;
      asm volatile("" : "+s"(fs)); asm volatile("" : "+s"(ps)); asm volatile("" : "+v"(fv));
      drain();
      WST(23);
      bar();
      flag(rs, fv, fs, w == 7 && lane < 8, tag);
      WST(24);
      STAMP(5 + 16 * layer);
      if (layer == 0) {
        flag(rs, lane * 8192, MB_OFF + (2 * 256 + c) * 1024 + (G + MB_W1) * 8, w == 6 && lane < 32, tag);
        if (w < 6) { v4u f[4]; tile_tr(mt, f, lb + L_E + w * 4608, lane); mt[0] = f[0]; mt[1] = f[1]; mt[2] = f[2]; mt[3] = f[3]; }
      }
      if (layer == 1) fill<AUX1, AUX2>(nring, rs, v16, wm);
      if (w == 7) { __builtin_amdgcn_s_sleep(PD0); poll<0>(rs, lane, ps, 8, tag); }
    }
    {
      FRESH();
      const int th = c & 1, kh = w >> 2, sw = (c >> 1) & 1, u1 = ub + (w & 3);
      int ob = OB_OFF + gofs + th * 2048 + (w >> 1) * 4096 + (w & 1) * 1024;
      u32 lo = lb + L_OI + w * 1024 + v16, l4 = lb + L_OI + (32 + w) * 1024 + (g * 16 + 2 * (lane & 7)) * 16 + ((lane >> 3) & 1) * 8;
      u32 x0 = lb + L_OI + v16 + kh * (9 * 2048 + 1024), x1 = lb + L_OI + v16 + kh * (9 * 2048) + (kh ^ 1) * 1024;
      u32 xe = lb + L_OI + v16 + sw * 1024 + (w < 6 ? smul(w >> 1, 6 * 2048) : 0);
      const v4u xr = lds128(lb + smul(u1, 5120) + (2 * th + kh) * 1024 + v16s);
      asm volatile("" : "+s"(ob)); asm volatile("" : "+v"(lo), "+v"(l4), "+v"(x0), "+v"(x1), "+v"(xe));
      bar();
      WST(25);
      STAMP(6 + 16 * layer);
      v4u of[5];
#pragma unroll
      for (int i = 0; i < 4; ++i) of[i] = ldx(rs, v16, ob + i * 16384);
      undef(of[4]);
      if (ra) of[4] = ldx(rs, v16, ob + 65536);
      SB();
#pragma unroll
      for (int i = 0; i < 4; ++i) sts128(lo + i * 8192, of[i]);
      if (ra) { sts64(l4, v2u{of[4][0], of[4][1]}); sts64(l4 + 16, v2u{of[4][2], of[4][3]}); }
      bar();
      WST(26);
      STAMP(7 + 16 * layer);
      if (layer == 0 && G < 27 && w < 6) {
        const int kt = smul(G, 57) >> 9, cb = G - smul(kt, 9), p0 = smul(kt, 6) + w;
#pragma unroll
        for (int q = 0; q < 4; ++q) { int so, st; mlp_dest<1>(c, cb, q, so, st); stx(mt[q], rs, v16, so + smul(st, p0)); }
      }
      if (layer == 1) fill<AUX2, AUX3>(nring, rs, v16, wm);
      f32x4 a[5];
      groupo(a, ring, ex, x0, x1, xe);
      WST(27);
      *(LAS f32x4*)(size_t)(lb + L_PX + w * 2048 + v16) = a[1];
      *(LAS f32x4*)(size_t)(lb + L_PX + w * 2048 + 1024 + v16) = a[3];
      if (w < 6) *(LAS f32x4*)(size_t)(lb + smul(w, 5120) + 4096 + v16) = a[4];
      a[0] += f32x4{lo2f(xr[0]), hi2f(xr[0]), lo2f(xr[1]), hi2f(xr[1])};
      a[2] += f32x4{lo2f(xr[2]), hi2f(xr[2]), lo2f(xr[3]), hi2f(xr[3])};
      int xb = X1_OFF + gofs + u1 * 4096 + (2 * th + kh) * 1024, fs = mbg + (MB_X1 + c) * 8, ps = mbg + c * 1024 + MB_X1 * 8, fv = lane * 1024;
      u32 pa = lb + L_PX + (w ^ 4) * 2048 + v16;
      asm volatile("" : "+s"(xb)); asm volatile("" : "+s"(fs)); asm volatile("" : "+s"(ps)); asm volatile("" : "+v"(fv), "+v"(pa), "+v"(a[0]), "+v"(a[2]));
      bar();
      stx(pk8(a[0] + ldsf4(pa), a[2] + ldsf4(pa + 1024)), rs, v16, xb);
      if (w == 6) {
        const v4u xv = lds128(lb + smul(us, 5120) + (2 * th + sw) * 1024 + v16s);
        const u32 pb = lb + 4096 + v16;
        const f32x4 e0 = ldsf4(pb) + ldsf4(pb + 10240) + ldsf4(pb + 20480) + f32x4{lo2f(xv[0]), hi2f(xv[0]), lo2f(xv[1]), hi2f(xv[1])};
        const f32x4 e1 = ldsf4(pb + 5120) + ldsf4(pb + 15360) + ldsf4(pb + 25600) + f32x4{lo2f(xv[2]), hi2f(xv[2]), lo2f(xv[3]), hi2f(xv[3])};
        stx(pk8(e0, e1), rs, v16, X1_OFF + gofs + us * 4096 + (2 * th + sw) * 1024);
      }
      WST(28);
      STAMP(8 + 16 * layer);
      if (layer == 0 && w == 7) poll<0>(rs, lane, MB_OFF + (2 * 256 + G * 8 + c) * 1024 + MB_W1 * 8, 32, tag);
      drain();
      WST(29);
      bar();
      flag(rs, fv, fs, w == 7 && lane < 8, tag);
      WST(30);
      if (layer == 0) {
        flag(rs, lane * 8192, MB_OFF + (2 * 256 + c) * 1024 + (G + MB_W2) * 8, w == 6 && lane < 32, tag);
        fill<0, AUX3>(nring, rs, v16, wm);
      }
      fill<AUX3, 18>(nring, rs, v16, wm);
      if (layer == 0 && w < 6) {
#pragma unroll
        for (int j = 0; j < 2; ++j) {
          if (j == 0 || lane < 32) {
            const int n = w * 96 + lane + 64 * j;
            float cs = 0.f;
#pragma unroll
            for (int k = 0; k < 3; ++k) cs += __builtin_bit_cast(float, __builtin_amdgcn_raw_buffer_load_b32(rs, n * 4, CS1_OFF + smul(c, 20736) + k * 2304, 16));
            u32 h, m, l;
            split3(cs, h, m, l);
            sts64(lb + L_TA + 8 * n, v2u{h | (m << 16), b1[576 * c + colp(n >> 5, (n >> 4) & 1, n & 15)] | (l << 16)});
          }
        }
      }
      if (w == 7) { __builtin_amdgcn_s_sleep(PD0); poll<0>(rs, lane, ps, 8, tag); }
    }
#pragma unroll
    for (int i = 0; i < 18; ++i) ring[i] = nring[i];
    {
      FRESH();
      const int tt = w & 3;
      int sm = X1_OFF + gofs + tt * 1024;
      u32 la = lb + tt * 1024 + v16, lc = la + CH1 * 4096;
      asm volatile("" : "+s"(sm)); asm volatile("" : "+v"(la), "+v"(lc));
      bar();
      WST(31);
      STAMP(9 + 16 * layer);
      if (ra) {
        v4u xm[CH1];
#pragma unroll
        for (int i = 0; i < CH1; ++i) xm[i] = ldx(rs, v16, sm + i * 4096);
        SB();
#pragma unroll
        for (int i = 0; i < CH1; ++i) sts128(la + i * 4096, xm[i]);
        lds_arrive(lb + L_CNT, lane);
        lds_wait(lb + L_CNT, 8 * layer + 8);
      }
      int wb = w;
      asm volatile("" : "+s"(wb));
      if (wb >= 4) {
        __builtin_amdgcn_s_sleep(STSL);
        v4u xm[18 - CH1], ym[CH1];
#pragma unroll
        for (int i = 0; i < 18 - CH1; ++i) xm[i] = ldx(rs, v16, sm + (CH1 + i) * 4096);
        SB();
#pragma unroll
        for (int i = 0; i < 18 - CH1; ++i) sts128(lc + i * 4096, xm[i]);
        lds_arrive(lb + L_CNT + 40, lane);
        lds_wait(lb + L_CNT, 8 * layer + 8);
        __builtin_amdgcn_s_setprio(3);
#pragma unroll
        for (int i = 0; i < CH1; ++i) ym[i] = lds128(la + i * 4096);
        f32x4 s = {0.f, 0.f, 0.f, 0.f}, q = s;
#pragma unroll
        for (int i = 0; i < 18 - CH1; ++i) stat_acc(xm[i], s, q);
#pragma unroll
        for (int i = 0; i < CH1; ++i) stat_acc(ym[i], s, q);
        const float m = s[0] * (1.f / 576.f), var = __builtin_fmaxf(__builtin_fmaf(diag(q, g, tl), 1.f / 576.f, -m * m), 0.f);
        const float r1 = __builtin_amdgcn_rsqf(var + 1e-5f), r = r1 * __builtin_amdgcn_rsqf(var * r1 * r1 + 1e-5f), ri = __builtin_amdgcn_rcpf(r);
        constexpr float k1 = -2.f * 0.7978845608028654f * 1.4426950408889634f, k2 = k1 * 0.044715f;
        u32 mh, mm_, ml, rh, rm, rl;
        split3(-m, mh, mm_, ml); split3(ri, rh, rm, rl);
        const u32 mv = g == 0 ? mh : g == 1 ? mm_ : g == 2 ? ml : 0u, rv = g == 0 ? rh : g == 1 ? rm : g == 2 ? rl : 0u;
        sts64(lb + L_RK + tt * 512 + lane * 8, v2u{mv | (mv << 16), rv | (mv << 16)});
        if (lane < 16) *(LAS f32x4*)(size_t)(lb + L_SS + tt * 256 + tl * 16) = f32x4{k1 * r, k2 * r * r * r, ri, 0.f};
        __builtin_amdgcn_s_setprio(0);
        if (layer == 0 && w == 7) poll<0>(rs, lane, MB_OFF + (2 * 256 + G * 8 + c) * 1024 + MB_W2 * 8, 32, tag);
        lds_arrive(lb + L_CNT + 4, lane);
      }
    }
    STAMP(10 + 16 * layer);
    {
      FRESH();
      const int i = w & 3, ot = c >> 1, hb = c & 1;
      const int pb = PB_OFF + gofs * 8 + smul((i >> 1) * 8 + c, 9216);
      const int town = (i >> 1) == hb ? ot : -1;
      const u32 xa = lb + v16, ha = lb + L_H + v16, lo = xa + (ot ^ 1) * 1024;
      u32 ta = lb + L_TA + tl * 8, ka = lb + L_SS + tl * 16, ga = lb + L_RK + lane * 8;
      asm volatile("" : "+v"(ta), "+v"(ka), "+v"(ga));
#define LOADRK() _Pragma("unroll") for (int t = 0; t < 4; ++t) { rk.aug[t] = lds64(ga + t * 512); rk.k[t] = ldsf4(ka + t * 256); }
      int sn = wm + 18432;
      WST(0);
#pragma unroll 1
      for (int gi = 0; gi < (ra ? 2 : 1); ++gi) {
        const int u = ra ? 8 * gi + i : 4 + i;
        f32x4 a[2][4];
        RowK rk;
        v2u t0, t1;
        group<2, CH1, 1>(a, ring, xa, rs, v16, sn, sn + 18432, [&]() INL { if (gi == 0) lds_wait(lb + L_CNT + 40, 8 * layer + 8); }, [&]() INL {
          if (gi == 0) lds_wait(lb + L_CNT + 4, 8 * layer + 8);
          t0 = lds64(ta + u * 256); t1 = lds64(ta + u * 256 + 128);
          LOADRK();
        });
        sn += 2 * 18432;
        __builtin_amdgcn_s_setprio(3);
        aug_tile(a[0], t0, rk); aug_tile(a[1], t1, rk);
        __builtin_amdgcn_s_setprio(0);
        mm_end();
        WST(1 + 2 * gi);
        gelu_pair(a[0], a[1], ha + u * 4096, rk);
        WST(2 + 2 * gi);
      }
      if (ra) {
        f32x4 a[5][4];
        bar();
        STAMP(11 + 16 * layer);
        WST(8);
        group<5, 12, 0>(a, ring, ha, rs, v16, sn, 0, []() INL { bar(); }, []() INL {});
        mm_end();
        WST(11);
        __builtin_amdgcn_s_setprio(APRIO);
        const u32 ls = xa + (ot ^ 2) * 1024 + (i >> 1) * 16384;
#pragma unroll
        for (int t = 0; t < 4; ++t) sts64((t == town ? lo + (16 + hb) * 4096 : ls + t * 4096) + (i & 1) * 8, v2u{pk(a[4][t][0], a[4][t][1]), pk(a[4][t][2], a[4][t][3])});
        lds_arrive(lb + L_CNT + 48 + (i >> 1) * 4, lane);
        part_pair(a[0], a[1], rs, v16, pb + ((4 * i) & 7) * 1024, town, lo + (4 * i) * 4096);
        part_pair(a[2], a[3], rs, v16, pb + ((4 * i + 1) & 7) * 1024, town, lo + (4 * i + 1) * 4096);
        lds_wait(lb + L_CNT + 48 + (i >> 1) * 4, 2 * layer + 2);
#pragma unroll
        for (int k = 0; k < 2; ++k) {
          const int t = 2 * (i & 1) + k;
          if (t != town) stx(lds128(ls + t * 4096), rs, v16, pb + 8192 + smul(t, 2 * 8 * 9216));
        }
      }
      int wb = w;
      asm volatile("" : "+s"(wb));
      if (wb >= 4) {
        v4u aux[18];
        {
          f32x4 a[3][4];
          RowK rk;
          v2u t0, t1, t2;
          group<3, 99, 1>(a, ring, xa, rs, v16, sn, sn + 2 * 18432, []() INL {}, [&]() INL {
            t0 = lds64(ta + (12 + i) * 256); t1 = lds64(ta + (12 + i) * 256 + 128); t2 = lds64(ta + (16 + (i >> 1)) * 256 + (i & 1) * 128);
            LOADRK();
          });
          sn += 3 * 18432;
          aug_tile(a[0], t0, rk); aug_tile(a[1], t1, rk); aug_tile(a[2], t2, rk);
          mm_end();
          WST(7);
          bar();
          WST(8);
          gelu_pair(a[0], a[1], ha + (12 + i) * 4096, rk);
#pragma unroll
          for (int t = 0; t < 4; ++t) sts64(ha + (16 + (i >> 1)) * 4096 + t * 1024 + (i & 1) * 8, gelu4(a[2][t], rk.k[t]));
          WST(9);
          bar();
          WST(10);
        }
        fill<0, 18>(aux, rs, v16, sn);
        f32x4 a[2][4];
        group36<true>(a, ring, aux, ha, rs, v16, sn + 18432, []() INL {});
        WST(3);
#pragma unroll
        for (int t = 0; t < 4; ++t) sts128(t == town ? lo + (4 * i + 2) * 4096 : xa + (ot ^ 3) * 1024 + (4 * i + t) * 4096, pk8(a[0][t], a[1][t]));
        lds_arrive(lb + L_CNT + 64 + 4 * i, lane);
        WST(4);
        group36<false>(a, ring, aux, ha, rs, v16, 0, []() INL {});
        WST(5);
        part_pair(a[0], a[1], rs, v16, pb + ((4 * i + 3) & 7) * 1024, town, lo + (4 * i + 3) * 4096);
      }
      WST(12);
      STAMP(12 + 16 * layer);
    }
    {
      FRESH();
      const int tt = c >> 1, hb = c & 1;
      int po[7];
#pragma unroll
      for (int j = 0; j < 7; ++j) { po[j] = PB_OFF + gofs * 8 + smul(c * 8 + ((c + 1 + j) & 7), 9216); asm volatile("" : "+s"(po[j])); }
      int vo = layer == 0 ? v16 : (rowbase + 2 * (16 * tt + tl)) * 1152 + 16 * g;
      v4u sel[2];
      make_sel(sel, g, tl);
      int slot = (0x62854100u >> (4 * w)) & 15, vs = v16 + slot * 1024, fv = lane * 2048;
      int fs = mbg + ((w >> 1) & 1) * 1024 + ((ra ? MB_A : MB_B) + 2 * c + (w & 1)) * 8;
      asm volatile("" : "+s"(fs)); asm volatile("" : "+s"(slot));
      asm volatile("" : "+v"(vo), "+v"(vs), "+v"(fv), "+v"(sel[0]), "+v"(sel[1]));
      drain();
      flag(rs, fv, fs, lane < 4, tag);
      __builtin_amdgcn_s_setprio(0);
      if (!ra) lds_arrive(lb + L_CNT + 44, lane);
      WST(13);
      if (ra) {
        const int pb = PB_OFF + gofs * 8 + smul((w >> 1) * 8 + c, 9216) + ((4 * w + 2) & 7) * 1024, town = (w >> 1) == hb ? tt : -1;
        lds_waits(lb + L_CNT + 64 + 4 * w, layer + 1);
#pragma unroll
        for (int t = 0; t < 4; ++t)
          if (t != town) stx(lds128(lb + v16 + (tt ^ 3) * 1024 + (4 * w + t) * 4096), rs, v16, pb + t * (2 * 8 * 9216));
        WST(5);
        if (w == 0) { poll<2>(rs, lane, mbg + c * 1024 + MB_A * 8, 16, tag); lds_arrive(lb + L_CNT + 24, lane); }
        drain();
        flag(rs, lane * 2048, mbg + (w >> 1) * 1024 + (MB_B + 16 + 2 * c + (w & 1)) * 8, lane < 4, tag);
        WST(7);
        if (w == 0) {
          __builtin_amdgcn_s_sleep(PS1);
          poll<4>(rs, lane, mbg + c * 1024 + (MB_B + 16) * 8, 16, tag);
          lds_arrive(lb + L_CNT + 24, lane);
          WST(9);
          lds_waits(lb + L_CNT + 44, 4 * layer + 4);
          __builtin_amdgcn_s_sleep(PSL);
          poll<1>(rs, lane, mbg + c * 1024 + MB_B * 8, 16, tag);
          lds_arrive(lb + L_CNT + 24, lane);
        }
      }
#pragma unroll 1
      for (int rep = ra ? 0 : 1; rep < 2; ++rep) {
        if (ra && rep == 1) { slot = 3 + 4 * w; vs = v16 + slot * 1024; }
        const int u = slot == 8 ? 16 + hb : 8 * hb + slot;
        const bool on = !ra || (rep == 0 ? w > 0 : w < 2);
        v4u pv[7];
#pragma unroll
        for (int j = 0; j < 7; ++j) undef(pv[j]);
        if (on) {
          if (!ra || (NOEARLY && rep == 0)) lds_wait(lb + L_CNT + 44, 4 * layer + 4);
          lds_waits(lb + L_CNT + 24, 3 * layer + (ra ? 1 + 2 * rep : w < 6 ? 1 : 2));
          if (rep == 1) { WST(14); STAMP(13 + 16 * layer); } else WST(6);
#pragma unroll
          for (int j = 0; j < 7; ++j) pv[j] = ldx(rs, vs, po[j]);
        }
        SB();
        if (on) {
          const v4u xv = lds128(lb + u * 4096 + tt * 1024 + v16), ov = lds128(lb + u * 4096 + (tt ^ 1) * 1024 + v16);
          f32x4 ya = mfma(sel[0], ov, mfma(sel[0], xv, ldsf4(lb + L_TB2 + u * 128 + g * 32)));
          f32x4 yb = mfma(sel[1], ov, mfma(sel[1], xv, ldsf4(lb + L_TB2 + u * 128 + g * 32 + 16)));
#pragma unroll
          for (int j = 0; j < 7; ++j) { ya = mfma(sel[0], pv[j], ya); yb = mfma(sel[1], pv[j], yb); }
          if (HOLD && w != 1) lds_waits(lb + L_CNT + 24, 3 * layer + 3);
          if (layer == 0) stx(pk8(ya, yb), rs, vo, X2_OFF + gofs + u * 4096 + tt * 1024);
          else st128<16>(pk8(ya, yb), ro, vo, 64 * u);
        }
      }
      WST(15);
      if (layer == 0) {
        STAMP(14);
        drain();
        const bool nxt = c >= 6 && (G & 15) != 15;
        if (lds_ticket(lb + L_CNT + 56, lane) == 7) flag(rs, lane * 1024 + (lane & 8) * 2, mbg + (MB_X2 + c) * 8, lane < 8 || (nxt && lane < 16), tag);
      }
      WST(16);
    }
    STAMP(15 + 16 * layer);
    if (layer == 0) {
      FRESH();
      static_assert(CH1 == 6, "halo split");
      const int tt = w & 3;
      if (FW) lds_waits(lb + L_CNT + 56, 8);
      fill<0, 18>(ring, rs, v16, wq);
      int sm = X2_OFF + gofs + tt * 1024, sh = X2_OFF - 73728 + gofs + 3072 + tt * 4096;
      u32 la = lb + tt * 1024 + v16s + (ra ? 0 : 6 * 5120), lh = lb + 4096 + smul(tt + (ra ? 0 : 6), 5120) + v16s;
      asm volatile("" : "+s"(sm)); asm volatile("" : "+s"(sh)); asm volatile("" : "+v"(la), "+v"(lh));
      if (w == 7) poll<1>(rs, lane, mbg + c * 1024 + MB_X2 * 8, first ? 8 : 10, tag);
      bar();
      WST(17);
      STAMP(16);
      v4u xh[3];
#pragma unroll
      for (int i = 0; i < 3; ++i) xh[i] = v4u{0u, 0u, 0u, 0u};
      if (ra) {
        v4u xm[6];
#pragma unroll
        for (int i = 0; i < 6; ++i) xm[i] = ldx(rs, v16, sm + i * 4096);
        if (!first) { xh[0] = ldx(rs, v16, sh); if (tt < 2) xh[1] = ldx(rs, v16, sh + 16384); }
        SB();
#pragma unroll
        for (int i = 0; i < 6; ++i) sts128(la + i * 5120, xm[i]);
        sts128(lh, xh[0]);
        if (tt < 2) sts128(lh + 20480, xh[1]);
        lds_arrive(lb + L_CNT, lane);
      }
      int wb = w;
      asm volatile("" : "+s"(wb));
      if (wb >= 4) {
        __builtin_amdgcn_s_sleep(STSL);
        v4u xm[12];
#pragma unroll
        for (int i = 0; i < 12; ++i) xm[i] = ldx(rs, v16, sm + (6 + i) * 4096);
        if (!first) {
#pragma unroll
          for (int i = 0; i < 3; ++i) xh[i] = ldx(rs, v16, sh + (6 + 4 * i) * 4096);
        }
        SB();
#pragma unroll
        for (int i = 0; i < 12; ++i) sts128(la + i * 5120, xm[i]);
#pragma unroll
        for (int i = 0; i < 3; ++i) sts128(lh + i * 20480, xh[i]);
        lds_arrive(lb + L_CNT + 40, lane);
      }
    }
#pragma unroll
    for (int j = 0; j < 4; ++j) undef(mt[j]);
  }
  if (bid == 0 && tid == 448) stx2(tag, rs, 0, EP_OFF);
  if (w_ == 1) { while (__builtin_amdgcn_s_memrealtime() - prb < 10000ull) __builtin_amdgcn_s_sleep(1); }
}

extern "C" void kernel_launch(void* const* d_in, const int* in_sizes, int n_in, void* d_out, int out_size, void* d_ws, size_t ws_size,
                              hipStream_t stream) {
  const u16* x = (const u16*)d_in[0];
  const u16* wqkv = (const u16*)d_in[1];
  const u16* wo = (const u16*)d_in[2];
  const u16* w1 = (const u16*)d_in[4];
  const u16* b1 = (const u16*)d_in[5];
  const u16* w2 = (const u16*)d_in[6];
  const u16* b2 = (const u16*)d_in[7];
  (void)hipFuncSetAttribute((const void*)k_main, hipFuncAttributeMaxDynamicSharedMemorySize, L_TOTAL);
  k_main<<<256, 512, L_TOTAL, stream>>>(x, wqkv, wo, w1, b1, w2, b2, (u16*)d_out, (char*)d_ws);
}
```

```cpp
#include <hip/hip_runtime.h>

typedef unsigned short u16;
typedef unsigned u32;
typedef unsigned long long u64;
typedef u32 v4u __attribute__((ext_vector_type(4)));
typedef u32 v2u __attribute__((ext_vector_type(2)));
typedef float f32x4 __attribute__((ext_vector_type(4)));
typedef float f32x2 __attribute__((ext_vector_type(2)));
typedef short s16x4 __attribute__((ext_vector_type(4)));
typedef __bf16 bf16x2 __attribute__((ext_vector_type(2)));
typedef __bf16 bf16x8 __attribute__((ext_vector_type(8)));
#define GAS __attribute__((address_space(1)))
#define LAS __attribute__((address_space(3)))
#define DI __device__ __forceinline__
#define RLX __ATOMIC_RELAXED, __HIP_MEMORY_SCOPE_AGENT

constexpr int CODE_BYTES = 57344;
constexpr int CH1 = 6;
constexpr int EP_OFF = 0;
constexpr int MB_OFF = 4096, MB_BYTES = 3 * 256 * 1024;
constexpr int MB_O = 0, MB_X1 = 16, MB_A = 32, MB_B = 48, MB_X2 = 80, MB_WQ = 0, MB_W1 = 32, MB_W2 = 64;
constexpr int CSQ_OFF = MB_OFF + MB_BYTES;
constexpr int CS1_OFF = CSQ_OFF + 8 * 18432;
constexpr int WQ_OFF = CS1_OFF + 8 * 20736;
constexpr int WO_OFF = WQ_OFF + 64 * 36864;
constexpr int WM_OFF = WO_OFF + 80 * 18432;
constexpr int OB_OFF = WM_OFF + 64 * 165888;
constexpr int X1_OFF = OB_OFF + 64 * 73728;
constexpr int X2_OFF = X1_OFF + 64 * 73728;
constexpr int PB_OFF = X2_OFF + 32 * 73728;
constexpr int WS_USED = PB_OFF + 2 * 32 * 64 * 9216;
constexpr int L_E = 92160, L_OI = 92160, L_H = 73728;
constexpr int L_TQ = 92160;
constexpr int L_PX = 129024;
constexpr int L_SS = 147456;
constexpr int L_RK = L_SS + 1024;
constexpr int L_CNT = L_RK + 2048;
constexpr int L_TA = L_CNT + 128;
constexpr int L_TB2 = L_TA + 4608;
constexpr int L_TCQ = L_TB2 + 2304;
constexpr int L_TOTAL = L_TCQ + 1024;

DI float lo2f(u32 x) { return __builtin_bit_cast(float, (u32)x << 16); }
DI float hi2f(u32 x) { return __builtin_bit_cast(float, (u32)x & 0xffff0000u); }
DI u32 pk(float a, float b) { f32x2 f = {a, b}; return __builtin_bit_cast(u32, __builtin_convertvector(f, bf16x2)); }
DI f32x4 mfma(v4u a, v4u b, f32x4 c) {
  return __builtin_amdgcn_mfma_f32_16x16x32_bf16(__builtin_bit_cast(bf16x8, a), __builtin_bit_cast(bf16x8, b), c, 0, 0, 0);
}
DI f32x4 mfma16(v2u a, v2u b, f32x4 c) {
  return __builtin_amdgcn_mfma_f32_16x16x16bf16_1k(__builtin_bit_cast(s16x4, a), __builtin_bit_cast(s16x4, b), c, 0, 0, 0);
}
DI void mm(f32x4& c, v4u a, v4u b) { asm volatile("v_mfma_f32_16x16x32_bf16 %0, %1, %2, %0" : "+v"(c) : "v"(a), "v"(b)); }
DI void mm0(f32x4& c, v4u a, v4u b) { asm volatile("v_mfma_f32_16x16x32_bf16 %0, %1, %2, 0" : "=&v"(c) : "v"(a), "v"(b)); }
DI void mmk16(f32x4& c, v2u a, v2u b) { asm volatile("v_mfma_f32_16x16x16_bf16 %0, %1, %2, %0" : "+v"(c) : "v"(a), "v"(b)); }
DI void mm_end() { __builtin_amdgcn_sched_barrier(0); asm volatile("s_nop 7\n\ts_nop 1" ::: "memory"); __builtin_amdgcn_sched_barrier(0); }
#define SB() __builtin_amdgcn_sched_barrier(0)
#define INL __attribute__((always_inline))
DI int smul(int a, int k) { int r; asm("s_mul_i32 %0, %1, %2" : "=s"(r) : "s"(a), "s"(k)); return r; }
DI void bar() {
  asm volatile("s_waitcnt lgkmcnt(0)" ::: "memory");
  __builtin_amdgcn_s_barrier();
  asm volatile("" ::: "memory");
}
typedef __amdgpu_buffer_rsrc_t rsrc_t;
DI v4u ldw(rsrc_t rs, int v, int s) { return __builtin_amdgcn_raw_buffer_load_b128(rs, v, s, 16); }
DI v4u ldp(rsrc_t rs, int v, int s) { return __builtin_amdgcn_raw_buffer_load_b128(rs, v, s, 0); }
constexpr int OOB = 0x70000000;
DI v4u ldx(rsrc_t rs, int v, int s) { return __builtin_amdgcn_raw_buffer_load_b128(rs, v, s, 16); }
DI v2u ldx2(rsrc_t rs, int v, int s) { return __builtin_amdgcn_raw_buffer_load_b64(rs, v, s, 16); }
DI void stx(v4u d, rsrc_t rs, int v, int s) { __builtin_amdgcn_raw_buffer_store_b128(d, rs, v, s, 16); asm volatile("s_nop 1" :: "v"(d)); }
DI void stx2(v2u d, rsrc_t rs, int v, int s) { __builtin_amdgcn_raw_buffer_store_b64(d, rs, v, s, 16); }
DI v4u lds128(u32 a) { return *(const LAS v4u*)(size_t)a; }
DI v2u lds64(u32 a) { return *(const LAS v2u*)(size_t)a; }
DI f32x4 ldsf4(u32 a) { return *(const LAS f32x4*)(size_t)a; }
DI f32x2 ldsf2(u32 a) { return *(const LAS f32x2*)(size_t)a; }
DI void sts128(u32 a, v4u v) { *(LAS v4u*)(size_t)a = v; }
DI void sts64(u32 a, v2u v) { *(LAS v2u*)(size_t)a = v; }
DI v2u ldstr(u32 a) { return __builtin_bit_cast(v2u, __builtin_amdgcn_ds_read_tr16_b64_v4i16((LAS s16x4*)(size_t)a)); }

DI int colp(int u, int e, int m) { return 32 * u + 8 * (m >> 2) + 4 * e + (m & 3); }

DI void lds_arrive(u32 a, int lane) {
  if (lane == 0) asm volatile("s_waitcnt lgkmcnt(0)\n\tds_add_u32 %0, %1" :: "v"(a), "v"(1u) : "memory");
}
DI void lds_waits(u32 a, u32 n) {
  while ((u32)__builtin_amdgcn_readfirstlane(*(volatile LAS u32*)(size_t)a) < n) __builtin_amdgcn_s_sleep(2);
  asm volatile("" ::: "memory");
}
DI void lds_wait(u32 a, u32 n) {
  while ((u32)__builtin_amdgcn_readfirstlane(*(volatile LAS u32*)(size_t)a) < n) {}
  asm volatile("" ::: "memory");
}
#define REFILL(f_, NU) do { const int q_ = (f_) % 18, u_ = (f_) / 18; \
    if (u_ < (NU) - 1) ring[q_] = ldw(rs, v16 + (q_ & 3) * 1024, s0 + u_ * 18432 + (q_ >> 2) * 4096); \
    else if (LAST == 1) ring[q_] = ldw(rs, v16 + (q_ & 3) * 1024, slast + (q_ >> 2) * 4096); } while (0)
template <int NT, int MIDBAR, int LAST, class MID, class PRE>
DI void group(f32x4 (&acc)[NT][4], v4u (&ring)[18], u32 xa, rsrc_t rs, int v16, int s0, int slast, MID&& mid, PRE&& pre) {
  v4u xf[2][4];
  u32 xb = xa + 9 * 4096;
  asm volatile("" : "+v"(xa), "+v"(xb));
#pragma unroll
  for (int t = 0; t < 4; ++t) xf[0][t] = lds128(xa + t * 1024);
  SB();
#pragma unroll
  for (int ks = 0; ks < 18; ++ks) {
    const int kn = ks + 1;
#pragma unroll
    for (int n = 0; n < NT; ++n) {
      const int f = ks * NT + n;
#pragma unroll
      for (int t = 0; t < 4; ++t) {
        if (ks == 0) mm0(acc[n][t], ring[f % 18], xf[0][t]); else mm(acc[n][t], ring[f % 18], xf[ks & 1][t]);
        if (t == 0 && f > 0) REFILL(f - 1, NT);
        const int n0 = kn == MIDBAR ? NT - 2 : 0;
        const int q = n == n0 ? t - 1 : (n == n0 + 1 && t == 1 ? 3 : -1);
        if (q == 0 && kn == MIDBAR) { mid(); SB(); }
        if (q >= 0 && kn < 18) xf[kn & 1][q] = kn < 9 ? lds128(xa + kn * 4096 + q * 1024) : lds128(xb + (kn - 9) * 4096 + q * 1024);
        if (kn == 18 && n == 0 && t == 1) pre();
        SB();
      }
    }
  }
  REFILL(18 * NT - 1, NT);
  SB();
}
template <int REF, int MIDBAR, class MID, class PRE>
DI void group36(f32x4 (&acc)[2][4], v4u (&r0)[18], v4u (&r1)[18], u32 xa, rsrc_t rs, int v16, int sn, MID&& mid, PRE&& pre) {
  v4u xf[2][4];
  u32 xb = xa + 9 * 4096;
  asm volatile("" : "+v"(xa), "+v"(xb));
#pragma unroll
  for (int t = 0; t < 4; ++t) xf[0][t] = lds128(xa + t * 1024);
  SB();
#pragma unroll
  for (int f = 0; f < 36; ++f) {
    const int ks = f >> 1, n = f & 1, kn = ks + 1;
#pragma unroll
    for (int t = 0; t < 4; ++t) {
      if (ks == 0) mm0(acc[n][t], r0[f], xf[0][t]); else mm(acc[n][t], f < 18 ? r0[f % 18] : r1[f % 18], xf[ks & 1][t]);
      if (REF == 2 && t == 0 && f > 0) (f - 1 < 18 ? r0[(f - 1) % 18] : r1[(f - 1) % 18]) = ldw(rs, v16 + ((f - 1) & 3) * 1024, sn + ((f - 1) >> 2) * 4096);
      if (REF == 1 && t == 0 && n == 1) r0[ks] = ldw(rs, v16 + (ks & 3) * 1024, sn + (ks >> 2) * 4096);
      const int q = n == 0 ? t - 1 : (t == 1 ? 3 : -1);
      if (q == 0 && kn == MIDBAR) { mid(); SB(); }
      if (q >= 0 && kn < 18) xf[kn & 1][q] = kn < 9 ? lds128(xa + kn * 4096 + q * 1024) : lds128(xb + (kn - 9) * 4096 + q * 1024);
      if (f == 34 && t == 1) pre();
      SB();
    }
  }
  if (REF == 2) r1[17] = ldw(rs, v16 + 3072, sn + 8 * 4096);
  SB();
}
DI void group36h(f32x4 (&acc)[2][2], const v4u (&r0)[18], const v4u (&r1)[18], u32 xa) {
  v4u xf[3][2];
  u32 xb = xa + 9 * 4096;
  asm volatile("" : "+v"(xa), "+v"(xb));
#pragma unroll
  for (int k = 0; k < 2; ++k) { xf[k][0] = lds128(xa + k * 4096); xf[k][1] = lds128(xa + k * 4096 + 1024); }
  SB();
#pragma unroll
  for (int f = 0; f < 36; ++f) {
    const int ks = f >> 1, n = f & 1, kn = ks + 2;
#pragma unroll
    for (int t = 0; t < 2; ++t) {
      if (ks == 0) mm0(acc[n][t], r0[f], xf[0][t]); else mm(acc[n][t], f < 18 ? r0[f % 18] : r1[f % 18], xf[ks % 3][t]);
      if (n == 0 && kn < 18) xf[kn % 3][t] = kn < 9 ? lds128(xa + kn * 4096 + t * 1024) : lds128(xb + (kn - 9) * 4096 + t * 1024);
      SB();
    }
  }
  mm_end();
}
template <class PRE>
DI void groupq(f32x4 (&a0)[5], f32x4 (&a1)[4], v4u (&ring)[18], u32 x0, u32 x1, u32 x4, rsrc_t rs, int v16, int s0, int slast, u32 cnt, u32 want, PRE&& pre) {
  constexpr int LAST = 1;
  v4u xf[2][5];
  u32 y0 = x0 + 46080, y1 = x1 + 46080, y4 = x4 + 46080;
  asm volatile("" : "+v"(y0), "+v"(y1), "+v"(y4));
  xf[0][0] = lds128(x0); xf[0][1] = lds128(x1); xf[0][2] = lds128(x1 + 1024); xf[0][3] = lds128(x1 + 2048); xf[0][4] = lds128(x4);
  SB();
#pragma unroll
  for (int ks = 0; ks < 18; ++ks) {
    const int sl = (2 * ks) % 18, kn = ks + 1, o = (kn % 9) * 5120;
#pragma unroll
    for (int t = 0; t < 5; ++t) {
      if (ks == 0) mm0(a0[t], ring[sl], xf[0][t]); else mm(a0[t], ring[sl], xf[ks & 1][t]);
      if (t == 0 && ks > 0) REFILL(2 * ks - 1, 2);
      if (t == 1 && kn == CH1) { lds_wait(cnt, want); SB(); }
      if (t == 0 && ks == 17) pre();
      if (t > 0 && kn < 18) xf[kn & 1][t - 1] = lds128(t == 1 ? (kn < 9 ? x0 : y0) + o : (kn < 9 ? x1 : y1) + o + (t - 2) * 1024);
      SB();
    }
#pragma unroll
    for (int t = 0; t < 4; ++t) {
      if (ks == 0) mm0(a1[t], ring[sl + 1], xf[0][t]); else mm(a1[t], ring[sl + 1], xf[ks & 1][t]);
      if (t == 0) REFILL(2 * ks, 2);
      if (t == 1 && kn < 18) xf[kn & 1][4] = lds128((kn < 9 ? x4 : y4) + o);
      SB();
    }
  }
  mm_end();
  REFILL(35, 2);
  SB();
}
template <class PRE> DI void groupo(f32x4 (&a)[4], const v4u (&ring)[18], u32 x0, PRE&& pre) {
  v4u xf[3][2];
#pragma unroll
  for (int k = 0; k < 2; ++k) { xf[k][0] = lds128(x0 + k * 2048); xf[k][1] = lds128(x0 + k * 2048 + 1024); }
  SB();
#pragma unroll
  for (int ks = 0; ks < 9; ++ks) {
    const int kn = ks + 2;
#pragma unroll
    for (int j = 0; j < 4; ++j) {
      mm(a[j], ring[2 * ks + (j >> 1)], xf[ks % 3][j & 1]);
      if (kn < 9 && j < 2) xf[kn % 3][j] = lds128(x0 + kn * 2048 + j * 1024);
      if (ks == 7 && j == 0) pre();
      SB();
    }
  }
  mm_end();
}
DI float max3(float a, float b, float c) { float r; asm("v_max3_f32 %0, %1, %2, %3" : "=v"(r) : "v"(a), "v"(b), "v"(c)); return r; }
DI float max2(float a, float b) { float r; asm("v_max_f32 %0, %1, %2" : "=v"(r) : "v"(a), "v"(b)); return r; }
DI f32x4 unlo(v4u x) { return f32x4{lo2f(x[0]), hi2f(x[0]), lo2f(x[1]), hi2f(x[1])}; }
DI f32x4 unhi(v4u x) { return f32x4{lo2f(x[2]), hi2f(x[2]), lo2f(x[3]), hi2f(x[3])}; }
template <int LO, int HI> DI void fill(v4u (&ring)[18], rsrc_t rs, int v16, int s) {
  SB();
#pragma unroll
  for (int i = LO; i < HI; ++i) ring[i] = ldw(rs, v16 + (i & 3) * 1024, s + (i >> 2) * 4096);
  SB();
}
template <class T> DI void undef(T& v) { asm volatile("" : "=v"(v)); }
DI void stat_acc(v4u v, f32x4& s, f32x4& q) { s = mfma(v4u{0x3f803f80u, 0x3f803f80u, 0x3f803f80u, 0x3f803f80u}, v, s); q = mfma(v, v, q); }
DI float diag(f32x4 q, int tl) {
  const float a = (tl & 1) ? q[1] : q[0], b = (tl & 1) ? q[3] : q[2];
  return (tl & 2) ? b : a;
}
DI void split3(float x, u32& h, u32& m, u32& l) {
  h = pk(x, 0.f); x -= lo2f(h);
  m = pk(x, 0.f); x -= lo2f(m);
  l = pk(x, 0.f);
}
struct RowK { f32x4 k[4]; v2u aug[4]; };
template <int N> DI void gelu(float (&v)[N], f32x4 k) {
  float y[N];
#pragma unroll
  for (int r = 0; r < N; ++r) y[r] = v[r] * v[r];
  SB();
#pragma unroll
  for (int r = 0; r < N; ++r) y[r] = __builtin_fmaf(y[r], k[1], k[0]);
  SB();
#pragma unroll
  for (int r = 0; r < N; ++r) y[r] = v[r] * y[r];
  SB();
#pragma unroll
  for (int r = 0; r < N; ++r) y[r] = __builtin_amdgcn_exp2f(y[r]);
  SB();
#pragma unroll
  for (int r = 0; r < N; ++r) y[r] = __builtin_fmaf(y[r], k[2], k[2]);
  SB();
#pragma unroll
  for (int r = 0; r < N; ++r) y[r] = __builtin_amdgcn_rcpf(y[r]);
  SB();
#pragma unroll
  for (int r = 0; r < N; ++r) v[r] = v[r] * y[r];
  SB();
}
DI v2u gelu4(f32x4 a, f32x4 k) {
  float v[4] = {a[0], a[1], a[2], a[3]};
  gelu<4>(v, k);
  return v2u{pk(v[0], v[1]), pk(v[2], v[3])};
}
DI void aug_tile(f32x4 (&a)[4], v2u ta, const RowK& k) {
#pragma unroll
  for (int t = 0; t < 4; ++t) mmk16(a[t], ta, k.aug[t]);
}
DI void gelu_pair(f32x4 (&a0)[4], f32x4 (&a1)[4], u32 hp, const RowK& k) {
#pragma unroll
  for (int t = 0; t < 4; ++t) {
    float v[8] = {a0[t][0], a0[t][1], a0[t][2], a0[t][3], a1[t][0], a1[t][1], a1[t][2], a1[t][3]};
    gelu<8>(v, k.k[t]);
    sts128(hp + t * 1024, v4u{pk(v[0], v[1]), pk(v[2], v[3]), pk(v[4], v[5]), pk(v[6], v[7])});
  }
}
DI v4u pk8(f32x4 a, f32x4 b) { return v4u{pk(a[0], a[1]), pk(a[2], a[3]), pk(b[0], b[1]), pk(b[2], b[3])}; }
DI void part_pair(const f32x4 (&a0)[4], const f32x4 (&a1)[4], rsrc_t rs, int v16, int sb, int town, u32 lown) {
#pragma unroll
  for (int t = 0; t < 4; ++t) {
    const v4u d = pk8(a0[t], a1[t]);
    if (t == town) sts128(lown, d); else stx(d, rs, v16, sb + t * (2 * 8 * 9216));
  }
}
DI void make_sel(v4u (&sel)[2], int g, int tl) {
  const u32 one = g == (tl >> 2) ? ((tl & 1) ? 0x3f800000u : 0x00003f80u) : 0u, lo = (tl & 2) ? 0u : one, hi = (tl & 2) ? one : 0u;
  sel[0] = v4u{lo, hi, 0u, 0u};
  sel[1] = v4u{0u, 0u, lo, hi};
}
typedef GAS u64 gu64;
DI void drain() { asm volatile("s_waitcnt vmcnt(0) lgkmcnt(0)" ::: "memory"); __builtin_amdgcn_s_waitcnt(0x0070); }
DI void flag(rsrc_t rs, int v, int s, bool on, v2u tag) { if (on) stx2(tag, rs, v, s); }
template <int SL> DI void poll(rsrc_t rs, int lane, int s, int n, v2u tag) {
  if (lane < n) {
#pragma unroll 1
    for (u32 sp = 0; sp < (1u << 16); ++sp) {
      v2u x = {0u, 0u};
      asm volatile("s_nop 4\n\tbuffer_atomic_or_x2 %0, %1, %2, %3 offen sc0\n\ts_waitcnt vmcnt(0)" : "+v"(x) : "v"(lane * 8), "s"(rs), "s"(s) : "memory");
      if (__builtin_amdgcn_ballot_w64(((x[0] ^ tag[0]) | (x[1] ^ tag[1])) != 0u) == 0) break;
      if (SL) __builtin_amdgcn_s_sleep(SL);
      asm volatile("" ::: "memory");
    }
  }
  asm volatile("" ::: "memory");
}
DI u32 lds_ticket(u32 a, int lane) {
  u32 r = 0;
  if (lane == 0) asm volatile("s_waitcnt lgkmcnt(0)\n\tds_add_rtn_u32 %0, %1, %2\n\ts_waitcnt lgkmcnt(0)" : "=v"(r) : "v"(a), "v"(1u) : "memory");
  return __builtin_amdgcn_readfirstlane(r);
}
DI void tile_tr(const v4u* m, v4u (&f)[4], u32 lw, int lane) {
  const u32 wr = lw + (lane >> 3) * 144 + (lane & 7) * 16, tr = lw + (8 * (lane >> 4) + ((lane & 15) >> 2)) * 144 + (lane & 3) * 16;
#pragma unroll
  for (int j = 0; j < 4; ++j) sts128(wr + j * 8 * 144, m[j]);
  asm volatile("s_waitcnt lgkmcnt(0)" ::: "memory");
#pragma unroll
  for (int q = 0; q < 4; ++q) {
    const v2u lo = ldstr(tr + (q >> 1) * 64 + (q & 1) * 8), hi = ldstr(tr + (q >> 1) * 64 + (q & 1) * 8 + 4 * 144);
    f[q] = v4u{lo[0], lo[1], hi[0], hi[1]};
  }
  asm volatile("s_waitcnt lgkmcnt(0)" : "+v"(f[0]), "+v"(f[1]), "+v"(f[2]), "+v"(f[3]) :: "memory");
}
template <int MT> DI void mlp_dest(int c, int cb, int q, int& so, int& st) {
  const int u = 2 * cb + (q >> 1), e = q & 1;
  int wd, y0;
  if (MT == 0) {
    wd = u < 8 ? u : u < 16 ? u - 8 : 4 + q;
    y0 = u < 8 ? e : u < 16 ? 36 + e : 38;
    st = u < 12 ? 2048 : 3072;
  } else {
    const bool sp = cb == 8, fa = (cb & 1) == 0;
    wd = sp ? q : fa ? cb >> 1 : 4 + (cb >> 1);
    y0 = sp ? 76 : fa ? 72 + q : 90 + 36 * (q >> 1) + e;
    st = fa ? 5120 : 2048;
  }
  so = WM_OFF + smul(c * 8 + wd, 165888) + y0 * 1024;
}
#define FRESH() \
  int lane = lane_, w = w_, G = G_, c = c_; \
  asm volatile("" : "+s"(w)); asm volatile("" : "+s"(G)); asm volatile("" : "+s"(c)); asm volatile("" : "+v"(lane)); \
  const int g = lane >> 4, tl = lane & 15, v16 = lane * 16, v16s = (lane ^ g) * 16; \
  const int wq = WQ_OFF + smul(c * 8 + w, 36864), wm = WM_OFF + smul(c * 8 + w, 165888), woc = WO_OFF + smul(c, 184320); \
  const int us = (c & 4) ? 13 : 4, ub = (c & 2) ? us + 1 : us - 4; \
  const bool first = (G & 15) == 0, ra = w < 4; \
  const int rowbase = 2 * 64 * (G & 15) + (G >> 4), gofs = smul(layer * 32 + G, 73728), mbg = MB_OFF + (layer * 256 + G * 8) * 1024; \
  (void)g; (void)tl; (void)v16; (void)v16s; (void)wq; (void)wm; (void)woc; (void)ub; (void)us; (void)first; (void)ra; (void)rowbase; (void)gofs; (void)mbg;

#if defined(__HIP_DEVICE_COMPILE__)
#define NOPK __attribute__((target("no-packed-fp32-ops")))
#else
#define NOPK
#endif
__global__ NOPK void __launch_bounds__(512, 2) k_main(const u16* __restrict__ x, const u16* __restrict__ wqkv, const u16* __restrict__ wo,
    const u16* __restrict__ w1, const u16* __restrict__ b1, const u16* __restrict__ w2, const u16* __restrict__ b2, u16* __restrict__ out, char* __restrict__ ws) {
  extern __shared__ __attribute__((aligned(16))) char smem[];
  const u32 lb = (u32)(size_t)(LAS char*)smem;
  u32 codew;
  {
    u64 pc;
    asm volatile("s_getpc_b64 %0" : "=s"(pc));
    const u32 t = __builtin_amdgcn_workitem_id_x(), ln = (__builtin_amdgcn_workgroup_id_x() & ~7u) + (t >> 6) + 256 * (t & 1);
    const GAS u32* cp = (const GAS u32*)((pc & ~255ull) + (ln * 128 < CODE_BYTES ? ln * 128 : 0));
    codew = *cp;
    SB();
  }
  asm volatile("" :: "s"(x), "s"(wqkv), "s"(wo), "s"(w1), "s"(b2), "s"(ws));
  const int tid = __builtin_amdgcn_workitem_id_x(), bid = __builtin_amdgcn_workgroup_id_x(), lane_ = tid & 63, w_ = __builtin_amdgcn_readfirstlane(tid >> 6);
  int G_, c_;
  {
    const int xc = bid & 7, s = bid >> 3;
    G_ = 8 * (xc & 3) + (s >> 2); c_ = 4 * (xc >> 2) + (s & 3);
  }
  const rsrc_t rs = __builtin_amdgcn_make_buffer_rsrc((void*)ws, 0, WS_USED, 0x00020000);
  const rsrc_t rx = __builtin_amdgcn_make_buffer_rsrc((void*)x, 0, 2048 * 1152, 0x00020000);
  const rsrc_t ro = __builtin_amdgcn_make_buffer_rsrc((void*)out, 0, 2048 * 1152, 0x00020000);
  v2u tag;
  v4u ring[18];
  f32x4 m0, m1;
  v2u km;
#define MASKS() \
  _Pragma("unroll") for (int r = 0; r < 4; ++r) { \
    const int kl = 4 * g + r; \
    m0[r] = (kl >= tl + 9 && !(first && w < 2)) ? 0.f : -1e30f; \
    m1[r] = (kl <= tl && kl + 7 >= tl) ? 0.f : -1e30f; \
  } \
  km[0] = (4 * g >= tl + 9 ? 0xffffu : 0u) | (4 * g + 1 >= tl + 9 ? 0xffff0000u : 0u); \
  km[1] = (4 * g + 2 >= tl + 9 ? 0xffffu : 0u) | (4 * g + 3 >= tl + 9 ? 0xffff0000u : 0u); \
  asm volatile("" : "+v"(m0), "+v"(m1), "+v"(km));
  {
    const int layer = 2;
    FRESH();
    if (tid < 32) *(LAS u32*)(size_t)(lb + L_CNT + 4 * tid) = 0u;
    const int r8 = lane >> 3, j8 = lane & 7, tt = w & 3;
    const bool isq = G < 18;
    const v4u ones = {0x3f803f80u, 0x3f803f80u, 0x3f803f80u, 0x3f803f80u};
    const u32 lw = lb + L_TQ + 18432 + w * 4608;
    v4u mw[8];
    const int kt = smul(G, 57) >> 9, cb = G - smul(kt, 9), t3 = (tt - c - 1) & 3, p0 = smul(kt, 6) + 2 * t3;
    const bool lead = !ra && t3 == 3;
    const bool is1 = G < 27 && !ra && !lead;
    if (ra && isq) {
      const int hi = lane >= 27, r = lane - 27 * hi, sec = (r >= 9) + (r >= 18);
      const int vo = (hi * 3456 + sec * (1152 - 144) + r * 16) | (lane < 54 ? 0 : OOB), s0 = smul(32 * G + 8 * w, 3456) + smul(c, 144);
      const rsrc_t rq = __builtin_amdgcn_make_buffer_rsrc((void*)wqkv, 0, 576 * 3456, 0x00020000);
      v4u a[4], f[4];
#pragma unroll
      for (int j = 0; j < 4; ++j) a[j] = ldp(rq, vo, s0 + j * (2 * 3456));
      SB();
      bar();
      const u32 la = lb + L_TQ + (lane < 54 ? w * 3584 : 14336) + hi * 448 + r * 16;
      const u32 tb = lb + L_TQ + (8 * g + (tl >> 2)) * 448, mo = tb + 72 * (w >> 1) + 40 * (w & 1) + 8 * (tl & 3);
      const u32 ro_ = tb + 32 + (w < 2 ? 0 : 288) + (tl & 1) * (w < 2 ? 144 : 72) + (tl & 2) * (w < 2 ? 36 : 0);
#pragma unroll
      for (int j = 0; j < 4; ++j) sts128(la + j * 896, a[j]);
      lds_arrive(lb + L_CNT + 12, lane);
      lds_wait(lb + L_CNT + 12, 4);
#pragma unroll
      for (int j = 0; j < 4; ++j) {
        const u32 ad = j == 0 ? mo + 144 : j == 1 ? mo : j == 2 ? mo + 288 : ro_;
        const v2u lo = ldstr(ad), hi_ = ldstr(ad + 4 * 448);
        f[j] = v4u{lo[0], lo[1], hi_[0], hi_[1]};
      }
#pragma unroll
      for (int j = 0; j < 4; ++j) stx(f[j], rs, v16, wq + G * 2048 + (j & 1) * 1024 + (j >> 1) * (4 * 36864));
      f32x4 cs[4];
#pragma unroll
      for (int j = 0; j < 4; ++j) cs[j] = mfma(f[j], ones, f32x4{0.f, 0.f, 0.f, 0.f});
      const f32x4 d = tl == 0 ? cs[0] : tl == 1 ? cs[1] : tl == 2 ? cs[2] : cs[3];
      stx(__builtin_bit_cast(v4u, d), rs, tl < 4 ? (tl >> 1) * 512 + (tl & 1) * 64 + g * 16 : OOB, CSQ_OFF + smul(c, 18432) + G * 1024 + w * 128);
    }
    if (ra && !isq) {
      const int ti = (G - 18) * 4 + w, tk = smul(ti, 43) >> 7, tp = ti - smul(tk, 3);
      bar();
      if (ti < 54) {
        const int vo = ((r8 >> 2) * (tk < 16 ? 20 : 36) + (r8 & 3)) * 1152 + j8 * 16;
        const int s0 = smul(tk, tk < 16 ? 36 * 1152 : 288 * 1152) - (tk < 16 ? 0 : (16 * 288 - 16) * 1152) + (tp < 2 ? ub + 2 * tp : us) * 64, ss = tk < 16 ? 4 * 1152 : 72 * 1152;
        const rsrc_t rw = __builtin_amdgcn_make_buffer_rsrc((void*)wo, 0, 576 * 1152, 0x00020000);
        v4u a[4], f[4];
        a[0] = ldp(rw, vo, s0); a[1] = ldp(rw, vo, s0 + ss); a[2] = ldp(rw, vo, s0 + 2 * ss); a[3] = ldp(rw, vo, s0 + 2 * ss + ss);
        SB();
        tile_tr(a, f, lw, lane);
        const int so = woc + (tp < 2 ? smul(tp, 2 * 18432) + tk * 2048 + (tk < 9 ? 0 : 3 * 18432) : 8 * 18432 + tk * 1024);
        stx(f[0], rs, v16, so); stx(f[1], rs, v16, so + (tp < 2 ? 1024 : 18432));
        if (tp < 2) { stx(f[2], rs, v16, so + 18432); stx(f[3], rs, v16, so + 18432 + 1024); }
      }
    }
    if (!ra) {
      bar();
      u64 ep = 0;
#define MBWARM(lo_, hi_) {     \
        const u32 z = 0, mo = lane >= (lo_) && lane < (hi_) ? (lane >> 4) * (256 * 1024) + (lane & 15) * 64 : OOB; \
        asm volatile("s_nop 4\n\tbuffer_atomic_or %0, %1, %2, %3 offen" :: "v"(z), "v"(mo), "s"(rs), "s"(MB_OFF + (G * 8 + c) * 1024) : "memory"); }
      if (tt == ((c + 1) & 3)) {
        ep = __hip_atomic_load((gu64*)(ws + EP_OFF), RLX);
        MBWARM(32, 36);
        *(LAS u64*)(size_t)(lb + L_CNT + 32) = ep + 1;
        lds_arrive(lb + L_CNT + 28, lane);
      }
#define W1LOAD() { \
        const rsrc_t r1 = __builtin_amdgcn_make_buffer_rsrc((void*)w1, 0, 576 * 9216, 0x00020000); \
        const int s1 = smul(p0, 32 * 9216) + smul(c, 1152) + cb * 128; \
        _Pragma("unroll") for (int j = 0; j < 8; ++j) mw[j] = ldp(r1, r8 * 9216 + j8 * 16, s1 + j * (8 * 9216)); }
      SB();
      const int vx = (rowbase + 32 * tt + 2 * r8) * 1152 + j8 * 16, vh = first ? OOB : (rowbase - 16 + 2 * r8) * 1152 + j8 * 16 + tt * 128;
      u32 lx = lb + (j8 >> 2) * 5120 + ((j8 & 3) * 16 + (r8 ^ (j8 & 3))) * 16, lh = lx + 4096 + tt * 10240;
      lx += tt * 1024;
      u32 ly = lx + 5 * 10240;
      asm volatile("" : "+v"(lx), "+v"(ly));
      if (!lead) lds_wait(lb + L_CNT + 124, 1);
      SB();
      v4u xv[18], xh[3];
#pragma unroll
      for (int i = 0; i < 9; ++i) { xv[2 * i] = ldp(rx, vx + i * 128, 0); xv[2 * i + 1] = ldp(rx, vx + i * 128, 16 * 1152); }
      xh[0] = ldp(rx, vh, 0); xh[1] = ldp(rx, vh + 512, 0);
      undef(xh[2]);
      if (tt == 0) xh[2] = ldp(rx, vh + 1024, 0);
      const u32 bv = b2[tid - 256], bv2 = b2[tid], bv3 = b2[512 + lane];
      auto xput = [&]() INL {
      SB();
#pragma unroll
      for (int i = 0; i < 18; ++i) {
        sts128((i < 10 ? lx + (i >> 1) * 10240 : ly + ((i >> 1) - 5) * 10240) + (i & 1) * 128, xv[i]);
        if (i == 17 && lead) lds_arrive(lb + L_CNT + 124, lane);
      }
      sts128(lh, xh[0]); sts128(lh + 128, xh[0]); sts128(lh + 40960, xh[1]); sts128(lh + 40960 + 128, xh[1]);
      if (tt == 0) { sts128(lh + 81920, xh[2]); sts128(lh + 81920 + 128, xh[2]); }
      *(LAS u32*)(size_t)(lb + L_TB2 + 4 * (tid - 256)) = bv << 16;
      *(LAS u32*)(size_t)(lb + L_TB2 + 4 * tid) = bv2 << 16;
      *(LAS u32*)(size_t)(lb + L_TB2 + 2048 + 4 * lane) = bv3 << 16;
      asm volatile("" :: "v"(codew));
      };
      if (is1) { W1LOAD(); xput(); } else xput();
      lds_arrive(lb + L_CNT, lane);
      if (!lead) lds_arrive(lb + L_CNT + 40, lane);
      if (lead) { __builtin_amdgcn_s_sleep(32); MBWARM(0, 32); MBWARM(36, 48); }
    }
    if (ra) { drain(); asm volatile("" :: "v"(codew)); }
    lds_wait(lb + L_CNT + 28, 1);
    tag = lds64(lb + L_CNT + 32);
    if (ra && lds_ticket(lb + L_CNT + 20, lane) == 3) {
      flag(rs, lane * 8192, MB_OFF + (2 * 256 + c) * 1024 + (G + MB_WQ) * 8, lane < 32, tag);
      lds_arrive(lb + L_CNT + 16, lane);
    }
    if (w == 0) { poll<0>(rs, lane, mbg + c * 1024 + MB_WQ * 8, 18, tag); lds_arrive(lb + L_CNT + 8, lane); }
    MASKS();

    lds_wait(lb + L_CNT + 8, 1);
    if (lead) { poll<0>(rs, lane, mbg + c * 1024 + (MB_WQ + 18) * 8, 14, tag); lds_arrive(lb + L_CNT + 40, lane); }
    else if (!ra) __builtin_amdgcn_s_sleep(8);
    fill<0, 18>(ring, rs, v16, wq);
    __builtin_amdgcn_s_waitcnt(0x4F72);
      if (is1) {
        v4u f[2][4];
        f32x4 cs[4];
        tile_tr(mw, f[0], lb + L_PX + tt * 4608, lane);
        tile_tr(mw + 4, f[1], lb + L_PX + tt * 4608, lane);
#pragma unroll
        for (int q = 0; q < 4; ++q) cs[q] = mfma(f[1][q], ones, mfma(f[0][q], ones, f32x4{0.f, 0.f, 0.f, 0.f}));
        const f32x4 d = tl == 0 ? cs[0] : tl == 1 ? cs[1] : tl == 2 ? cs[2] : cs[3];
        lds_wait(lb + L_CNT + 16, 1);
#pragma unroll
        for (int q = 0; q < 4; ++q) { int so, st; mlp_dest<0>(c, cb, q, so, st); so += smul(st, p0); stx(f[0][q], rs, v16, so); stx(f[1][q], rs, v16, so + st); }
        stx(__builtin_bit_cast(v4u, d), rs, tl < 4 ? tl * 64 + g * 16 : OOB, CS1_OFF + smul(c, 20736) + smul(smul(kt, 3) + t3, 2304) + cb * 256);
      }
    if (lead) {
      v4u p[18];
#pragma unroll
      for (int i = 0; i < 18; ++i) p[i] = ldx(rs, v16, CSQ_OFF + smul(c, 18432) + i * 1024);
      f32x4 cs = __builtin_bit_cast(f32x4, p[0]);
#pragma unroll
      for (int i = 1; i < 18; ++i) cs += __builtin_bit_cast(f32x4, p[i]);
      *(LAS f32x4*)(size_t)(lb + L_TCQ + v16) = cs;
      lds_arrive(lb + L_CNT + 84, lane);
    }
  }

#pragma unroll 1
  for (int layer = 0; layer < 2; ++layer) {
    v4u mt[4], nring[18], ex[9];
    {
      FRESH();
      const int tt = w & 3;
      const bool swp = (w == 5) || (w == 7);
      f32x4 a0[5], a1[4];
      u32 x0 = lb + v16s + (swp ? 4096 : 0), x1 = lb + v16s + 1024, x4 = lb + v16s + (swp ? 0 : 4096);
      int sq = wq + 18432, so = woc + smul(w, 18432);
      const int p = w & 1, qt = w >> 1;
      u32 e0 = lb + L_E + smul(ra ? w : 4 + w, 2560) + lane * 8, e1 = lb + L_E + smul(ra ? 4 + w : 10 + qt, 2560) + lane * 8;
      u32 sa = lb + L_SS + tl * 8, ca = lb + L_TCQ + w * 128 + g * 16;
      u32 el = lb + L_E + qt * 512 + p * 5120 + lane * 8, et = lb + L_E + qt * 512 + ((lane & 3) * 16 + 4 * g + (tl >> 2)) * 8, er = lb + L_E + 12 * 2560 + qt * 512 + p * 256 + tl * 8;
      int om_ = OB_OFF + gofs + qt * 1024 + (2 * c + p) * 4096, or_ = OB_OFF + gofs + qt * 1024 + (16 + (c >> 2)) * 4096 + (c & 3) * 256 + p * 128, fs = mbg + (MB_O + c) * 8;
      asm volatile("" : "+s"(sq)); asm volatile("" : "+s"(so)); asm volatile("" : "+s"(om_)); asm volatile("" : "+s"(or_)); asm volatile("" : "+s"(fs));
      asm volatile("" : "+v"(x0), "+v"(x1), "+v"(x4), "+v"(e0), "+v"(e1), "+v"(sa), "+v"(ca), "+v"(el), "+v"(et), "+v"(er));
      f32x2 rs_[5];
      f32x4 c0, c1;
      auto rowk = [&]() INL {
        lds_wait(lb + L_CNT + 84, 4 * layer + 5);
#pragma unroll
        for (int t = 0; t < 5; ++t) rs_[t] = ldsf2(sa + (t == 0 ? (swp ? 512 : 0) : t == 4 ? (swp ? 0 : 512) : t * 128));
        c0 = ldsf4(ca); c1 = ldsf4(ca + 64);
      };
      if (layer == 1) {
      int sm = X2_OFF - 32 * 73728 + gofs + tt * 1024, sh = X2_OFF - 33 * 73728 + gofs + 3072 + tt * 4096;
      u32 la = lb + tt * 1024 + v16s + (ra ? 0 : 6 * 5120), lh = lb + 4096 + smul(tt + (ra ? 0 : 6), 5120) + v16s;
      asm volatile("" : "+s"(sm)); asm volatile("" : "+s"(sh)); asm volatile("" : "+v"(la), "+v"(lh));
      if (w == 7) poll<1>(rs, lane, mbg - 256 * 1024 + c * 1024 + MB_X2 * 8, first ? 8 : 10, tag);
      bar();
      v4u xh[3];
#pragma unroll
      for (int i = 0; i < 3; ++i) xh[i] = v4u{0u, 0u, 0u, 0u};
      if (ra) {
        v4u xm[6];
#pragma unroll
        for (int i = 0; i < 6; ++i) xm[i] = ldx(rs, v16, sm + i * 4096);
        if (!first) { xh[0] = ldx(rs, v16, sh); if (tt < 2) xh[1] = ldx(rs, v16, sh + 16384); }
        SB();
#pragma unroll
        for (int i = 0; i < 6; ++i) sts128(la + i * 5120, xm[i]);
        sts128(lh, xh[0]);
        __builtin_amdgcn_s_waitcnt(0x0F70);
        if (tt < 2) sts128(lh + 20480, xh[1]);
        lds_arrive(lb + L_CNT, lane);
      }
      int wb = w;
      asm volatile("" : "+s"(wb));
      if (wb >= 4) {
        __builtin_amdgcn_s_sleep(3);
        v4u xm[12];
#pragma unroll
        for (int i = 0; i < 12; ++i) xm[i] = ldx(rs, v16, sm + (6 + i) * 4096);
        if (!first) {
#pragma unroll
          for (int i = 0; i < 3; ++i) xh[i] = ldx(rs, v16, sh + (6 + 4 * i) * 4096);
        }
        SB();
#pragma unroll
        for (int i = 0; i < 12; ++i) sts128(la + i * 5120, xm[i]);
#pragma unroll
        for (int i = 0; i < 3; ++i) sts128(lh + i * 20480, xh[i]);
        lds_arrive(lb + L_CNT + 40, lane);
      }
      }
      lds_wait(lb + L_CNT, 8 * layer + 4);
      groupq(a0, a1, ring, x0, x1, x4, rs, v16, sq, so, lb + L_CNT + 40, 8 * layer + 4, [&]() INL {
        asm volatile("" : "=v"(rs_[0]), "=v"(rs_[1]), "=v"(rs_[2]), "=v"(rs_[3]), "=v"(rs_[4]), "=v"(c0), "=v"(c1));
        if (!ra) rowk();
      });
      if (!ra) lds_arrive(lb + L_CNT + 120, lane);
#define W2LOAD() { \
        const int kt = smul(G, 57) >> 9, cb = G - smul(kt, 9), p0 = smul(kt, 6) + w - 1; \
        const rsrc_t r2 = __builtin_amdgcn_make_buffer_rsrc((void*)w2, 0, 4608 * 1152, 0x00020000); \
        _Pragma("unroll") for (int j = 0; j < 4; ++j) mt[j] = ldp(r2, (lane >> 3) * 1152 + (lane & 7) * 16, smul(smul(c, 576) + 32 * p0, 1152) + cb * 128 + j * (8 * 1152)); }
#pragma unroll
      for (int j = 0; j < 4; ++j) undef(mt[j]);
      if (layer == 0 && G < 27 && w >= 1 && w < 4) W2LOAD();
      SB();
      if (ra) {
        f32x4 s = {0.f, 0.f, 0.f, 0.f}, q = s, sh_ = s, qh_ = s;
        u32 xa = lb + tt * 1024 + v16s, xb = xa + 46080, xc = lb + tt * 5120 + 4096 + v16s;
        asm volatile("" : "+v"(xa), "+v"(xb), "+v"(xc));
        v4u xh[5];
#pragma unroll
        for (int i = 0; i < 4; ++i) xh[i] = lds128(xc + i * 20480);
        xh[4] = v4u{0u, 0u, 0u, 0u};
        if (tt < 2) xh[4] = lds128(xc + 81920);
        const bool dg = g == (tl >> 2);
        v4u xm[9], ym[9];
#pragma unroll
        for (int i = 0; i < 9; ++i) xm[i] = lds128(xa + i * 5120);
#pragma unroll
        for (int i = 0; i < 5; ++i) stat_acc(xh[i], sh_, qh_);
#pragma unroll
        for (int i = 0; i < 9; ++i) stat_acc(xm[i], s, q);
#pragma unroll
        for (int i = 0; i < 9; ++i) ym[i] = lds128(xb + i * 5120);
        if (dg) *(LAS f32x2*)(size_t)(sa + (5 + tt) * 128) = f32x2{sh_[0], diag(qh_, tl)};
        const bool hl = lds_ticket(lb + L_CNT + 80, lane) == 4 * layer + 3;
        f32x2 hp[4];
#pragma unroll
        for (int j = 0; j < 4; ++j) hp[j] = ldsf2(sa + (5 + j) * 128);
#pragma unroll
        for (int i = 0; i < 9; ++i) stat_acc(ym[i], s, q);
        if (hl) {
          const f32x2 p = (hp[0] + hp[1]) + (hp[2] + hp[3]);
          const float mh = p[0] * (1.f / 576.f), rh = __builtin_amdgcn_rsqf(__builtin_fmaxf(__builtin_fmaf(p[1], 1.f / 576.f, -mh * mh), 0.f) + 1e-5f);
          *(LAS f32x2*)(size_t)(sa + 4 * 128) = f32x2{rh, -mh * rh};
        }
        const float m = s[0] * (1.f / 576.f), r = __builtin_amdgcn_rsqf(__builtin_fmaxf(__builtin_fmaf(diag(q, tl), 1.f / 576.f, -m * m), 0.f) + 1e-5f);
        if (dg) *(LAS f32x2*)(size_t)(sa + tt * 128) = f32x2{r, -m * r};
        lds_arrive(lb + L_CNT + 84, lane);
      }
      if (ra) rowk();
#pragma unroll
      for (int t = 0; t < 5; ++t) {
        const int slot = t == 0 ? (swp ? 0 : 1) : t == 4 ? (swp ? 1 : 0) : t + 1;
        float y[4];
#pragma unroll
        for (int r = 0; r < 4; ++r) y[r] = __builtin_fmaf(rs_[t][0], a0[t][r], rs_[t][1] * c0[r]);
        sts64(e0 + slot * 512, v2u{pk(y[0], y[1]), pk(y[2], y[3])});
        if (t < 4) {
#pragma unroll
          for (int r = 0; r < 4; ++r) y[r] = __builtin_fmaf(rs_[t][0], a1[t][r], rs_[t][1] * c1[r]);
          sts64(e1 + slot * 512, v2u{pk(y[0], y[1]), pk(y[2], y[3])});
        }
      }
      asm volatile("" : "=v"(ex[0]), "=v"(ex[1]), "=v"(ex[2]), "=v"(ex[3]), "=v"(ex[4]), "=v"(ex[5]), "=v"(ex[6]), "=v"(ex[7]), "=v"(ex[8]));
      if (ra) {
        lds_waits(lb + L_CNT + 120, 4 * layer + 4);
#pragma unroll
        for (int i = 0; i < 9; ++i) ex[i] = ldw(rs, v16 + (i & 3) * 1024, woc + smul(16 + 2 * (w & 1) + (w >> 1), 9216) + (i >> 2) * 4096);
      }
      if (layer == 0 && !ra) drain();
      const u32 ev = et + p * 5120;
      bar();
      const v2u qr = lds64(er + 512), kp = lds64(er + 128), kc = lds64(er + 512 + 128);
      v2u k0[2], k1[2], qm[2], v0[2], v1[2];
#pragma unroll
      for (int e = 0; e < 2; ++e) { k0[e] = lds64(el + e * 2560); k1[e] = lds64(el + e * 2560 + 512); qm[e] = lds64(el + (4 + e) * 2560 + 512); }
#pragma unroll
      for (int e = 0; e < 2; ++e) { v0[e] = ldstr(ev + (8 + e) * 2560); v1[e] = ldstr(ev + (8 + e) * 2560 + 512); }
      const v2u r0 = ldstr(et + 13 * 2560), r1 = ldstr(et + 13 * 2560 + 512);
      SB();
      if (layer == 0) flag(rs, lane * 8192, MB_OFF + (2 * 256 + c) * 1024 + (G + MB_W1) * 8, w == 0 && lane < 32, tag);
      if (layer == 0 && G < 27 && w >= 4 && w < 7) W2LOAD();
      SB();
      float sv[2][4], mx[2];
      {
        f32x4 s0[2], s1[2];
#pragma unroll
        for (int e = 0; e < 2; ++e) { s0[e] = mfma16(k0[e], qm[e], m0); s1[e] = mfma16(k1[e], qm[e], m1); }
        const v2u q0 = {g == 0 ? qr[0] : 0u, 0u}, q1 = {0u, g == 0 ? qr[1] : 0u};
        s0[0] = mfma16(kp, q0, s0[0]); s1[0] = mfma16(kc, q0, s1[0]); s0[1] = mfma16(kp, q1, s0[1]); s1[1] = mfma16(kc, q1, s1[1]);
        asm volatile("s_nop 7" : "+v"(s0[0]), "+v"(s1[0]), "+v"(s0[1]), "+v"(s1[1]));
#pragma unroll
        for (int e = 0; e < 2; ++e) {
#pragma unroll
          for (int r = 0; r < 4; ++r) sv[e][r] = max2(s0[e][r], s1[e][r]);
          mx[e] = max2(max3(sv[e][0], sv[e][1], sv[e][2]), sv[e][3]);
        }
        asm volatile("s_nop 1\n\tv_permlane16_swap_b32 %0, %1\n\tv_max_f32 %0, %0, %1\n\tv_mov_b32 %1, %0\n\ts_nop 1\n\tv_permlane32_swap_b32 %0, %1\n\t"
                     "v_max_f32 %0, %0, %1\n\tv_mov_b32 %1, %0\n\ts_nop 1\n\tv_permlane16_swap_b32 %0, %1" : "+v"(mx[0]), "+v"(mx[1]));
      }
      const v4u vr = {r0[0], r0[1], r1[0], r1[1]}, ones = {0x3f803f80u, 0x3f803f80u, 0x3f803f80u, 0x3f803f80u};
      f32x4 o[2], oq[2], sm[2];
#pragma unroll
      for (int e = 0; e < 2; ++e) {
        constexpr float sc = 0.23570226039551584f * 1.4426950408889634f;
        const float nb = -mx[e] * sc;
        float pe[4];
#pragma unroll
        for (int r = 0; r < 4; ++r) pe[r] = __builtin_amdgcn_exp2f(__builtin_fmaf(sv[e][r], sc, nb));
        const u32 pa = pk(pe[0], pe[1]), pb = pk(pe[2], pe[3]);
        const v4u pf = {pa & km[0], pb & km[1], pa & ~km[0], pb & ~km[1]};
        const f32x4 z = {0.f, 0.f, 0.f, 0.f};
        sm[e] = mfma(ones, pf, z);
        o[e] = mfma(v4u{v0[e][0], v0[e][1], v1[e][0], v1[e][1]}, pf, z);
        oq[e] = mfma(vr, pf, z);
      }
      v4u om; v2u orr;
#pragma unroll
      for (int e = 0; e < 2; ++e) {
        const float inv = __builtin_amdgcn_rcpf(sm[e][0]);
        om[2 * e] = pk(o[e][0] * inv, o[e][1] * inv); om[2 * e + 1] = pk(o[e][2] * inv, o[e][3] * inv);
        orr[e] = pk(oq[e][2 * e] * inv, oq[e][2 * e + 1] * inv);
      }
      stx(om, rs, v16, om_);
      stx2(orr, rs, g == p ? tl * 8 : OOB, or_);
      int fv = lane * 1024;
      asm volatile("" : "+v"(fv));
      drain();
      if (lds_ticket(lb + L_CNT + 88, lane) == 8 * layer + 7) {
        flag(rs, fv, fs, lane < 8, tag);
      }
      if (layer == 0 && w >= 1 && w < 7) {
        lds_wait(lb + L_CNT + 88, 8);
        v4u f[4]; tile_tr(mt, f, lb + L_E + w * 4608, lane); mt[0] = f[0]; mt[1] = f[1]; mt[2] = f[2]; mt[3] = f[3];
      }
    }
    {
      FRESH();
      const int th = c & 1, sw = (c >> 1) & 1, u1 = ub + (w & 3);
      int ob = OB_OFF + gofs + th * 2048 + (w >> 1) * 4096 + (w & 1) * 1024, xb = X1_OFF + gofs + u1 * 4096 + th * 2048, fs = mbg + (MB_X1 + c) * 8, fv = lane * 1024;
      u32 lo = lb + L_OI + w * 1024 + v16, l4 = lb + L_OI + (32 + w) * 1024 + (g * 16 + 2 * (lane & 7)) * 16 + ((lane >> 3) & 1) * 8;
      u32 x0 = lb + L_OI + v16 + (w >> 2) * (9 * 2048), xe = lb + L_OI + v16 + sw * 1024 + (w >> 1) * (9 * 2048), pa = lb + L_PX + (w & 3) * 4096 + v16;
      f32x4 a[4];
#pragma unroll
      for (int j = 0; j < 4; ++j) a[j] = f32x4{0.f, 0.f, 0.f, 0.f};
      if (ra) {
        const v4u x0r = lds128(lb + smul(u1, 5120) + th * 2048 + v16s), x1r = lds128(lb + smul(u1, 5120) + th * 2048 + 1024 + v16s);
        a[0] = unlo(x0r); a[1] = unlo(x1r); a[2] = unhi(x0r); a[3] = unhi(x1r);
      }
      asm volatile("" : "+s"(ob)); asm volatile("" : "+s"(xb)); asm volatile("" : "+s"(fs));
      asm volatile("" : "+v"(lo), "+v"(l4), "+v"(x0), "+v"(xe), "+v"(pa), "+v"(fv), "+v"(a[0]), "+v"(a[1]), "+v"(a[2]), "+v"(a[3]));
      if (layer == 0 && w == 7) poll<2>(rs, lane, MB_OFF + (2 * 256 + G * 8 + c) * 1024 + MB_W1 * 8, 32, tag);
      if (w == 0) { lds_wait(lb + L_CNT + 88, 8 * layer + 8); poll<0>(rs, lane, mbg + c * 1024 + MB_O * 8, 8, tag); }
      bar();
      v4u of[5];
#pragma unroll
      for (int i = 0; i < 4; ++i) of[i] = ldx(rs, v16, ob + i * 16384);
      undef(of[4]);
      if (ra) of[4] = ldx(rs, v16, ob + 65536);
      SB();
#pragma unroll
      for (int i = 0; i < 4; ++i) sts128(lo + i * 8192, of[i]);
      if (ra) { sts64(l4, v2u{of[4][0], of[4][1]}); sts64(l4 + 16, v2u{of[4][2], of[4][3]}); }
      bar();
      if (layer == 0 && G < 27 && w >= 1 && w < 7) {
        const int kt = smul(G, 57) >> 9, cb = G - smul(kt, 9), p0 = smul(kt, 6) + w - 1;
#pragma unroll
        for (int q = 0; q < 4; ++q) { int so, st; mlp_dest<1>(c, cb, q, so, st); stx(mt[q], rs, v16, so + smul(st, p0)); }
      }
      asm volatile("" : "=v"(nring[0]), "=v"(nring[1]), "=v"(nring[2]), "=v"(nring[3]), "=v"(nring[4]), "=v"(nring[5]), "=v"(nring[6]), "=v"(nring[7]), "=v"(nring[8]));
      if (!ra) fill<0, 9>(nring, rs, v16, wm);
      f32x4 pp[4];
      v4u ef[9];
      int wb = w;
      asm volatile("" : "+s"(wb));
      groupo(a, ring, x0, [&]() INL {
#pragma unroll
        for (int j = 0; j < 4; ++j) undef(pp[j]);
#pragma unroll
        for (int k = 0; k < 9; ++k) undef(ef[k]);
        if (ra) {
#pragma unroll
          for (int k = 0; k < 9; ++k) ef[k] = lds128(xe + k * 2048);
        }
        if (wb >= 4) {
          lds_wait(lb + L_CNT + 96 + 4 * (w & 3), layer + 1);
#pragma unroll
          for (int j = 0; j < 4; ++j) pp[j] = ldsf4(pa + j * 1024);
        }
      });
      if (wb >= 4) {
        stx(pk8(a[0] + pp[0], a[2] + pp[2]), rs, v16, xb);
        stx(pk8(a[1] + pp[1], a[3] + pp[3]), rs, v16, xb + 1024);
      }
      bool stored = !ra;
      if (ra) {
#pragma unroll
        for (int j = 0; j < 4; ++j) *(LAS f32x4*)(size_t)(pa + j * 1024) = a[j];
        f32x4 b;
#pragma unroll
        for (int k = 0; k < 9; ++k) { if (k == 0) mm0(b, ex[0], ef[0]); else mm(b, ex[k], ef[k]); }
        lds_arrive(lb + L_CNT + 96 + 4 * w, lane);
        mm_end();
        *(LAS f32x4*)(size_t)(lb + smul(w, 5120) + 4096 + v16) = b;
        if (lds_ticket(lb + L_CNT + 92, lane) == 4 * layer + 3) {
          const v4u xv = lds128(lb + smul(us, 5120) + (2 * th + sw) * 1024 + v16s);
          const u32 pb = lb + 4096 + v16;
          stx(pk8(ldsf4(pb) + ldsf4(pb + 10240) + unlo(xv), ldsf4(pb + 5120) + ldsf4(pb + 15360) + unhi(xv)), rs, v16, X1_OFF + gofs + us * 4096 + (2 * th + sw) * 1024);
          stored = true;
        }
        fill<0, 9>(nring, rs, v16, wm);
      }
      if (stored) {
        drain();
        if (lds_ticket(lb + L_CNT + 112, lane) == 5 * layer + 4) flag(rs, fv, fs, lane < 8, tag);
      }
      lds_wait(lb + L_CNT + 112, 5 * layer + 5);
      if (layer == 0) {
        drain();
      }
      if (layer == 0 && w >= 1 && w < 1 + 3 && lane < 48) {
        const int n = ((w - 1) * 48 + lane) * 4;
        v4u p[9];
#pragma unroll
        for (int k = 0; k < 9; ++k) p[k] = ldx(rs, n * 4, CS1_OFF + smul(c, 20736) + k * 2304);
        const v2u bb = *(const GAS v2u*)(b1 + 576 * c + colp(n >> 5, (n >> 4) & 1, n & 15));
        f32x4 cs = __builtin_bit_cast(f32x4, p[0]);
#pragma unroll
        for (int k = 1; k < 9; ++k) cs += __builtin_bit_cast(f32x4, p[k]);
#pragma unroll
        for (int r = 0; r < 4; ++r) {
          u32 h, m, l;
          split3(cs[r], h, m, l);
          sts64(lb + L_TA + 8 * (n + r), v2u{h | (m << 16), ((r & 1) ? bb[r >> 1] >> 16 : bb[r >> 1] & 0xffffu) | (l << 16)});
        }
      }
      fill<9, 18>(nring, rs, v16, wm);
    }
#pragma unroll
    for (int i = 0; i < 18; ++i) ring[i] = nring[i];
    {
      FRESH();
      const int tt = w & 3;
      int sm = X1_OFF + gofs + tt * 1024;
      u32 la = lb + tt * 1024 + v16, lc = la + CH1 * 4096;
      const int i = tt, ot = c >> 1, hb = c & 1;
      u32 xa = lb + v16, ha = lb + L_H + v16, ta = lb + L_TA + tl * 8, ka = lb + L_SS + tl * 16, ga = lb + L_RK + lane * 8;
#define LOADRK() _Pragma("unroll") for (int t = 0; t < 4; ++t) { rk.aug[t] = lds64(ga + t * 512); rk.k[t] = ldsf4(ka + t * 256); }
      int sn = wm + 18432;
      asm volatile("" : "+s"(sm)); asm volatile("" : "+s"(sn));
      asm volatile("" : "+v"(la), "+v"(lc), "+v"(xa), "+v"(ha), "+v"(ta), "+v"(ka), "+v"(ga));
      if (w == 0) poll<0>(rs, lane, mbg + c * 1024 + MB_X1 * 8, 8, tag);
      bar();
      if (ra) {
        v4u xm[CH1];
#pragma unroll
        for (int i = 0; i < CH1; ++i) xm[i] = ldx(rs, v16, sm + i * 4096);
        SB();
#pragma unroll
        for (int i = 0; i < CH1; ++i) sts128(la + i * 4096, xm[i]);
        lds_arrive(lb + L_CNT, lane);
        lds_wait(lb + L_CNT, 8 * layer + 8);
      }
      int wb = w;
      asm volatile("" : "+s"(wb));
      if (wb >= 4) {
        if (layer == 0) flag(rs, lane * 8192, MB_OFF + (2 * 256 + c) * 1024 + (G + MB_W2) * 8, w == 6 && lane < 32, tag);
        __builtin_amdgcn_s_sleep(3);
        v4u xm[18 - CH1], ym[CH1];
#pragma unroll
        for (int i = 0; i < 18 - CH1; ++i) xm[i] = ldx(rs, v16, sm + (CH1 + i) * 4096);
        SB();
#pragma unroll
        for (int i = 0; i < 18 - CH1; ++i) sts128(lc + i * 4096, xm[i]);
        lds_arrive(lb + L_CNT + 40, lane);
        lds_wait(lb + L_CNT, 8 * layer + 8);
        __builtin_amdgcn_s_setprio(3);
#pragma unroll
        for (int i = 0; i < CH1; ++i) ym[i] = lds128(la + i * 4096);
        f32x4 s = {0.f, 0.f, 0.f, 0.f}, q = s;
#pragma unroll
        for (int i = 0; i < 18 - CH1; ++i) stat_acc(xm[i], s, q);
#pragma unroll
        for (int i = 0; i < CH1; ++i) stat_acc(ym[i], s, q);
        const float m = s[0] * (1.f / 576.f), var = __builtin_fmaxf(__builtin_fmaf(diag(q, tl), 1.f / 576.f, -m * m), 0.f);
        const float r1 = __builtin_amdgcn_rsqf(var + 1e-5f), r = r1 * __builtin_amdgcn_rsqf(var * r1 * r1 + 1e-5f), ri = __builtin_amdgcn_rcpf(r);
        constexpr float k1 = -2.f * 0.7978845608028654f * 1.4426950408889634f, k2 = k1 * 0.044715f;
        const u32 a0_ = pk(-m, -m), b0_ = pk(ri, -m);
        const float m1_ = -m - lo2f(a0_), i1_ = ri - lo2f(b0_);
        const u32 a1_ = pk(m1_, m1_), b1_ = pk(i1_, m1_);
        const float m2_ = m1_ - lo2f(a1_), i2_ = i1_ - lo2f(b1_);
        if (g == (tl >> 2)) {
          const u32 ra_ = lb + L_RK + tt * 512 + tl * 8;
          sts64(ra_, v2u{a0_, b0_}); sts64(ra_ + 128, v2u{a1_, b1_}); sts64(ra_ + 256, v2u{pk(m2_, m2_), pk(i2_, m2_)}); sts64(ra_ + 384, v2u{0u, 0u});
          *(LAS f32x4*)(size_t)(ka + tt * 256) = f32x4{k1 * r, k2 * r * r * r, ri, 0.f};
        }
        __builtin_amdgcn_s_setprio(0);
        if (layer == 0 && w == 7) poll<0>(rs, lane, MB_OFF + (2 * 256 + G * 8 + c) * 1024 + MB_W2 * 8, 32, tag);
        lds_arrive(lb + L_CNT + 4, lane);
      }
#pragma unroll 1
      for (int gi = 0; gi < (ra ? 2 : 1); ++gi) {
        const int u = ra ? 8 * gi + i : 4 + i;
        f32x4 a[2][4];
        RowK rk;
        v2u t0, t1;
        group<2, CH1, 1>(a, ring, xa, rs, v16, sn, sn + 18432, [&]() INL { if (gi == 0) lds_wait(lb + L_CNT + 40, 8 * layer + 8); }, [&]() INL {
          if (gi == 0) lds_wait(lb + L_CNT + 4, 4 * layer + 4);
          t0 = lds64(ta + u * 256); t1 = lds64(ta + u * 256 + 128);
          LOADRK();
        });
        sn += 2 * 18432;
        __builtin_amdgcn_s_setprio(3);
        aug_tile(a[0], t0, rk); aug_tile(a[1], t1, rk);
        __builtin_amdgcn_s_setprio(0);
        mm_end();
        gelu_pair(a[0], a[1], ha + u * 4096, rk);
        lds_arrive(lb + L_CNT + 60, lane);
      }
      const int pb = PB_OFF + gofs * 8 + smul((i >> 1) * 8 + c, 9216);
      const int town = (i >> 1) == hb ? ot : -1;
      const u32 lo = xa + (ot ^ 1) * 1024;
      if (ra) {
        f32x4 a[5][4];
        lds_wait(lb + L_CNT + 60, 12 * layer + 12);
        group<5, 12, 0>(a, ring, ha, rs, v16, sn, 0, []() INL { bar(); }, []() INL {});
        mm_end();
        __builtin_amdgcn_s_setprio(3);
        const u32 ls = xa + (ot ^ 2) * 1024 + (i >> 1) * 16384;
#pragma unroll
        for (int t = 0; t < 4; ++t) sts64((t == town ? lo + (16 + hb) * 4096 : ls + t * 4096) + (i & 1) * 8, v2u{pk(a[4][t][0], a[4][t][1]), pk(a[4][t][2], a[4][t][3])});
        lds_arrive(lb + L_CNT + 48 + (i >> 1) * 4, lane);
        part_pair(a[0], a[1], rs, v16, pb + ((4 * i) & 7) * 1024, town, lo + (4 * i) * 4096);
        part_pair(a[2], a[3], rs, v16, pb + ((4 * i + 1) & 7) * 1024, town, lo + (4 * i + 1) * 4096);
        lds_wait(lb + L_CNT + 48 + (i >> 1) * 4, 2 * layer + 2);
#pragma unroll
        for (int k = 0; k < 2; ++k) {
          const int t = 2 * (i & 1) + k;
          if (t != town) stx(lds128(ls + t * 4096), rs, v16, pb + 8192 + smul(t, 2 * 8 * 9216));
        }
      }
      if (wb >= 4) {
        v4u aux[18];
        {
          f32x4 a[3][4];
          RowK rk;
          v2u t0, t1, t2;
          __builtin_amdgcn_s_setprio(2);
          group<3, 99, 1>(a, ring, xa, rs, v16, sn, sn + 2 * 18432, []() INL {}, [&]() INL {
            t0 = lds64(ta + (12 + i) * 256); t1 = lds64(ta + (12 + i) * 256 + 128); t2 = lds64(ta + (16 + (i >> 1)) * 256 + (i & 1) * 128);
            LOADRK();
          });
          sn += 3 * 18432;
          aug_tile(a[0], t0, rk); aug_tile(a[1], t1, rk); aug_tile(a[2], t2, rk);
          mm_end();
          gelu_pair(a[0], a[1], ha + (12 + i) * 4096, rk);
#pragma unroll
          for (int t = 0; t < 4; ++t) sts64(ha + (16 + (i >> 1)) * 4096 + t * 1024 + (i & 1) * 8, gelu4(a[2][t], rk.k[t]));
          __builtin_amdgcn_s_setprio(0);
          bar();
        }
        fill<0, 18>(aux, rs, v16, sn);
        f32x4 a[2][4];
        group36<2, 99>(a, ring, aux, ha, rs, v16, sn + 18432, []() INL {}, []() INL {});
        mm_end();
#pragma unroll
        for (int t = 0; t < 4; ++t) sts128(t == town ? lo + (4 * i + 2) * 4096 : xa + (ot ^ 3) * 1024 + (4 * i + t) * 4096, pk8(a[0][t], a[1][t]));
        lds_arrive(lb + L_CNT + 64 + 4 * i, lane);
#pragma unroll
        for (int hf = 0; hf < 2; ++hf) {
          const int t0 = (ot & 2) ^ (2 - 2 * hf);
          f32x4 d[2][2];
          group36h(d, ring, aux, ha + t0 * 1024);
#pragma unroll
          for (int t = 0; t < 2; ++t) {
            if (t0 + t == town) sts128(lo + (4 * i + 3) * 4096, pk8(d[0][t], d[1][t]));
            else stx(pk8(d[0][t], d[1][t]), rs, v16, pb + ((4 * i + 3) & 7) * 1024 + smul(t0 + t, 2 * 8 * 9216));
          }
        }
      }
    }
    {
      FRESH();
      const int tt = c >> 1, hb = c & 1;
      int po[7];
#pragma unroll
      for (int j = 0; j < 7; ++j) { po[j] = PB_OFF + gofs * 8 + smul(c * 8 + ((c + 1 + j) & 7), 9216); asm volatile("" : "+s"(po[j])); }
      int vo = layer == 0 ? v16 : (rowbase + 2 * (16 * tt + tl)) * 1152 + 16 * g;
      v4u sel[2];
      make_sel(sel, g, tl);
      int slot = (0x62854100u >> (4 * w)) & 15, vs = v16 + slot * 1024, fv = lane * 2048;
      int fs = mbg + ((w >> 1) & 1) * 1024 + ((ra ? MB_A : MB_B) + 2 * c + (w & 1)) * 8;
      asm volatile("" : "+s"(fs)); asm volatile("" : "+s"(slot));
      asm volatile("" : "+v"(vo), "+v"(vs), "+v"(fv), "+v"(sel[0]), "+v"(sel[1]));
      drain();
      flag(rs, fv, fs, lane < 4, tag);
      __builtin_amdgcn_s_setprio(0);
      if (!ra) lds_arrive(lb + L_CNT + 44, lane);
      if (ra) {
        const int pb = PB_OFF + gofs * 8 + smul((w >> 1) * 8 + c, 9216) + ((4 * w + 2) & 7) * 1024, town = (w >> 1) == hb ? tt : -1;
        lds_waits(lb + L_CNT + 64 + 4 * w, layer + 1);
#pragma unroll
        for (int t = 0; t < 4; ++t)
          if (t != town) stx(lds128(lb + v16 + (tt ^ 3) * 1024 + (4 * w + t) * 4096), rs, v16, pb + t * (2 * 8 * 9216));
        if (w == 0) { poll<2>(rs, lane, mbg + c * 1024 + MB_A * 8, 16, tag); lds_arrive(lb + L_CNT + 24, lane); }
        drain();
        flag(rs, lane * 2048, mbg + (w >> 1) * 1024 + (MB_B + 16 + 2 * c + (w & 1)) * 8, lane < 4, tag);
        if (w == 0) {
          __builtin_amdgcn_s_sleep(8);
          poll<4>(rs, lane, mbg + c * 1024 + (MB_B + 16) * 8, 16, tag);
          lds_arrive(lb + L_CNT + 24, lane);
          lds_waits(lb + L_CNT + 44, 4 * layer + 4);
          __builtin_amdgcn_s_sleep(6);
          poll<1>(rs, lane, mbg + c * 1024 + MB_B * 8, 16, tag);
          lds_arrive(lb + L_CNT + 24, lane);
        }
      }
#pragma unroll 1
      for (int rep = ra ? 0 : 1; rep < 2; ++rep) {
        if (ra && rep == 1) { slot = 3 + 4 * w; vs = v16 + slot * 1024; }
        const int u = slot == 8 ? 16 + hb : 8 * hb + slot;
        const bool on = !ra || (rep == 0 ? w > 0 : w < 2);
        if (on) {
          v4u pv[7];
          if (!ra) lds_wait(lb + L_CNT + 44, 4 * layer + 4);
          lds_waits(lb + L_CNT + 24, 3 * layer + (ra ? 1 + 2 * rep : w < 6 ? 1 : 2));
#pragma unroll
          for (int j = 0; j < 7; ++j) pv[j] = ldx(rs, vs, po[j]);
          SB();
          const v4u xv = lds128(lb + u * 4096 + tt * 1024 + v16), ov = lds128(lb + u * 4096 + (tt ^ 1) * 1024 + v16);
          f32x4 ya = mfma(sel[0], ov, mfma(sel[0], xv, ldsf4(lb + L_TB2 + u * 128 + g * 32)));
          f32x4 yb = mfma(sel[1], ov, mfma(sel[1], xv, ldsf4(lb + L_TB2 + u * 128 + g * 32 + 16)));
#pragma unroll
          for (int j = 0; j < 7; ++j) { ya = mfma(sel[0], pv[j], ya); yb = mfma(sel[1], pv[j], yb); }
          if (layer == 0) stx(pk8(ya, yb), rs, vo, X2_OFF + gofs + u * 4096 + tt * 1024);
          else stx(pk8(ya, yb), ro, vo, 64 * u);
        }
      }
      if (layer == 0) {
        drain();
        const bool nxt = c >= 6 && (G & 15) != 15;
        if (lds_ticket(lb + L_CNT + 56, lane) == 7) flag(rs, lane * 1024 + (lane & 8) * 2, mbg + (MB_X2 + c) * 8, lane < 8 || (nxt && lane < 16), tag);
      }
    }
    undef(m0); undef(m1); undef(km);
    if (layer == 0) {
      FRESH();
      static_assert(CH1 == 6, "halo split");
      lds_waits(lb + L_CNT + 56, 8);
      fill<0, 18>(ring, rs, v16, wq);
      MASKS();
    }
  }
  if (bid == 0 && tid == 448) stx2(tag, rs, 0, EP_OFF);
}

extern "C" void kernel_launch(void* const* d_in, const int* in_sizes, int n_in, void* d_out, int out_size, void* d_ws, size_t ws_size,
                              hipStream_t stream) {
  const u16* x = (const u16*)d_in[0];
  const u16* wqkv = (const u16*)d_in[1];
  const u16* wo = (const u16*)d_in[2];
  const u16* w1 = (const u16*)d_in[4];
  const u16* b1 = (const u16*)d_in[5];
  const u16* w2 = (const u16*)d_in[6];
  const u16* b2 = (const u16*)d_in[7];
  (void)hipFuncSetAttribute((const void*)k_main, hipFuncAttributeMaxDynamicSharedMemorySize, L_TOTAL);
  k_main<<<256, 512, L_TOTAL, stream>>>(x, wqkv, wo, w1, b1, w2, b2, (u16*)d_out, (char*)d_ws);
}
```
